```python
import torch

def _softmax(x, axis=-1):
    return torch.softmax(x, dim=axis)
def _gelu(x):
    return torch.nn.functional.gelu(x, approximate="tanh")
def _silu(x):
    return torch.nn.functional.silu(x)
def _sigmoid(x):
    return torch.sigmoid(x)
def _layernorm(x, axis=-1, eps=1e-5):
    m = x.mean(dim=axis, keepdim=True)
    v = x.var(dim=axis, keepdim=True, unbiased=False)
    return (x - m) / torch.sqrt(v + eps)
def _scatter_add(tgt, idx, src, axis=0):
    return tgt.clone().index_add_(axis, idx, src)
def _segment_sum(src, seg, n):
    out = torch.zeros((n,) + src.shape[1:], dtype=src.dtype)
    return out.index_add_(0, seg, src)
def _pad(x, pw):
    flat = tuple(v for pair in reversed(pw) for v in pair)
    return torch.nn.functional.pad(x, flat)

kernel_name = 'parallel_sum__linear_kernel_hedgehog__dense_activation_gelu__prompt'

def setup_inputs():

    _w = (lambda a: (lambda t: t.to(torch.bfloat16) if t.is_floating_point() else t)(torch.as_tensor(a)))
    def _A_setup():
        B, S, D, H = 1, 2048, 1024, 32
        Dh = D // H
        return {
            "x": _w((lambda *s: torch.randn(*s, dtype=torch.bfloat16) * 0.05)(B, S, D)),
            "wqkv": _w((lambda *s: torch.randn(*s, dtype=torch.bfloat16) * 0.2)(D, 3 * D)),
            "w_phi": _w((lambda *s: torch.randn(*s, dtype=torch.bfloat16) * 0.2)(H, Dh, Dh) * (Dh ** -0.5)),
            "wo": _w((lambda *s: torch.randn(*s, dtype=torch.bfloat16) * 0.05)(D, D)),
        }
    _A_in = _A_setup()

    def _B_setup():
        B, S, D, F = 1, 2048, 1024, 3584
        return {
            "x": _w((lambda *s: torch.randn(*s, dtype=torch.bfloat16) * 0.05)(B, S, D)),
            "w1": _w((lambda *s: torch.randn(*s, dtype=torch.bfloat16) * 0.8)(D, F) * (D ** -0.5)),
            "b1": _w((lambda *s: torch.randn(*s, dtype=torch.bfloat16) * 0.8)(F) * 0.01),
            "w2": _w((lambda *s: torch.randn(*s, dtype=torch.bfloat16) * 0.2)(F, D) * (F ** -0.5)),
            "b2": _w((lambda *s: torch.randn(*s, dtype=torch.bfloat16) * 0.2)(D) * 0.01),
        }
    _B_in = _B_setup()

    return {
        "A_x": _A_in["x"],
        "A_wqkv": _A_in["wqkv"],
        "A_w_phi": _A_in["w_phi"],
        "A_wo": _A_in["wo"],
        "B_x": _B_in["x"],
        "B_w1": _B_in["w1"],
        "B_b1": _B_in["b1"],
        "B_w2": _B_in["w2"],
        "B_b2": _B_in["b2"],
    }

def reference(A_x, A_wqkv, A_w_phi, A_wo, B_x, B_w1, B_b1, B_w2, B_b2):
    def _A_ref(x, wqkv, w_phi, wo):
        B, S, D = x.shape
        H = 32; Dh = D // H
        qkv = (lambda x, *d: x.permute(*d))((x @ wqkv).reshape(B, S, 3, H, Dh), 2, 0, 3, 1, 4)
        q, k, v = qkv[0], qkv[1], qkv[2]
        pq = torch.einsum("bhsd,hdf->bhsf", q, w_phi)
        pk = torch.einsum("bhsd,hdf->bhsf", k, w_phi)
        phi_q = torch.concatenate([_softmax(pq, axis=-1), _softmax(-pq, axis=-1)], axis=-1)
        phi_k = torch.concatenate([_softmax(pk, axis=-1), _softmax(-pk, axis=-1)], axis=-1)
        kv = torch.einsum("bhsf,bhsd->bhfd", phi_k, v)
        z  = torch.einsum("bhsf->bhf", phi_k)
        num = torch.einsum("bhqf,bhfd->bhqd", phi_q, kv)
        den = torch.einsum("bhqf,bhf->bhq", phi_q, z)[..., None] + 1e-6
        o = num / den
        return (lambda x, *d: x.permute(*d))(o, 0, 2, 1, 3).reshape(B, S, D) @ wo

    def _B_ref(x, w1, b1, w2, b2):
        h = torch.einsum("bsd,df->bsf", x, w1) + b1
        h = _gelu(h)
        return torch.einsum("bsf,fd->bsd", h, w2) + b2

    x = A_x
    xn = _layernorm(x, axis=-1)
    return x + _A_ref(xn, A_wqkv, A_w_phi, A_wo) + _B_ref(xn, B_w1, B_b1, B_w2, B_b2)
```

```cpp
#include <hip/hip_runtime.h>
#include <hip/hip_bf16.h>
#include <stdint.h>

typedef unsigned short u16;
typedef short bf16x8 __attribute__((ext_vector_type(8)));
typedef short s16x4 __attribute__((ext_vector_type(4)));
typedef float f32x4 __attribute__((ext_vector_type(4)));
typedef float f32x2 __attribute__((ext_vector_type(2)));
typedef unsigned u32x4 __attribute__((ext_vector_type(4)));
typedef unsigned u32x2 __attribute__((ext_vector_type(2)));
typedef __bf16 bf16v2 __attribute__((ext_vector_type(2)));
#define LAS __attribute__((address_space(3)))
#define GAS __attribute__((address_space(1)))
#define DI __device__ __forceinline__
#define RLX_AGENT __ATOMIC_RELAXED, __HIP_MEMORY_SCOPE_AGENT

constexpr int S_ = 2048, D_ = 1024, F_ = 3584;
constexpr int SLOT = 33280;
constexpr int BOFF = 16384;
constexpr int PSTR = 1056;
constexpr int RING = 4 * SLOT;
constexpr int G1_WPHI = RING;
constexpr int G1_BIAS = RING + 4096;
constexpr int G1_LDS = RING + 5120;
constexpr int G2_LDS = RING;
constexpr int NKB = 144;
constexpr int N1 = 28;
constexpr int N2 = 72 - N1;

constexpr size_t WS_FLAGS = 0;
constexpr size_t WS_XNF = 1u << 20;
constexpr size_t WS_AF = WS_XNF + (4u << 20);
constexpr size_t WS_PHIQ = WS_AF + (size_t)NKB * 128 * 1024;
constexpr size_t WS_KVP = WS_PHIQ + (8u << 20);
constexpr size_t WS_PART = WS_KVP + (3u << 20);
constexpr int FLAG_STRIDE = 64;
constexpr int FLAG_OCNT = 0;
constexpr int FLAG_PART = 8;
constexpr int FLAG_TMO = 8 + 128;
constexpr int FLAG_WORDS = (8 + 128 + 1) * FLAG_STRIDE;

template <int N> struct IC { static constexpr int value = N; };
#define MFMA16(a, b, c) __builtin_amdgcn_mfma_f32_16x16x32_bf16((a), (b), (c), 0, 0, 0)
template <int N> DI void wait_vm() { asm volatile("s_waitcnt vmcnt(%0)" ::"i"(N) : "memory"); }
DI void wait_lgkm0() { asm volatile("s_waitcnt lgkmcnt(0)" ::: "memory"); }
DI void bar() { __builtin_amdgcn_sched_barrier(0); __builtin_amdgcn_s_barrier(); __builtin_amdgcn_sched_barrier(0); }
DI void dma16(const void* g, unsigned l) {
  asm volatile("s_mov_b32 m0, %1\n\ts_nop 0\n\tglobal_load_lds_dwordx4 %0, off" ::"v"(g), "s"(l) : "memory");
}
DI void dma16s(const void* sb, unsigned voff, unsigned l) {
  asm volatile("s_mov_b32 m0, %2\n\ts_nop 0\n\tglobal_load_lds_dwordx4 %0, %1" ::"v"(voff), "s"(sb), "s"(l) : "memory");
}
DI void dma16s_x4k(const void* sb, unsigned voff, unsigned l) {
  asm volatile("s_mov_b32 m0, %2\n\ts_nop 0\n\tglobal_load_lds_dwordx4 %0, %1\n\tglobal_load_lds_dwordx4 %0, %1 offset:1024\n\t"
               "global_load_lds_dwordx4 %0, %1 offset:2048\n\tglobal_load_lds_dwordx4 %0, %1 offset:3072" ::"v"(voff), "s"(sb), "s"(l) : "memory");
}
DI s16x4 trread(LAS char* p) { return __builtin_amdgcn_ds_read_tr16_b64_v4i16((LAS s16x4*)p); }
DI bf16x8 cat(s16x4 lo, s16x4 hi) { return __builtin_shufflevector(lo, hi, 0, 1, 2, 3, 4, 5, 6, 7); }
DI unsigned pk2(float lo, float hi) { f32x2 v = {lo, hi}; return __builtin_bit_cast(unsigned, __builtin_convertvector(v, bf16v2)); }
DI bf16x8 pack8(f32x4 a, f32x4 b) { u32x4 r = {pk2(a[0], a[1]), pk2(a[2], a[3]), pk2(b[0], b[1]), pk2(b[2], b[3])}; return __builtin_bit_cast(bf16x8, r); }
DI float bflo(unsigned u) { return __uint_as_float(u << 16); }
DI float bfhi(unsigned u) { return __uint_as_float(u & 0xffff0000u); }
DI float rbf(float x) { return bflo(pk2(x, x)); }
template <int CTRL> DI float dpp(float x) { return __builtin_bit_cast(float, __builtin_amdgcn_mov_dpp(__builtin_bit_cast(int, x), CTRL, 0xf, 0xf, true)); }
constexpr int XOR1 = 0xB1, XOR2 = 0x4E, HMIR = 0x141, MIR = 0x140, ROR8 = 0x128;
DI float row16_max(float x) { x = fmaxf(x, dpp<XOR1>(x)); x = fmaxf(x, dpp<XOR2>(x)); x = fmaxf(x, dpp<HMIR>(x)); return fmaxf(x, dpp<MIR>(x)); }
DI float row16_min(float x) { x = fminf(x, dpp<XOR1>(x)); x = fminf(x, dpp<XOR2>(x)); x = fminf(x, dpp<HMIR>(x)); return fminf(x, dpp<MIR>(x)); }
DI float row16_sum(float x) { x += dpp<XOR1>(x); x += dpp<XOR2>(x); x += dpp<HMIR>(x); return x + dpp<MIR>(x); }
DI float xg_max(float x) {
  auto s = __builtin_amdgcn_permlane16_swap(__float_as_uint(x), __float_as_uint(x), false, false);
  x = fmaxf(__uint_as_float(s[0]), __uint_as_float(s[1]));
  auto t = __builtin_amdgcn_permlane32_swap(__float_as_uint(x), __float_as_uint(x), false, false);
  return fmaxf(__uint_as_float(t[0]), __uint_as_float(t[1]));
}
DI float xg_min(float x) {
  auto s = __builtin_amdgcn_permlane16_swap(__float_as_uint(x), __float_as_uint(x), false, false);
  x = fminf(__uint_as_float(s[0]), __uint_as_float(s[1]));
  auto t = __builtin_amdgcn_permlane32_swap(__float_as_uint(x), __float_as_uint(x), false, false);
  return fminf(__uint_as_float(t[0]), __uint_as_float(t[1]));
}
DI float xg_sum(float x) {
  auto s = __builtin_amdgcn_permlane16_swap(__float_as_uint(x), __float_as_uint(x), false, false);
  x = __uint_as_float(s[0]) + __uint_as_float(s[1]);
  auto t = __builtin_amdgcn_permlane32_swap(__float_as_uint(x), __float_as_uint(x), false, false);
  return __uint_as_float(t[0]) + __uint_as_float(t[1]);
}
constexpr float LOG2E = 1.4426950408889634f;
DI float gelu1(float x) {
  const float c1 = -2.0f * 0.7978845608028654f * LOG2E, c3 = c1 * 0.044715f;
  float e = __builtin_amdgcn_exp2f(x * fmaf(x * x, c3, c1));
  return x * __builtin_amdgcn_rcpf(1.0f + e);
}
DI f32x4 gelu4(f32x4 v) { f32x4 r = {gelu1(v[0]), gelu1(v[1]), gelu1(v[2]), gelu1(v[3])}; return r; }

__global__ __launch_bounds__(512) void k0_ln(const u16* __restrict__ x, u16* __restrict__ xnf, unsigned* __restrict__ flags) {
  __shared__ float red[2][8][8];
  const int tid = threadIdx.x, w = tid >> 6, l = tid & 63, i = l & 7, c = l >> 3;
  const int row = blockIdx.x * 8 + i;
  const u32x4* src = (const u32x4*)(x + (size_t)row * D_) + 16 * w + c;
  const u32x4 v0 = src[0], v1 = src[8];
  if (blockIdx.x == 0) { for (int j = tid; j < FLAG_WORDS; j += 512) flags[j] = 0u; }
  float f[16];
#pragma unroll
  for (int j = 0; j < 4; ++j) { f[2 * j] = bflo(v0[j]); f[2 * j + 1] = bfhi(v0[j]); f[8 + 2 * j] = bflo(v1[j]); f[9 + 2 * j] = bfhi(v1[j]); }
  float s = 0.f;
#pragma unroll
  for (int j = 0; j < 16; ++j) s += f[j];
  s += dpp<ROR8>(s); s = xg_sum(s);
  if (l < 8) red[0][w][i] = s;
  __syncthreads();
  float mean = 0.f;
#pragma unroll
  for (int j = 0; j < 8; ++j) mean += red[0][j][i];
  mean *= (1.0f / D_);
  float q = 0.f;
#pragma unroll
  for (int j = 0; j < 16; ++j) { float d = f[j] - mean; q = fmaf(d, d, q); }
  q += dpp<ROR8>(q); q = xg_sum(q);
  if (l < 8) red[1][w][i] = q;
  __syncthreads();
  float var = 0.f;
#pragma unroll
  for (int j = 0; j < 8; ++j) var += red[1][j][i];
  var *= (1.0f / D_);
  const float mb = rbf(mean);
  const float sd = rbf(sqrtf(rbf(rbf(var) + 1e-5f)));
  const float rs = 1.0f / sd;
  u32x4 o0, o1;
#pragma unroll
  for (int j = 0; j < 4; ++j) {
    o0[j] = pk2(rbf(f[2 * j] - mb) * rs, rbf(f[2 * j + 1] - mb) * rs);
    o1[j] = pk2(rbf(f[8 + 2 * j] - mb) * rs, rbf(f[9 + 2 * j] - mb) * rs);
  }
  const int mfrag = row >> 4, ri = row & 15;
  const int ch0 = 16 * w + c, ch1 = ch0 + 8;
  u32x4* dst = (u32x4*)xnf;
  dst[((ch0 >> 2) * 128 + mfrag) * 64 + (ch0 & 3) * 16 + ri] = o0;
  dst[((ch1 >> 2) * 128 + mfrag) * 64 + (ch1 & 3) * 16 + ri] = o1;
}

struct G1Args {
  const u16* xnf; const u16* wqkv; const u16* w1; const u16* b1; const u16* wphi;
  u16* af; u16* phiq; float* kvp;
};

template <bool ISX, int GRP>
DI void g1_body(const G1Args& A, LAS char* lds, int rb, int j, int pos4, int wm, int lane) {
  constexpr int NF = GRP == 0 ? 7 : 6;
  const int g = lane >> 4, q = (lane & 15) >> 2, p = lane & 3;
  const int hp = 2 * j + (pos4 >> 1);
  const int cp = pos4 == 0 ? 0 : pos4 == 3 ? 384 : pos4 == 1 ? 96 : 224;
  const int co = pos4 == 0 ? 64 : pos4 == 3 ? 368 : pos4 == 1 ? 80 : 352;
  const int cr = pos4 == 0 ? 0 : pos4 == 3 ? 320 : pos4 == 1 ? 64 : 192;
  constexpr int RBASE = ISX ? 256 : 128;
  const int colT = cp + 32 * (2 * GRP), colU = cp + 32 * (ISX ? GRP : 2 * GRP + 1);

  const unsigned lbase = (unsigned)(size_t)lds;
  const char* dsrc = nullptr; long dstep = 0; int dj = 0;
  const char* xsrc = (const char*)A.xnf + (size_t)(rb * 16 + 4 * wm) * 1024;
  const unsigned xoff = lane * 16;
  if (GRP == 0) {
    const int u0 = 8 * (wm & 1) + 4 * (wm >> 1), c = lane & 31, line = c >> 3, ch = c & 7, row = u0 + 16 * (lane >> 5);
    const bool fromq = ISX ? (line < 2) : (line < 1);
    const int ld = fromq ? 3 * D_ : F_;
    const u16* base = fromq ? (A.wqkv + (ISX ? (1 + line) * D_ : 0) + 64 * hp) : (A.w1 + 448 * j + cr + 64 * (line - (ISX ? 2 : 1)));
    dsrc = (const char*)(base + (size_t)row * ld + ch * 8); dstep = 64L * ld; dj = 2 * ld;
  }
  const unsigned ddst = lbase + (GRP == 0 ? BOFF + 4 * wm * PSTR : 4 * wm * 1024);
  auto issue = [&](int slot) {
    if (GRP == 0) {
#pragma unroll
      for (int jj = 0; jj < 4; ++jj) dma16(dsrc + jj * dj, ddst + slot * SLOT + jj * PSTR);
      dsrc += dstep;
    } else {
      dma16s_x4k(xsrc, xoff, ddst + slot * SLOT);
      xsrc += 128 * 1024;
    }
  };
  if (GRP == 0) dma16s(A.wphi + (size_t)hp * 2048 + wm * 512, xoff, lbase + G1_WPHI + wm * 1024);
  else if (wm == 0) { const int nch = ISX ? 16 : 24; dma16s(A.b1 + 448 * j + cr, (lane < nch ? lane : 0) * 16, lbase + G1_BIAS); }
  issue(0); issue(1); issue(2);
  wait_vm<8>();
  bar();

  f32x4 acc[4][NF];
  {
    LAS char* bp = lds + G1_BIAS + 8 * g;
#pragma unroll
    for (int f = 0; f < NF; ++f) {
      f32x4 b4 = {0.f, 0.f, 0.f, 0.f};
      if (f >= (ISX ? 4 : 2)) {
        const int col = f < 4 ? colT + 16 * (f - 2) : f < 6 ? colU + 16 * (f - 4) : co;
        const u32x2 bb = *(LAS u32x2*)(bp + (col - cr) * 2);
        b4 = (f32x4){bflo(bb[0]), bfhi(bb[0]), bflo(bb[1]), bfhi(bb[1])};
      }
#pragma unroll
      for (int m = 0; m < 4; ++m) acc[m][f] = b4;
    }
  }
  const int lb = BOFF + PSTR * (q + 4 * (g & 1)) + 512 * (g >> 1) + 8 * p;
  LAS char* pA = lds + wm * 4096 + lane * 16;
  LAS char* pS = lds + lb + 64 * GRP;
  LAS char* pT = lds + lb + (ISX ? 128 + 64 * GRP : RBASE + (colT - cr) * 2);
  LAS char* pU = lds + lb + RBASE + (colU - cr) * 2;
  LAS char* pO = lds + lb + RBASE + (co - cr) * 2;
  if (GRP == 1) bar();

  auto step = [&](int s, auto tag) {
    constexpr int T = decltype(tag)::value;
    const int so = (s & 3) * SLOT;
    bf16x8 a[4], b[NF];
#pragma unroll
    for (int f = 0; f < NF; ++f) {
      LAS char* pp = (f < 2 ? pS : f < 4 ? pT : f < 6 ? pU : pO) + so + (f < 6 ? (f & 1) * 32 : 0);
      b[f] = cat(trread(pp), trread(pp + 8 * PSTR));
    }
#pragma unroll
    for (int m = 0; m < 4; ++m) a[m] = *(LAS bf16x8*)(pA + so + m * 1024);
    if (T == 3) issue((s + 3) & 3);
    wait_lgkm0();
    if (GRP == 1 && T > 0) wait_vm<(T == 3 ? 8 : T == 2 ? 4 : 0)>();
    bar();
    __builtin_amdgcn_s_setprio(1);
#pragma unroll
    for (int f = 0; f < NF; ++f)
#pragma unroll
      for (int m = 0; m < 4; ++m) {
        if (ISX && (f == 2 || f == 3)) acc[m][f] = MFMA16(a[m], b[f], acc[m][f]);
        else acc[m][f] = MFMA16(b[f], a[m], acc[m][f]);
      }
    __builtin_amdgcn_s_setprio(0);
    if (GRP == 0 && T > 0) wait_vm<(T == 3 ? 8 : T == 2 ? 4 : 0)>();
    if (GRP == 0 || T > 0) bar();
  };
#pragma unroll 1
  for (int s = 0; s < 29; ++s) step(s, IC<3>{});
  step(29, IC<2>{}); step(30, IC<1>{}); step(31, IC<0>{});

  const int mf0 = rb * 16 + wm * 4;
  {
    u32x4* af4 = (u32x4*)A.af;
    if (!ISX) {
      const int kb = (448 * j + colT) >> 5;
#pragma unroll
      for (int m = 0; m < 4; ++m)
        af4[((size_t)kb * 128 + mf0 + m) * 64 + lane] = __builtin_bit_cast(u32x4, pack8(gelu4(acc[m][2]), gelu4(acc[m][3])));
    }
    {
      const int kb = (448 * j + colU) >> 5;
#pragma unroll
      for (int m = 0; m < 4; ++m)
        af4[((size_t)kb * 128 + mf0 + m) * 64 + lane] = __builtin_bit_cast(u32x4, pack8(gelu4(acc[m][4]), gelu4(acc[m][5])));
    }
    if (GRP == 0) {
      const int kb = (448 * j + co) >> 5, n = (co >> 4) & 1;
#pragma unroll
      for (int m = 0; m < 4; ++m) {
        const f32x4 gv = gelu4(acc[m][NF - 1]);
        u32x2 o = {pk2(gv[0], gv[1]), pk2(gv[2], gv[3])};
        ((u32x2*)A.af)[(((size_t)kb * 128 + mf0 + m) * 64 + lane) * 2 + n] = o;
      }
    }
  }
  const int head = 2 * hp + GRP;
  bf16x8 wp[2];
  {
    LAS char* pw = lds + G1_WPHI + GRP * 2048 + (4 * g + q) * 64 + 8 * p;
    wp[0] = cat(trread(pw), trread(pw + 1024));
    wp[1] = cat(trread(pw + 32), trread(pw + 1024 + 32));
  }
  const f32x4 z4 = {0.f, 0.f, 0.f, 0.f};
  if (!ISX) {
    u32x4* pq4 = (u32x4*)A.phiq;
#pragma unroll
    for (int m = 0; m < 4; ++m) {
      const bf16x8 qb = pack8(acc[m][0], acc[m][1]);
      const f32x4 p0 = MFMA16(wp[0], qb, z4), p1 = MFMA16(wp[1], qb, z4);
      float mx = fmaxf(fmaxf(fmaxf(p0[0], p0[1]), fmaxf(p0[2], p0[3])), fmaxf(fmaxf(p1[0], p1[1]), fmaxf(p1[2], p1[3])));
      float mn = fminf(fminf(fminf(p0[0], p0[1]), fminf(p0[2], p0[3])), fminf(fminf(p1[0], p1[1]), fminf(p1[2], p1[3])));
      mx = xg_max(mx) * LOG2E; mn = xg_min(mn) * LOG2E;
      f32x4 e0, e1, n0, n1;
#pragma unroll
      for (int r = 0; r < 4; ++r) {
        e0[r] = __builtin_amdgcn_exp2f(fmaf(p0[r], LOG2E, -mx)); e1[r] = __builtin_amdgcn_exp2f(fmaf(p1[r], LOG2E, -mx));
        n0[r] = __builtin_amdgcn_exp2f(fmaf(p0[r], -LOG2E, mn)); n1[r] = __builtin_amdgcn_exp2f(fmaf(p1[r], -LOG2E, mn));
      }
      const float sp = xg_sum(((e0[0] + e0[1]) + (e0[2] + e0[3])) + ((e1[0] + e1[1]) + (e1[2] + e1[3])));
      const float sn = xg_sum(((n0[0] + n0[1]) + (n0[2] + n0[3])) + ((n1[0] + n1[1]) + (n1[2] + n1[3])));
      const float rp = __builtin_amdgcn_rcpf(sp), rn = __builtin_amdgcn_rcpf(sn);
      const size_t o = ((size_t)(mf0 + m) * 32 + head) * 128 + lane;
      pq4[o] = __builtin_bit_cast(u32x4, pack8(e0 * rp, e1 * rp));
      pq4[o + 64] = __builtin_bit_cast(u32x4, pack8(n0 * rn, n1 * rn));
    }
  } else {
    f32x4 kv[4][3];
    const bf16x8 ones = {0x3F80, 0x3F80, 0x3F80, 0x3F80, 0x3F80, 0x3F80, 0x3F80, 0x3F80};
#pragma unroll
    for (int u = 0; u < 2; ++u) {
      f32x4 ph[4][2];
#pragma unroll
      for (int mm = 0; mm < 2; ++mm) {
        const int m = 2 * u + mm;
        const bf16x8 ka = pack8(acc[m][0], acc[m][1]);
        const f32x4 p0 = MFMA16(ka, wp[0], z4), p1 = MFMA16(ka, wp[1], z4);
#pragma unroll
        for (int r = 0; r < 4; ++r) {
          const float mx = row16_max(fmaxf(p0[r], p1[r])) * LOG2E, mn = row16_min(fminf(p0[r], p1[r])) * LOG2E;
          const float e0 = __builtin_amdgcn_exp2f(fmaf(p0[r], LOG2E, -mx)), e1 = __builtin_amdgcn_exp2f(fmaf(p1[r], LOG2E, -mx));
          const float n0 = __builtin_amdgcn_exp2f(fmaf(p0[r], -LOG2E, mn)), n1 = __builtin_amdgcn_exp2f(fmaf(p1[r], -LOG2E, mn));
          const float rp = __builtin_amdgcn_rcpf(row16_sum(e0 + e1)), rn = __builtin_amdgcn_rcpf(row16_sum(n0 + n1));
          ph[0][mm][r] = e0 * rp; ph[1][mm][r] = e1 * rp; ph[2][mm][r] = n0 * rn; ph[3][mm][r] = n1 * rn;
        }
      }
      const bf16x8 v0 = pack8(acc[2 * u][2], acc[2 * u + 1][2]), v1 = pack8(acc[2 * u][3], acc[2 * u + 1][3]);
#pragma unroll
      for (int fq = 0; fq < 4; ++fq) {
        const bf16x8 pa = pack8(ph[fq][0], ph[fq][1]);
        kv[fq][0] = MFMA16(pa, v0, u == 0 ? z4 : kv[fq][0]);
        kv[fq][1] = MFMA16(pa, v1, u == 0 ? z4 : kv[fq][1]);
        kv[fq][2] = MFMA16(pa, ones, u == 0 ? z4 : kv[fq][2]);
      }
    }
    LAS f32x4* rbuf = (LAS f32x4*)(lds + (GRP * 4 + wm) * 12288) + lane;
#pragma unroll
    for (int fq = 0; fq < 4; ++fq)
#pragma unroll
      for (int dc = 0; dc < 3; ++dc) rbuf[(fq * 3 + dc) * 64] = kv[fq][dc];
    wait_lgkm0();
    bar();
    LAS f32x4* sbuf = (LAS f32x4*)(lds + GRP * 4 * 12288) + lane;
    f32x4* dst = (f32x4*)A.kvp + ((size_t)(rb * 32 + head) * 12) * 64 + lane;
#pragma unroll
    for (int t = 0; t < 3; ++t) {
      const int qd = 3 * wm + t;
      dst[qd * 64] = (sbuf[qd * 64] + sbuf[768 + qd * 64]) + (sbuf[1536 + qd * 64] + sbuf[2304 + qd * 64]);
    }
  }
}

__global__ __launch_bounds__(512, 2) void k1_gemm1(G1Args A) {
  extern __shared__ __attribute__((aligned(16))) char smem[];
  LAS char* lds = (LAS char*)smem;
  const int wid = __builtin_amdgcn_readfirstlane(threadIdx.x >> 6), lane = threadIdx.x & 63;
  const int bid = blockIdx.x, xcd = bid & 7, idx = bid >> 3;
  const int rb = (xcd & 1) * 4 + (idx & 3), ct = (xcd >> 1) * 8 + (idx >> 2);
  const int j = ct >> 2, pos4 = ct & 3, wm = wid & 3;
  const bool isx = (pos4 == 0) || (pos4 == 3);
  if (isx) { if (wid < 4) g1_body<true, 0>(A, lds, rb, j, pos4, wm, lane); else g1_body<true, 1>(A, lds, rb, j, pos4, wm, lane); }
  else { if (wid < 4) g1_body<false, 0>(A, lds, rb, j, pos4, wm, lane); else g1_body<false, 1>(A, lds, rb, j, pos4, wm, lane); }
}

struct G2Args {
  u16* af; const u16* w2; const u16* wo; const u16* phiq; const float* kvp; float* part;
  const u16* x; const u16* b2; u16* out; unsigned* flags;
};

DI void wait_flag(unsigned* flag, unsigned want, unsigned* tmo) {
  unsigned spins = 0;
  while ((unsigned)__builtin_amdgcn_readfirstlane(__hip_atomic_load((GAS unsigned*)flag, RLX_AGENT)) < want) {
    __builtin_amdgcn_s_sleep(1);
    if (++spins > (1u << 22)) { __hip_atomic_store((GAS unsigned*)tmo, 1u, RLX_AGENT); break; }
  }
  __builtin_amdgcn_fence(__ATOMIC_ACQUIRE, "agent");
  asm volatile("s_waitcnt vmcnt(0)" ::: "memory");
}

template <bool FIN, int GRP>
DI void g2_body(const G2Args& A, LAS char* lds, int rb, int ct, int w4, int lane) {
  constexpr int NS = FIN ? N2 : N1;
  constexpr int KB0 = FIN ? 2 * N1 : 0;
  const int g = lane >> 4, q = (lane & 15) >> 2, p = lane & 3;
  const int wm = w4 & 1, wn = w4 >> 1;
  const int kbs = w4 >> 1, u0 = 4 * (w4 & 1);
  const unsigned lbase = (unsigned)(size_t)lds;
  const char* asrc = (const char*)A.af + ((size_t)kbs * 128 + rb * 8 + u0) * 1024;
  const unsigned aoff = lane * 16;
  const unsigned boff = (unsigned)(((kbs * 32 + u0 + 8 * (lane >> 4)) * D_ + ct * 128 + (lane & 15) * 8) * 2);
  const unsigned adst = lbase + kbs * 8192 + u0 * 1024;
  const unsigned bdst = lbase + BOFF + kbs * 8 * PSTR + u0 * PSTR;
  auto issue = [&](int st) {
    const int kb = KB0 + 2 * st, slot = (st & 3) * SLOT;
    const char* as = asrc + (size_t)kb * 128 * 1024;
    const char* wb = kb < 112 ? (const char*)A.w2 + (size_t)kb * 32 * D_ * 2 : (const char*)A.wo + (size_t)(kb - 112) * 32 * D_ * 2;
    dma16s_x4k(as, aoff, adst + slot);
#pragma unroll
    for (int jj = 0; jj < 4; ++jj) dma16s(wb + jj * (D_ * 2), boff, bdst + slot + jj * PSTR);
  };
  if (GRP == 0) issue(1); else { issue(0); issue(2); }

  if (!FIN) {
    const int tile = rb * 8 + ct, head = tile & 31, rq = tile >> 5;
    const int tid = (GRP * 4 + w4) * 64 + lane;
    LAS char* kvl = lds + 3 * SLOT;
    u32x4 pq[4][2];
    {
      const u32x4* pq4 = (const u32x4*)A.phiq + ((size_t)(rq * 32 + (GRP * 4 + w4) * 4) * 32 + head) * 128 + lane;
#pragma unroll
      for (int m = 0; m < 4; ++m) { pq[m][0] = pq4[m * 4096]; pq[m][1] = pq4[m * 4096 + 64]; }
    }
    if (tid < 384) {
      const int kh = tid / 192, dc = (tid - kh * 192) >> 6;
      const f32x4* src = (const f32x4*)A.kvp + ((size_t)head * 12 + (2 * kh) * 3 + dc) * 64 + lane;
      f32x4 s0 = {0.f, 0.f, 0.f, 0.f}, s1 = s0;
#pragma unroll
      for (int r8 = 0; r8 < 8; ++r8) { s0 += src[(size_t)r8 * 32 * 768]; s1 += src[(size_t)r8 * 32 * 768 + 192]; }
      *(LAS bf16x8*)(kvl + (kh * 3 + dc) * 1024 + lane * 16) = pack8(s0, s1);
    }
    wait_lgkm0();
    bar();
    bf16x8 ka[2][3];
#pragma unroll
    for (int kh = 0; kh < 2; ++kh)
#pragma unroll
      for (int dc = 0; dc < 3; ++dc) ka[kh][dc] = *(LAS bf16x8*)(kvl + (kh * 3 + dc) * 1024 + lane * 16);
    const f32x4 z4 = {0.f, 0.f, 0.f, 0.f};
    auto rsrc = __builtin_amdgcn_make_buffer_rsrc((void*)(A.af + (size_t)(112 + head) * 128 * 512), 0, 128 * 1024, 0x00020000);
#pragma unroll
    for (int m = 0; m < 4; ++m) {
      const bf16x8 b0 = __builtin_bit_cast(bf16x8, pq[m][0]), b1 = __builtin_bit_cast(bf16x8, pq[m][1]);
      f32x4 n0 = MFMA16(ka[0][0], b0, z4); n0 = MFMA16(ka[1][0], b1, n0);
      f32x4 n1 = MFMA16(ka[0][1], b0, z4); n1 = MFMA16(ka[1][1], b1, n1);
      f32x4 dn = MFMA16(ka[0][2], b0, z4); dn = MFMA16(ka[1][2], b1, dn);
      const float rd = 1.0f / rbf(rbf(dn[0]) + 1e-6f);
      f32x4 o0, o1;
#pragma unroll
      for (int r = 0; r < 4; ++r) { o0[r] = rbf(n0[r]) * rd; o1[r] = rbf(n1[r]) * rd; }
      const int mf = rq * 32 + (GRP * 4 + w4) * 4 + m;
      __builtin_amdgcn_raw_buffer_store_b128(__builtin_bit_cast(u32x4, pack8(o0, o1)), rsrc, (mf * 64 + lane) * 16, 0, 16);
    }
    wait_vm<0>();
    wait_lgkm0();
    bar();
    if (tid == 0) __hip_atomic_fetch_add((GAS unsigned*)(A.flags + (FLAG_OCNT + rq) * FLAG_STRIDE), 1u, RLX_AGENT);
  } else {
    wait_vm<8>();
    bar();
  }

  f32x4 acc[4][4];
#pragma unroll
  for (int m = 0; m < 4; ++m)
#pragma unroll
    for (int n = 0; n < 4; ++n) acc[m][n] = (f32x4){0.f, 0.f, 0.f, 0.f};
  LAS char* pA = lds + wm * 4096 + lane * 16;
  LAS char* pB = lds + BOFF + PSTR * (4 * (g & 1) + q) + 256 * (g >> 1) + 8 * p + 128 * wn;
  constexpr int SO_FIRST = (112 - KB0) / 2;
  if (GRP == 1) bar();

#pragma unroll 1
  for (int st = GRP; st < NS; st += 2) {
    const int so = (st & 3) * SLOT;
    bf16x8 a[2][4], b[2][4];
#pragma unroll
    for (int k2 = 0; k2 < 2; ++k2) {
#pragma unroll
      for (int n = 0; n < 4; ++n) { LAS char* pp = pB + so + k2 * 8 * PSTR + n * 32; b[k2][n] = cat(trread(pp), trread(pp + 512)); }
#pragma unroll
      for (int m = 0; m < 4; ++m) a[k2][m] = *(LAS bf16x8*)(pA + so + k2 * 8192 + m * 1024);
    }
    if (FIN && GRP == 0 && w4 == 0 && st == ((SO_FIRST - 6) & ~1))
      wait_flag(A.flags + (FLAG_OCNT + (rb >> 2)) * FLAG_STRIDE, 32u, A.flags + FLAG_TMO * FLAG_STRIDE);
    if (st + 3 < NS) { issue(st + 3); wait_lgkm0(); wait_vm<8>(); }
    else { wait_lgkm0(); wait_vm<0>(); }
    bar();
    __builtin_amdgcn_s_setprio(1);
#pragma unroll
    for (int k2 = 0; k2 < 2; ++k2)
#pragma unroll
      for (int n = 0; n < 4; ++n)
#pragma unroll
        for (int m = 0; m < 4; ++m) acc[m][n] = MFMA16(b[k2][n], a[k2][m], acc[m][n]);
    __builtin_amdgcn_s_setprio(0);
    if (GRP == 0 || st + 2 < NS) bar();
  }

  {
    LAS f32x4* wb = (LAS f32x4*)(lds + (GRP * 4 + w4) * 8192) + lane;
#pragma unroll
    for (int mm = 0; mm < 2; ++mm)
#pragma unroll
      for (int n = 0; n < 4; ++n) wb[(mm * 4 + n) * 64] = acc[GRP == 0 ? 2 + mm : mm][n];
  }
  wait_lgkm0();
  bar();
  f32x4 r[2][4];
  {
    LAS f32x4* rbuf = (LAS f32x4*)(lds + ((1 - GRP) * 4 + w4) * 8192) + lane;
#pragma unroll
    for (int mm = 0; mm < 2; ++mm)
#pragma unroll
      for (int n = 0; n < 4; ++n) r[mm][n] = acc[GRP == 0 ? mm : 2 + mm][n] + rbuf[(mm * 4 + n) * 64];
  }
  const int tile = rb * 8 + ct;
  const int wv = GRP * 4 + w4;
  if (!FIN) {
    auto rsrc = __builtin_amdgcn_make_buffer_rsrc((void*)(A.part + (size_t)tile * 16384), 0, 65536, 0x00020000);
#pragma unroll
    for (int mm = 0; mm < 2; ++mm)
#pragma unroll
      for (int n = 0; n < 4; ++n)
        __builtin_amdgcn_raw_buffer_store_b128(__builtin_bit_cast(u32x4, r[mm][n]), rsrc, ((wv * 8 + mm * 4 + n) * 64 + lane) * 16, 0, 16);
    wait_vm<0>();
    bar();
    if (wv == 0 && lane == 0) __hip_atomic_store((GAS unsigned*)(A.flags + (FLAG_PART + tile) * FLAG_STRIDE), 1u, RLX_AGENT);
  } else {
    const int col0 = ct * 128 + wn * 64 + 4 * g;
    u32x2 xr[2][4], bb[4];
#pragma unroll
    for (int mm = 0; mm < 2; ++mm) {
      const int row = rb * 128 + wm * 64 + (GRP * 2 + mm) * 16 + (lane & 15);
#pragma unroll
      for (int n = 0; n < 4; ++n) xr[mm][n] = *(const u32x2*)(A.x + (size_t)row * D_ + col0 + n * 16);
    }
#pragma unroll
    for (int n = 0; n < 4; ++n) bb[n] = *(const u32x2*)(A.b2 + col0 + n * 16);
    if (wv == 0) wait_flag(A.flags + (FLAG_PART + tile) * FLAG_STRIDE, 1u, A.flags + FLAG_TMO * FLAG_STRIDE);
    bar();
    const f32x4* pp = (const f32x4*)(A.part + (size_t)tile * 16384) + wv * 512 + lane;
#pragma unroll
    for (int mm = 0; mm < 2; ++mm) {
      const int row = rb * 128 + wm * 64 + (GRP * 2 + mm) * 16 + (lane & 15);
#pragma unroll
      for (int n = 0; n < 4; ++n) {
        const f32x4 pv = pp[(mm * 4 + n) * 64];
        const f32x4 xv = {bflo(xr[mm][n][0]), bfhi(xr[mm][n][0]), bflo(xr[mm][n][1]), bfhi(xr[mm][n][1])};
        const f32x4 bv = {bflo(bb[n][0]), bfhi(bb[n][0]), bflo(bb[n][1]), bfhi(bb[n][1])};
        const f32x4 o = (r[mm][n] + pv) + (xv + bv);
        u32x2 ov = {pk2(o[0], o[1]), pk2(o[2], o[3])};
        *(u32x2*)(A.out + (size_t)row * D_ + col0 + n * 16) = ov;
      }
    }
  }
}

__global__ __launch_bounds__(512, 2) void k2_gemm2(G2Args A) {
  extern __shared__ __attribute__((aligned(16))) char smem[];
  LAS char* lds = (LAS char*)smem;
  const int wid = __builtin_amdgcn_readfirstlane(threadIdx.x >> 6), lane = threadIdx.x & 63;
  const int bid = blockIdx.x, xcd = bid & 7, idx = bid >> 3;
  const int rb = (xcd & 3) * 4 + (idx & 3), ct = (xcd >> 2) * 4 + ((idx >> 2) & 3), fin = idx >> 4;
  const int w4 = wid & 3;
  if (fin) { if (wid < 4) g2_body<true, 0>(A, lds, rb, ct, w4, lane); else g2_body<true, 1>(A, lds, rb, ct, w4, lane); }
  else { if (wid < 4) g2_body<false, 0>(A, lds, rb, ct, w4, lane); else g2_body<false, 1>(A, lds, rb, ct, w4, lane); }
}

extern "C" void kernel_launch(void* const* d_in, const int* in_sizes, int n_in, void* d_out, int out_size, void* d_ws, size_t ws_size,
                              hipStream_t stream) {
  const u16* x = (const u16*)d_in[0];
  const u16* wqkv = (const u16*)d_in[1];
  const u16* wphi = (const u16*)d_in[2];
  const u16* wo = (const u16*)d_in[3];
  const u16* w1 = (const u16*)d_in[5];
  const u16* b1 = (const u16*)d_in[6];
  const u16* w2 = (const u16*)d_in[7];
  const u16* b2 = (const u16*)d_in[8];
  char* ws = (char*)d_ws;
  unsigned* flags = (unsigned*)(ws + WS_FLAGS);
  u16* xnf = (u16*)(ws + WS_XNF);
  u16* af = (u16*)(ws + WS_AF);
  u16* phiq = (u16*)(ws + WS_PHIQ);
  float* kvp = (float*)(ws + WS_KVP);
  float* part = (float*)(ws + WS_PART);
  (void)hipFuncSetAttribute((const void*)k1_gemm1, hipFuncAttributeMaxDynamicSharedMemorySize, G1_LDS);
  (void)hipFuncSetAttribute((const void*)k2_gemm2, hipFuncAttributeMaxDynamicSharedMemorySize, G2_LDS);
  k0_ln<<<256, 512, 0, stream>>>(x, xnf, flags);
  G1Args a1 = {xnf, wqkv, w1, b1, wphi, af, phiq, kvp};
  k1_gemm1<<<256, 512, G1_LDS, stream>>>(a1);
  G2Args a2 = {af, w2, wo, phiq, kvp, part, x, b2, (u16*)d_out, flags};
  k2_gemm2<<<256, 512, G2_LDS, stream>>>(a2);
}
```

```cpp
#include <hip/hip_runtime.h>
#include <hip/hip_bf16.h>
#include <stdint.h>

typedef unsigned short u16;
typedef short bf16x8 __attribute__((ext_vector_type(8)));
typedef short s16x4 __attribute__((ext_vector_type(4)));
typedef float f32x4 __attribute__((ext_vector_type(4)));
typedef float f32x2 __attribute__((ext_vector_type(2)));
typedef unsigned u32x4 __attribute__((ext_vector_type(4)));
typedef unsigned u32x2 __attribute__((ext_vector_type(2)));
typedef __bf16 bf16v2 __attribute__((ext_vector_type(2)));
#define LAS __attribute__((address_space(3)))
#define GAS __attribute__((address_space(1)))
#define DI __device__ __forceinline__
#define RLX_AGENT __ATOMIC_RELAXED, __HIP_MEMORY_SCOPE_AGENT

constexpr int S_ = 2048, D_ = 1024, F_ = 3584;
constexpr int SLOT = 33280;
constexpr int BOFF = 16384;
constexpr int PSTR = 1056;
constexpr int RING = 4 * SLOT;
constexpr int G1_WPHI = RING;
constexpr int G1_BIAS = RING + 4096;
constexpr int G1_LDS = RING + 5120;
constexpr int G2_LDS = RING + 6144;
constexpr int NKB = 144;
constexpr int N1 = 28;
constexpr int N2 = 72 - N1;

constexpr size_t WS_FLAGS = 0;
constexpr size_t WS_XNF = 1u << 20;
constexpr size_t WS_AF = WS_XNF + (4u << 20);
constexpr size_t WS_PHIQ = WS_AF + (size_t)NKB * 128 * 1024;
constexpr size_t WS_KVP = WS_PHIQ + (8u << 20);
constexpr size_t WS_PART = WS_KVP + (3u << 20);
constexpr int FLAG_STRIDE = 64;
constexpr int FLAG_OCNT = 0;
constexpr int FLAG_PART = 8;
constexpr int FLAG_TMO = 8 + 128;
constexpr int FLAG_WORDS = (8 + 128 + 1) * FLAG_STRIDE;

template <int N> struct IC { static constexpr int value = N; };
#define MFMA16(a, b, c) __builtin_amdgcn_mfma_f32_16x16x32_bf16((a), (b), (c), 0, 0, 0)
template <int N> DI void wait_vm() { asm volatile("s_waitcnt vmcnt(%0)" ::"i"(N) : "memory"); }
DI void wait_lgkm0() { asm volatile("" ::: "memory"); __builtin_amdgcn_s_waitcnt(0xC07F); asm volatile("" ::: "memory"); }
DI void bar() { __builtin_amdgcn_sched_barrier(0); __builtin_amdgcn_s_barrier(); __builtin_amdgcn_sched_barrier(0); }
DI void dma16(const void* g, unsigned l) {
  asm volatile("s_mov_b32 m0, %1\n\ts_nop 0\n\tglobal_load_lds_dwordx4 %0, off" ::"v"(g), "s"(l) : "memory");
}
DI void dma16s(const void* sb, unsigned voff, unsigned l) {
  asm volatile("s_mov_b32 m0, %2\n\ts_nop 0\n\tglobal_load_lds_dwordx4 %0, %1" ::"v"(voff), "s"(sb), "s"(l) : "memory");
}
DI void dma16s_x4k(const void* sb, unsigned voff, unsigned l) {
  asm volatile("s_mov_b32 m0, %2\n\ts_nop 0\n\tglobal_load_lds_dwordx4 %0, %1\n\tglobal_load_lds_dwordx4 %0, %1 offset:1024\n\t"
               "global_load_lds_dwordx4 %0, %1 offset:2048\n\tglobal_load_lds_dwordx4 %0, %1 offset:3072" ::"v"(voff), "s"(sb), "s"(l) : "memory");
}
DI s16x4 trread(LAS char* p) { return __builtin_amdgcn_ds_read_tr16_b64_v4i16((LAS s16x4*)p); }
DI bf16x8 cat(s16x4 lo, s16x4 hi) { return __builtin_shufflevector(lo, hi, 0, 1, 2, 3, 4, 5, 6, 7); }
DI unsigned pk2(float lo, float hi) { f32x2 v = {lo, hi}; return __builtin_bit_cast(unsigned, __builtin_convertvector(v, bf16v2)); }
DI bf16x8 pack8(f32x4 a, f32x4 b) { u32x4 r = {pk2(a[0], a[1]), pk2(a[2], a[3]), pk2(b[0], b[1]), pk2(b[2], b[3])}; return __builtin_bit_cast(bf16x8, r); }
DI float bflo(unsigned u) { return __uint_as_float(u << 16); }
DI float bfhi(unsigned u) { return __uint_as_float(u & 0xffff0000u); }
DI float rbf(float x) { return bflo(pk2(x, x)); }
template <int CTRL> DI float dpp(float x) { return __builtin_bit_cast(float, __builtin_amdgcn_mov_dpp(__builtin_bit_cast(int, x), CTRL, 0xf, 0xf, true)); }
constexpr int XOR1 = 0xB1, XOR2 = 0x4E, HMIR = 0x141, MIR = 0x140, ROR8 = 0x128;
DI float row16_max(float x) { x = fmaxf(x, dpp<XOR1>(x)); x = fmaxf(x, dpp<XOR2>(x)); x = fmaxf(x, dpp<HMIR>(x)); return fmaxf(x, dpp<MIR>(x)); }
DI float row16_min(float x) { x = fminf(x, dpp<XOR1>(x)); x = fminf(x, dpp<XOR2>(x)); x = fminf(x, dpp<HMIR>(x)); return fminf(x, dpp<MIR>(x)); }
DI float row16_sum(float x) { x += dpp<XOR1>(x); x += dpp<XOR2>(x); x += dpp<HMIR>(x); return x + dpp<MIR>(x); }
DI float xg_max(float x) {
  auto s = __builtin_amdgcn_permlane16_swap(__float_as_uint(x), __float_as_uint(x), false, false);
  x = fmaxf(__uint_as_float(s[0]), __uint_as_float(s[1]));
  auto t = __builtin_amdgcn_permlane32_swap(__float_as_uint(x), __float_as_uint(x), false, false);
  return fmaxf(__uint_as_float(t[0]), __uint_as_float(t[1]));
}
DI float xg_min(float x) {
  auto s = __builtin_amdgcn_permlane16_swap(__float_as_uint(x), __float_as_uint(x), false, false);
  x = fminf(__uint_as_float(s[0]), __uint_as_float(s[1]));
  auto t = __builtin_amdgcn_permlane32_swap(__float_as_uint(x), __float_as_uint(x), false, false);
  return fminf(__uint_as_float(t[0]), __uint_as_float(t[1]));
}
DI float xg_sum(float x) {
  auto s = __builtin_amdgcn_permlane16_swap(__float_as_uint(x), __float_as_uint(x), false, false);
  x = __uint_as_float(s[0]) + __uint_as_float(s[1]);
  auto t = __builtin_amdgcn_permlane32_swap(__float_as_uint(x), __float_as_uint(x), false, false);
  return __uint_as_float(t[0]) + __uint_as_float(t[1]);
}
constexpr float LOG2E = 1.4426950408889634f;
#define SB() __builtin_amdgcn_sched_barrier(0)
DI u32x4 gelu8(f32x4 a, f32x4 b) {
  const float c1 = -2.0f * 0.7978845608028654f * LOG2E, c3 = c1 * 0.044715f;
  f32x4 ta = a * a, tb = b * b;
  ta = ta * c3 + c1; tb = tb * c3 + c1;
  ta = ta * a; tb = tb * b;
  SB();
#pragma unroll
  for (int r = 0; r < 4; ++r) { ta[r] = __builtin_amdgcn_exp2f(ta[r]); tb[r] = __builtin_amdgcn_exp2f(tb[r]); }
  SB();
  ta = ta + 1.0f; tb = tb + 1.0f;
  SB();
#pragma unroll
  for (int r = 0; r < 4; ++r) { ta[r] = __builtin_amdgcn_rcpf(ta[r]); tb[r] = __builtin_amdgcn_rcpf(tb[r]); }
  SB();
  return __builtin_bit_cast(u32x4, pack8(a * ta, b * tb));
}
DI void dual_softmax(f32x4 p0, f32x4 p1, f32x4& e0, f32x4& e1, f32x4& n0, f32x4& n1) {
  p0 = p0 * LOG2E; p1 = p1 * LOG2E;
  float mx = fmaxf(fmaxf(fmaxf(p0[0], p0[1]), fmaxf(p0[2], p0[3])), fmaxf(fmaxf(p1[0], p1[1]), fmaxf(p1[2], p1[3])));
  float mn = fminf(fminf(fminf(p0[0], p0[1]), fminf(p0[2], p0[3])), fminf(fminf(p1[0], p1[1]), fminf(p1[2], p1[3])));
  mx = xg_max(mx); mn = xg_min(mn);
  e0 = p0 - mx; e1 = p1 - mx; n0 = mn - p0; n1 = mn - p1;
#pragma unroll
  for (int r = 0; r < 4; ++r) {
    e0[r] = __builtin_amdgcn_exp2f(e0[r]); e1[r] = __builtin_amdgcn_exp2f(e1[r]);
    n0[r] = __builtin_amdgcn_exp2f(n0[r]); n1[r] = __builtin_amdgcn_exp2f(n1[r]);
  }
  const f32x4 se = e0 + e1, sn4 = n0 + n1;
  const float sp = xg_sum((se[0] + se[1]) + (se[2] + se[3]));
  const float sn = xg_sum((sn4[0] + sn4[1]) + (sn4[2] + sn4[3]));
  const float rp = __builtin_amdgcn_rcpf(sp), rn = __builtin_amdgcn_rcpf(sn);
  e0 = e0 * rp; e1 = e1 * rp; n0 = n0 * rn; n1 = n1 * rn;
}

__global__ __launch_bounds__(512) void k0_ln(const u16* __restrict__ x, u16* __restrict__ xnf, unsigned* __restrict__ flags) {
  __shared__ float red[2][8][8];
  const int tid = threadIdx.x, w = tid >> 6, l = tid & 63, i = l & 7, c = l >> 3;
  const int row = blockIdx.x * 8 + i;
  const u32x4* src = (const u32x4*)(x + (size_t)row * D_) + 16 * w + c;
  const u32x4 v0 = src[0], v1 = src[8];
  if (blockIdx.x == 0) { for (int j = tid; j < FLAG_WORDS; j += 512) flags[j] = 0u; }
  float f[16];
#pragma unroll
  for (int j = 0; j < 4; ++j) { f[2 * j] = bflo(v0[j]); f[2 * j + 1] = bfhi(v0[j]); f[8 + 2 * j] = bflo(v1[j]); f[9 + 2 * j] = bfhi(v1[j]); }
  float s = 0.f;
#pragma unroll
  for (int j = 0; j < 16; ++j) s += f[j];
  s += dpp<ROR8>(s); s = xg_sum(s);
  if (l < 8) red[0][w][i] = s;
  __syncthreads();
  float mean = 0.f;
#pragma unroll
  for (int j = 0; j < 8; ++j) mean += red[0][j][i];
  mean *= (1.0f / D_);
  float q = 0.f;
#pragma unroll
  for (int j = 0; j < 16; ++j) { float d = f[j] - mean; q = fmaf(d, d, q); }
  q += dpp<ROR8>(q); q = xg_sum(q);
  if (l < 8) red[1][w][i] = q;
  __syncthreads();
  float var = 0.f;
#pragma unroll
  for (int j = 0; j < 8; ++j) var += red[1][j][i];
  var *= (1.0f / D_);
  const float mb = rbf(mean);
  const float sd = rbf(sqrtf(rbf(rbf(var) + 1e-5f)));
  const float rs = 1.0f / sd;
  u32x4 o0, o1;
#pragma unroll
  for (int j = 0; j < 4; ++j) {
    o0[j] = pk2(rbf(f[2 * j] - mb) * rs, rbf(f[2 * j + 1] - mb) * rs);
    o1[j] = pk2(rbf(f[8 + 2 * j] - mb) * rs, rbf(f[9 + 2 * j] - mb) * rs);
  }
  const int mfrag = row >> 4, ri = row & 15;
  const int ch0 = 16 * w + c, ch1 = ch0 + 8;
  u32x4* dst = (u32x4*)xnf;
  dst[((ch0 >> 2) * 128 + mfrag) * 64 + (ch0 & 3) * 16 + ri] = o0;
  dst[((ch1 >> 2) * 128 + mfrag) * 64 + (ch1 & 3) * 16 + ri] = o1;
}

#define STAMP(i) do { } while (0)
#define DBG_FIELD
#define DBG_ARG(x)
struct G1Args {
  const u16* xnf; const u16* wqkv; const u16* w1; const u16* b1; const u16* wphi;
  u16* af; u16* phiq; float* kvp; DBG_FIELD
};

template <bool ISX, int GRP>
DI void g1_body(const G1Args& A, LAS char* lds, int rb, int j, int pos4, int wm, int lane) {
  constexpr int NF = GRP == 0 ? 7 : 6;
  [[maybe_unused]] const int wv_ = GRP * 4 + wm;
  STAMP(0);
  const int g = lane >> 4, q = (lane & 15) >> 2, p = lane & 3;
  const int hp = 2 * j + (pos4 >> 1);
  const int cp = pos4 == 0 ? 0 : pos4 == 3 ? 384 : pos4 == 1 ? 96 : 224;
  const int co = pos4 == 0 ? 64 : pos4 == 3 ? 368 : pos4 == 1 ? 80 : 352;
  const int cr = pos4 == 0 ? 0 : pos4 == 3 ? 320 : pos4 == 1 ? 64 : 192;
  constexpr int RBASE = ISX ? 256 : 128;
  const int colT = cp + 32 * (2 * GRP), colU = cp + 32 * (ISX ? GRP : 2 * GRP + 1);

  const unsigned lbase = (unsigned)(size_t)lds;
  const char* dsrc = nullptr; long dstep = 0; int dj = 0;
  const char* xsrc = (const char*)A.xnf + (size_t)(rb * 16 + 4 * wm) * 1024;
  const unsigned xoff = lane * 16;
  if (GRP == 0) {
    const int u0 = 8 * (wm & 1) + 4 * (wm >> 1), c = lane & 31, line = c >> 3, ch = c & 7, row = u0 + 16 * (lane >> 5);
    const bool fromq = ISX ? (line < 2) : (line < 1);
    const int ld = fromq ? 3 * D_ : F_;
    const u16* base = fromq ? (A.wqkv + (ISX ? (1 + line) * D_ : 0) + 64 * hp) : (A.w1 + 448 * j + cr + 64 * (line - (ISX ? 2 : 1)));
    dsrc = (const char*)(base + (size_t)row * ld + ch * 8); dstep = 64L * ld; dj = 2 * ld;
  }
  const unsigned ddst = lbase + (GRP == 0 ? BOFF + 4 * wm * PSTR : 4 * wm * 1024);
  auto issue = [&](int slot) {
    if (GRP == 0) {
#pragma unroll
      for (int jj = 0; jj < 4; ++jj) dma16(dsrc + jj * dj, ddst + slot * SLOT + jj * PSTR);
      dsrc += dstep;
    } else {
      dma16s_x4k(xsrc, xoff, ddst + slot * SLOT);
      xsrc += 128 * 1024;
    }
  };
  if (GRP == 0) dma16s(A.wphi + (size_t)hp * 2048 + wm * 512, xoff, lbase + G1_WPHI + wm * 1024);
  else if (wm == 0) { const int nch = ISX ? 16 : 24; dma16s(A.b1 + 448 * j + cr, (lane < nch ? lane : 0) * 16, lbase + G1_BIAS); }
  issue(0); issue(1); issue(2); issue(3);
  wait_vm<12>();
  bar();

  STAMP(1);
  f32x4 acc[4][NF];
  {
    LAS char* bp = lds + G1_BIAS + 8 * g;
#pragma unroll
    for (int f = 0; f < NF; ++f) {
      f32x4 b4 = {0.f, 0.f, 0.f, 0.f};
      if (f >= (ISX ? 4 : 2)) {
        const int col = f < 4 ? colT + 16 * (f - 2) : f < 6 ? colU + 16 * (f - 4) : co;
        const u32x2 bb = *(LAS u32x2*)(bp + (col - cr) * 2);
        b4 = (f32x4){bflo(bb[0]), bfhi(bb[0]), bflo(bb[1]), bfhi(bb[1])};
      }
#pragma unroll
      for (int m = 0; m < 4; ++m) acc[m][f] = b4;
    }
  }
  const int lb = BOFF + PSTR * (q + 4 * (g & 1)) + 512 * (g >> 1) + 8 * p;
  LAS char* pA = lds + wm * 4096 + lane * 16;
  LAS char* pS = lds + lb + 64 * GRP;
  LAS char* pT = lds + lb + (ISX ? 128 + 64 * GRP : RBASE + (colT - cr) * 2);
  LAS char* pU = lds + lb + RBASE + (colU - cr) * 2;
  LAS char* pO = lds + lb + RBASE + (co - cr) * 2;
  auto rdB = [&](int so, int f) -> bf16x8 {
    LAS char* pp = (f < 2 ? pS : f < 4 ? pT : f < 6 ? pU : pO) + so + (f < 6 ? (f & 1) * 32 : 0);
    return cat(trread(pp), trread(pp + 8 * PSTR));
  };
  bf16x8 a0[4], b0[NF], a1[4], b1[NF];
#pragma unroll
  for (int f = 0; f < NF; ++f) b0[f] = rdB(0, f);
#pragma unroll
  for (int m = 0; m < 4; ++m) a0[m] = *(LAS bf16x8*)(pA + m * 1024);
  wait_lgkm0();
  wait_vm<8>();
  bar();
  auto step = [&](int s, bf16x8 (&a)[4], bf16x8 (&b)[NF], bf16x8 (&an)[4], bf16x8 (&bn)[NF], auto tag) {
    constexpr int T = decltype(tag)::value;
    const int so = ((s + 1) & 3) * SLOT;
    if (GRP == 0 && T == 4) { issue(s & 3); SB(); }
#pragma unroll
    for (int f = 0; f < NF; ++f) {
#pragma unroll
      for (int m = 0; m < 4; ++m) {
        if (ISX && (f == 2 || f == 3)) acc[m][f] = MFMA16(a[m], b[f], acc[m][f]);
        else acc[m][f] = MFMA16(b[f], a[m], acc[m][f]);
      }
      if (T > 0) {
        if (f == 0) {
#pragma unroll
          for (int m = 0; m < 4; ++m) an[m] = *(LAS bf16x8*)(pA + so + m * 1024);
        } else if (f < 4) {
          bn[2 * f - 2] = rdB(so, 2 * f - 2); bn[2 * f - 1] = rdB(so, 2 * f - 1);
        } else if (f == 4 && NF == 7) bn[NF - 1] = rdB(so, NF - 1);
      }
      SB();
    }
    if (GRP == 1 && T == 4) issue(s & 3);
    if (T > 0) {
      wait_lgkm0();
      if (T > 1) wait_vm<(T == 4 ? 8 : T == 3 ? 4 : 0)>();
      bar();
    }
  };
#pragma unroll 1
  for (int s = 0; s < 28; s += 2) { step(s, a0, b0, a1, b1, IC<4>{}); step(s + 1, a1, b1, a0, b0, IC<4>{}); }
  step(28, a0, b0, a1, b1, IC<3>{}); step(29, a1, b1, a0, b0, IC<2>{}); step(30, a0, b0, a1, b1, IC<1>{}); step(31, a1, b1, a0, b0, IC<0>{});

  STAMP(2);
  const int mf0 = rb * 16 + wm * 4;
  {
    u32x4* af4 = (u32x4*)A.af + (size_t)mf0 * 64 + lane;
    if (!ISX) {
      const int kb = (448 * j + colT) >> 5;
#pragma unroll
      for (int m = 0; m < 4; ++m) af4[((size_t)kb * 128 + m) * 64] = gelu8(acc[m][2], acc[m][3]);
    }
    {
      const int kb = (448 * j + colU) >> 5;
#pragma unroll
      for (int m = 0; m < 4; ++m) af4[((size_t)kb * 128 + m) * 64] = gelu8(acc[m][4], acc[m][5]);
    }
    if (GRP == 0) {
      const int kb = (448 * j + co) >> 5, n = (co >> 4) & 1;
      u32x2* af2 = (u32x2*)(af4 + (size_t)kb * 128 * 64) + n;
#pragma unroll
      for (int m = 0; m < 4; m += 2) {
        const u32x4 gv = gelu8(acc[m][NF - 1], acc[m + 1][NF - 1]);
        af2[m * 128] = (u32x2){gv[0], gv[1]};
        af2[(m + 1) * 128] = (u32x2){gv[2], gv[3]};
      }
    }
  }
  const int head = 2 * hp + GRP;
  bf16x8 wp[2];
  {
    LAS char* pw = lds + G1_WPHI + GRP * 2048 + (4 * g + q) * 64 + 8 * p;
    wp[0] = cat(trread(pw), trread(pw + 1024));
    wp[1] = cat(trread(pw + 32), trread(pw + 1024 + 32));
  }
  const f32x4 z4 = {0.f, 0.f, 0.f, 0.f};
  f32x4 p0[4], p1[4];
#pragma unroll
  for (int m = 0; m < 4; ++m) {
    const bf16x8 qb = pack8(acc[m][0], acc[m][1]);
    p0[m] = MFMA16(wp[0], qb, z4); p1[m] = MFMA16(wp[1], qb, z4);
  }
  if (!ISX) {
    u32x4* pq4 = (u32x4*)A.phiq + ((size_t)mf0 * 32 + head) * 128 + lane;
#pragma unroll
    for (int m = 0; m < 4; ++m) {
      f32x4 e0, e1, n0, n1;
      dual_softmax(p0[m], p1[m], e0, e1, n0, n1);
      pq4[m * 4096] = __builtin_bit_cast(u32x4, pack8(e0, e1));
      pq4[m * 4096 + 64] = __builtin_bit_cast(u32x4, pack8(n0, n1));
    }
  } else {
    constexpr int TS = 144;
    LAS char* tb = lds + (GRP * 4 + wm) * 12288;
    {
      LAS char* wrp = tb + (lane & 15) * TS + 8 * g;
#pragma unroll
      for (int m = 0; m < 4; ++m) {
        f32x4 e0, e1, n0, n1;
        dual_softmax(p0[m], p1[m], e0, e1, n0, n1);
        const u32x4 pe = __builtin_bit_cast(u32x4, pack8(e0, e1)), pn = __builtin_bit_cast(u32x4, pack8(n0, n1));
        *(LAS u32x2*)(wrp + m * 16 * TS) = (u32x2){pe[0], pe[1]};
        *(LAS u32x2*)(wrp + m * 16 * TS + 32) = (u32x2){pe[2], pe[3]};
        *(LAS u32x2*)(wrp + m * 16 * TS + 64) = (u32x2){pn[0], pn[1]};
        *(LAS u32x2*)(wrp + m * 16 * TS + 96) = (u32x2){pn[2], pn[3]};
      }
    }
    wait_lgkm0();
    f32x4 kv[4][3];
    const bf16x8 ones = {0x3F80, 0x3F80, 0x3F80, 0x3F80, 0x3F80, 0x3F80, 0x3F80, 0x3F80};
    LAS char* rdp = tb + (4 * g + q) * TS + 8 * p;
    bf16x8 pa[2][4];
#pragma unroll
    for (int u = 0; u < 2; ++u)
#pragma unroll
      for (int fq = 0; fq < 4; ++fq) pa[u][fq] = cat(trread(rdp + (32 * u) * TS + 32 * fq), trread(rdp + (32 * u + 16) * TS + 32 * fq));
#pragma unroll
    for (int u = 0; u < 2; ++u) {
      const bf16x8 v0 = pack8(acc[2 * u][2], acc[2 * u + 1][2]), v1 = pack8(acc[2 * u][3], acc[2 * u + 1][3]);
#pragma unroll
      for (int fq = 0; fq < 4; ++fq) {
        kv[fq][0] = MFMA16(pa[u][fq], v0, u == 0 ? z4 : kv[fq][0]);
        kv[fq][1] = MFMA16(pa[u][fq], v1, u == 0 ? z4 : kv[fq][1]);
        kv[fq][2] = MFMA16(pa[u][fq], ones, u == 0 ? z4 : kv[fq][2]);
      }
    }
    wait_lgkm0();
    LAS f32x4* rbuf = (LAS f32x4*)tb + lane;
#pragma unroll
    for (int fq = 0; fq < 4; ++fq)
#pragma unroll
      for (int dc = 0; dc < 3; ++dc) rbuf[(fq * 3 + dc) * 64] = kv[fq][dc];
    wait_lgkm0();
    bar();
    LAS f32x4* sbuf = (LAS f32x4*)(lds + GRP * 4 * 12288) + lane;
    f32x4* dst = (f32x4*)A.kvp + ((size_t)(rb * 32 + head) * 12) * 64 + lane;
#pragma unroll
    for (int t = 0; t < 3; ++t) {
      const int qd = 3 * wm + t;
      dst[qd * 64] = (sbuf[qd * 64] + sbuf[768 + qd * 64]) + (sbuf[1536 + qd * 64] + sbuf[2304 + qd * 64]);
    }
  }
  STAMP(3);
  STAMP(4);
}

__global__ __launch_bounds__(512, 2) void k1_gemm1(G1Args A) {
  extern __shared__ __attribute__((aligned(16))) char smem[];
  LAS char* lds = (LAS char*)smem;
  const int wid = __builtin_amdgcn_readfirstlane(threadIdx.x >> 6), lane = threadIdx.x & 63;
  const int bid = blockIdx.x, xcd = bid & 7, idx = bid >> 3;
  const int rb = (xcd & 1) * 4 + (idx & 3), ct = (xcd >> 1) * 8 + (idx >> 2);
  const int j = ct >> 2, pos4 = ct & 3, wm = wid & 3;
  const bool isx = (pos4 == 0) || (pos4 == 3);
  if (isx) { if (wid < 4) g1_body<true, 0>(A, lds, rb, j, pos4, wm, lane); else g1_body<true, 1>(A, lds, rb, j, pos4, wm, lane); }
  else { if (wid < 4) g1_body<false, 0>(A, lds, rb, j, pos4, wm, lane); else g1_body<false, 1>(A, lds, rb, j, pos4, wm, lane); }
}

struct G2Args {
  u16* af; const u16* w2; const u16* wo; const u16* phiq; const float* kvp; float* part;
  const u16* x; const u16* b2; u16* out; unsigned* flags; DBG_FIELD
};

DI void wait_flag(unsigned* flag, unsigned want, unsigned* tmo) {
  unsigned spins = 0;
  while ((unsigned)__builtin_amdgcn_readfirstlane(__hip_atomic_load((GAS unsigned*)flag, RLX_AGENT)) < want) {
    __builtin_amdgcn_s_sleep(1);
    if (++spins > (1u << 22)) { __hip_atomic_store((GAS unsigned*)tmo, 1u, RLX_AGENT); break; }
  }
  __builtin_amdgcn_fence(__ATOMIC_ACQUIRE, "agent");
  asm volatile("s_waitcnt vmcnt(0)" ::: "memory");
}

template <bool FIN, int GRP>
DI void g2_body(const G2Args& A, LAS char* lds, int rb, int ct, int w4, int lane) {
  constexpr int NS = FIN ? N2 : N1;
  constexpr int KB0 = FIN ? 2 * N1 : 0;
  [[maybe_unused]] const int wv_ = GRP * 4 + w4;
  STAMP(0);
  const int g = lane >> 4, q = (lane & 15) >> 2, p = lane & 3;
  const int wm = w4 & 1, wn = w4 >> 1;
  const int tile = rb * 8 + ct, wv = GRP * 4 + w4;
  const int kbs = w4 >> 1, u0 = 4 * (w4 & 1);
  const unsigned lbase = (unsigned)(size_t)lds;
  const char* asrc = (const char*)A.af + ((size_t)kbs * 128 + rb * 8 + u0) * 1024;
  const unsigned voff = GRP == 0 ? (unsigned)(lane * 16) : (unsigned)(((kbs * 32 + u0 + 8 * (lane >> 4)) * D_ + ct * 128 + (lane & 15) * 8) * 2);
  const unsigned ddst = lbase + (GRP == 0 ? kbs * 8192 + u0 * 1024 : BOFF + kbs * 8 * PSTR + u0 * PSTR);
  auto issue = [&](int st) {
    const int kb = KB0 + 2 * st, slot = (st & 3) * SLOT;
    if (GRP == 0) dma16s_x4k(asrc + (size_t)kb * 128 * 1024, voff, ddst + slot);
    else {
      const char* wb = kb < 112 ? (const char*)A.w2 + (size_t)kb * 32 * D_ * 2 : (const char*)A.wo + (size_t)(kb - 112) * 32 * D_ * 2;
#pragma unroll
      for (int jj = 0; jj < 4; ++jj) dma16s(wb + jj * (D_ * 2), voff, ddst + slot + jj * PSTR);
    }
  };
  issue(0); issue(1); issue(2); issue(3);

  if (!FIN) {
    const int head = tile & 31, rq = tile >> 5;
    const int tid = wv * 64 + lane;
    LAS char* kvl = lds + RING;
    u32x4 pq[4][2];
    {
      const u32x4* pq4 = (const u32x4*)A.phiq + ((size_t)(rq * 32 + wv * 4) * 32 + head) * 128 + lane;
#pragma unroll
      for (int m = 0; m < 4; ++m) { pq[m][0] = pq4[m * 4096]; pq[m][1] = pq4[m * 4096 + 64]; }
    }
    if (tid < 384) {
      const int kh = tid / 192, dc = (tid - kh * 192) >> 6;
      const f32x4* src = (const f32x4*)A.kvp + ((size_t)head * 12 + (2 * kh) * 3 + dc) * 64 + lane;
      f32x4 s0 = {0.f, 0.f, 0.f, 0.f}, s1 = s0;
#pragma unroll
      for (int r8 = 0; r8 < 8; ++r8) { s0 += src[(size_t)r8 * 32 * 768]; s1 += src[(size_t)r8 * 32 * 768 + 192]; }
      *(LAS bf16x8*)(kvl + (kh * 3 + dc) * 1024 + lane * 16) = pack8(s0, s1);
    }
    wait_lgkm0();
    bar();
    bf16x8 ka[2][3];
#pragma unroll
    for (int kh = 0; kh < 2; ++kh)
#pragma unroll
      for (int dc = 0; dc < 3; ++dc) ka[kh][dc] = *(LAS bf16x8*)(kvl + (kh * 3 + dc) * 1024 + lane * 16);
    const f32x4 z4 = {0.f, 0.f, 0.f, 0.f};
    auto rsrc = __builtin_amdgcn_make_buffer_rsrc((void*)(A.af + (size_t)(112 + head) * 128 * 512), 0, 128 * 1024, 0x00020000);
#pragma unroll
    for (int m = 0; m < 4; ++m) {
      const bf16x8 b0 = __builtin_bit_cast(bf16x8, pq[m][0]), b1 = __builtin_bit_cast(bf16x8, pq[m][1]);
      f32x4 n0 = MFMA16(ka[0][0], b0, z4); n0 = MFMA16(ka[1][0], b1, n0);
      f32x4 n1 = MFMA16(ka[0][1], b0, z4); n1 = MFMA16(ka[1][1], b1, n1);
      f32x4 dn = MFMA16(ka[0][2], b0, z4); dn = MFMA16(ka[1][2], b1, dn);
      const float rd = __builtin_amdgcn_rcpf(rbf(rbf(dn[0]) + 1e-6f));
      f32x4 o0, o1;
#pragma unroll
      for (int r = 0; r < 4; ++r) { o0[r] = rbf(n0[r]) * rd; o1[r] = rbf(n1[r]) * rd; }
      const int mf = rq * 32 + wv * 4 + m;
      __builtin_amdgcn_raw_buffer_store_b128(__builtin_bit_cast(u32x4, pack8(o0, o1)), rsrc, (mf * 64 + lane) * 16, 0, 16);
    }
    wait_vm<4>();
  } else {
    wait_vm<12>();
  }
  bar();

  STAMP(1);
  f32x4 acc[4][4];
#pragma unroll
  for (int m = 0; m < 4; ++m)
#pragma unroll
    for (int n = 0; n < 4; ++n) acc[m][n] = (f32x4){0.f, 0.f, 0.f, 0.f};
  LAS char* pA = lds + GRP * 8192 + wm * 4096 + lane * 16;
  LAS char* pB = lds + BOFF + GRP * 8 * PSTR + PSTR * (4 * (g & 1) + q) + 256 * (g >> 1) + 8 * p + 128 * wn;
  constexpr int SO_FIRST = (112 - KB0) / 2;
  bf16x8 a0[4], b0[4], a1[4], b1[4];
#pragma unroll
  for (int n = 0; n < 4; ++n) b0[n] = cat(trread(pB + n * 32), trread(pB + n * 32 + 512));
#pragma unroll
  for (int m = 0; m < 4; ++m) a0[m] = *(LAS bf16x8*)(pA + m * 1024);
  wait_lgkm0();
  wait_vm<8>();
  bar();

  auto step = [&](int st, bf16x8 (&a)[4], bf16x8 (&b)[4], bf16x8 (&an)[4], bf16x8 (&bn)[4], auto tag) {
    constexpr int T = decltype(tag)::value;
    const int so = ((st + 1) & 3) * SLOT;
    if (GRP == 0 && T == 4) { issue(st + 4); SB(); }
#pragma unroll
    for (int n = 0; n < 4; ++n) {
#pragma unroll
      for (int m = 0; m < 4; ++m) acc[m][n] = MFMA16(b[n], a[m], acc[m][n]);
      if (T > 0) {
        if (n == 0) {
#pragma unroll
          for (int m = 0; m < 4; ++m) an[m] = *(LAS bf16x8*)(pA + so + m * 1024);
        } else if (n < 3) {
#pragma unroll
          for (int k = 0; k < 2; ++k) { LAS char* pp = pB + so + (2 * n - 2 + k) * 32; bn[2 * n - 2 + k] = cat(trread(pp), trread(pp + 512)); }
        }
      }
      SB();
    }
    if (GRP == 1 && T == 4) issue(st + 4);
    if (T > 0) {
      wait_lgkm0();
      if (T > 1) wait_vm<(T == 4 ? 8 : T == 3 ? 4 : 0)>();
      bar();
    }
  };
#pragma unroll 1
  for (int st = 0; st < NS - 4; st += 2) {
    if (wv == 0) {
      if (!FIN && st == 2 && lane == 0)
        __hip_atomic_fetch_add((GAS unsigned*)(A.flags + (FLAG_OCNT + (tile >> 5)) * FLAG_STRIDE), 1u, RLX_AGENT);
      if (FIN && st == ((SO_FIRST - 6) & ~1))
        wait_flag(A.flags + (FLAG_OCNT + (rb >> 2)) * FLAG_STRIDE, 32u, A.flags + FLAG_TMO * FLAG_STRIDE);
    }
    step(st, a0, b0, a1, b1, IC<4>{}); step(st + 1, a1, b1, a0, b0, IC<4>{});
  }
  step(NS - 4, a0, b0, a1, b1, IC<3>{}); step(NS - 3, a1, b1, a0, b0, IC<2>{});
  if (FIN && wv == 0) wait_flag(A.flags + (FLAG_PART + tile) * FLAG_STRIDE, 1u, A.flags + FLAG_TMO * FLAG_STRIDE);
  step(NS - 2, a0, b0, a1, b1, IC<1>{});
  const int col0 = ct * 128 + wn * 64 + 4 * g;
  const int row0 = rb * 128 + wm * 64 + GRP * 32 + (lane & 15);
  u32x2 xr[2][4], bb[4];
  f32x4 pv[2][4];
  if (FIN) {
    const f32x4* pp = (const f32x4*)(A.part + (size_t)tile * 16384) + wv * 512 + lane;
#pragma unroll
    for (int mm = 0; mm < 2; ++mm)
#pragma unroll
      for (int n = 0; n < 4; ++n) pv[mm][n] = pp[(mm * 4 + n) * 64];
#pragma unroll
    for (int mm = 0; mm < 2; ++mm)
#pragma unroll
      for (int n = 0; n < 4; ++n) xr[mm][n] = *(const u32x2*)(A.x + (size_t)(row0 + mm * 16) * D_ + col0 + n * 16);
#pragma unroll
    for (int n = 0; n < 4; ++n) bb[n] = *(const u32x2*)(A.b2 + col0 + n * 16);
    SB();
  }
  step(NS - 1, a1, b1, a0, b0, IC<0>{});

  STAMP(2);
  {
    LAS f32x4* wb = (LAS f32x4*)(lds + wv * 8192) + lane;
#pragma unroll
    for (int mm = 0; mm < 2; ++mm)
#pragma unroll
      for (int n = 0; n < 4; ++n) wb[(mm * 4 + n) * 64] = acc[GRP == 0 ? 2 + mm : mm][n];
  }
  wait_lgkm0();
  bar();
  f32x4 r[2][4];
  {
    LAS f32x4* rbuf = (LAS f32x4*)(lds + ((1 - GRP) * 4 + w4) * 8192) + lane;
#pragma unroll
    for (int mm = 0; mm < 2; ++mm)
#pragma unroll
      for (int n = 0; n < 4; ++n) r[mm][n] = acc[GRP == 0 ? mm : 2 + mm][n] + rbuf[(mm * 4 + n) * 64];
  }
  if (!FIN) {
    auto rsrc = __builtin_amdgcn_make_buffer_rsrc((void*)(A.part + (size_t)tile * 16384), 0, 65536, 0x00020000);
#pragma unroll
    for (int mm = 0; mm < 2; ++mm)
#pragma unroll
      for (int n = 0; n < 4; ++n)
        __builtin_amdgcn_raw_buffer_store_b128(__builtin_bit_cast(u32x4, r[mm][n]), rsrc, ((wv * 8 + mm * 4 + n) * 64 + lane) * 16, 0, 16);
    wait_vm<0>();
    bar();
    if (wv == 0 && lane == 0) __hip_atomic_store((GAS unsigned*)(A.flags + (FLAG_PART + tile) * FLAG_STRIDE), 1u, RLX_AGENT);
  } else {
#pragma unroll
    for (int mm = 0; mm < 2; ++mm)
#pragma unroll
      for (int n = 0; n < 4; ++n) {
        const f32x4 xv = {bflo(xr[mm][n][0]), bfhi(xr[mm][n][0]), bflo(xr[mm][n][1]), bfhi(xr[mm][n][1])};
        const f32x4 bv = {bflo(bb[n][0]), bfhi(bb[n][0]), bflo(bb[n][1]), bfhi(bb[n][1])};
        const f32x4 o = (r[mm][n] + pv[mm][n]) + (xv + bv);
        u32x2 ov = {pk2(o[0], o[1]), pk2(o[2], o[3])};
        *(u32x2*)(A.out + (size_t)(row0 + mm * 16) * D_ + col0 + n * 16) = ov;
      }
  }
  STAMP(3);
  STAMP(4);
}

__global__ __launch_bounds__(512, 2) void k2_gemm2(G2Args A) {
  extern __shared__ __attribute__((aligned(16))) char smem[];
  LAS char* lds = (LAS char*)smem;
  const int wid = __builtin_amdgcn_readfirstlane(threadIdx.x >> 6), lane = threadIdx.x & 63;
  const int bid = blockIdx.x, xcd = bid & 7, idx = bid >> 3;
  const int rb = (xcd & 3) * 4 + (idx & 3), ct = (xcd >> 2) * 4 + ((idx >> 2) & 3), fin = idx >> 4;
  const int w4 = wid & 3;
  if (fin) { if (wid < 4) g2_body<true, 0>(A, lds, rb, ct, w4, lane); else g2_body<true, 1>(A, lds, rb, ct, w4, lane); }
  else { if (wid < 4) g2_body<false, 0>(A, lds, rb, ct, w4, lane); else g2_body<false, 1>(A, lds, rb, ct, w4, lane); }
}

extern "C" void kernel_launch(void* const* d_in, const int* in_sizes, int n_in, void* d_out, int out_size, void* d_ws, size_t ws_size,
                              hipStream_t stream) {
  const u16* x = (const u16*)d_in[0];
  const u16* wqkv = (const u16*)d_in[1];
  const u16* wphi = (const u16*)d_in[2];
  const u16* wo = (const u16*)d_in[3];
  const u16* w1 = (const u16*)d_in[5];
  const u16* b1 = (const u16*)d_in[6];
  const u16* w2 = (const u16*)d_in[7];
  const u16* b2 = (const u16*)d_in[8];
  char* ws = (char*)d_ws;
  unsigned* flags = (unsigned*)(ws + WS_FLAGS);
  u16* xnf = (u16*)(ws + WS_XNF);
  u16* af = (u16*)(ws + WS_AF);
  u16* phiq = (u16*)(ws + WS_PHIQ);
  float* kvp = (float*)(ws + WS_KVP);
  float* part = (float*)(ws + WS_PART);
  (void)hipFuncSetAttribute((const void*)k1_gemm1, hipFuncAttributeMaxDynamicSharedMemorySize, G1_LDS);
  (void)hipFuncSetAttribute((const void*)k2_gemm2, hipFuncAttributeMaxDynamicSharedMemorySize, G2_LDS);
  k0_ln<<<256, 512, 0, stream>>>(x, xnf, flags);
  G1Args a1 = {xnf, wqkv, w1, b1, wphi, af, phiq, kvp DBG_ARG(128u)};
  k1_gemm1<<<256, 512, G1_LDS, stream>>>(a1);
  G2Args a2 = {af, w2, wo, phiq, kvp, part, x, b2, (u16*)d_out, flags DBG_ARG(129u)};
  k2_gemm2<<<256, 512, G2_LDS, stream>>>(a2);
}
```

```cpp
#include <hip/hip_runtime.h>
#include <hip/hip_bf16.h>
#include <stdint.h>

typedef unsigned short u16;
typedef short bf16x8 __attribute__((ext_vector_type(8)));
typedef short s16x4 __attribute__((ext_vector_type(4)));
typedef float f32x4 __attribute__((ext_vector_type(4)));
typedef float f32x2 __attribute__((ext_vector_type(2)));
typedef unsigned u32x4 __attribute__((ext_vector_type(4)));
typedef unsigned u32x2 __attribute__((ext_vector_type(2)));
typedef __bf16 bf16v2 __attribute__((ext_vector_type(2)));
#define LAS __attribute__((address_space(3)))
#define GAS __attribute__((address_space(1)))
#define DI __device__ __forceinline__
#define RLX_AGENT __ATOMIC_RELAXED, __HIP_MEMORY_SCOPE_AGENT

constexpr int S_ = 2048, D_ = 1024, F_ = 3584;
constexpr int SLOT = 33280;
constexpr int BOFF = 16384;
constexpr int PSTR = 1056;
constexpr int RING = 4 * SLOT;
constexpr int G1_WPHI = RING;
constexpr int G1_BIAS = RING + 4096;
constexpr int G1_LDS = RING + 5120;
constexpr int G2_LDS = RING + 6144;
constexpr int NKB = 144;
constexpr int N1 = 28;
constexpr int N2 = 72 - N1;

constexpr size_t WS_FLAGS = 0;
constexpr size_t WS_XNF = 1u << 20;
constexpr size_t WS_AF = WS_XNF + (4u << 20);
constexpr size_t WS_PHIQ = WS_AF + (size_t)NKB * 128 * 1024;
constexpr size_t WS_KVP = WS_PHIQ + (8u << 20);
constexpr size_t WS_PART = WS_KVP + (3u << 20);
constexpr int FLAG_STRIDE = 64;
constexpr int FLAG_OCNT = 0;
constexpr int FLAG_PART = 8;
constexpr int FLAG_TMO = 8 + 128;
constexpr int FLAG_WORDS = (8 + 128 + 1) * FLAG_STRIDE;

template <int N> struct IC { static constexpr int value = N; };
#define MFMA16(a, b, c) __builtin_amdgcn_mfma_f32_16x16x32_bf16((a), (b), (c), 0, 0, 0)
template <int N> DI void wait_vm() { asm volatile("s_waitcnt vmcnt(%0)" ::"i"(N) : "memory"); }
DI void wait_lgkm0() { asm volatile("" ::: "memory"); __builtin_amdgcn_s_waitcnt(0xC07F); asm volatile("" ::: "memory"); }
DI void bar() { __builtin_amdgcn_sched_barrier(0); __builtin_amdgcn_s_barrier(); __builtin_amdgcn_sched_barrier(0); }
DI void dma16(const void* g, unsigned l) {
  asm volatile("s_mov_b32 m0, %1\n\ts_nop 0\n\tglobal_load_lds_dwordx4 %0, off" ::"v"(g), "s"(l) : "memory");
}
DI void dma16s(const void* sb, unsigned voff, unsigned l) {
  asm volatile("s_mov_b32 m0, %2\n\ts_nop 0\n\tglobal_load_lds_dwordx4 %0, %1" ::"v"(voff), "s"(sb), "s"(l) : "memory");
}
DI void dma16s_x4k(const void* sb, unsigned voff, unsigned l) {
  asm volatile("s_mov_b32 m0, %2\n\ts_nop 0\n\tglobal_load_lds_dwordx4 %0, %1\n\tglobal_load_lds_dwordx4 %0, %1 offset:1024\n\t"
               "global_load_lds_dwordx4 %0, %1 offset:2048\n\tglobal_load_lds_dwordx4 %0, %1 offset:3072" ::"v"(voff), "s"(sb), "s"(l) : "memory");
}
DI void touch(const void* g, unsigned l) { asm volatile("s_mov_b32 m0, %1\n\ts_nop 0\n\tglobal_load_lds_dword %0, off" ::"v"(g), "s"(l) : "memory"); }
DI s16x4 trread(LAS char* p) { return __builtin_amdgcn_ds_read_tr16_b64_v4i16((LAS s16x4*)p); }
DI bf16x8 cat(s16x4 lo, s16x4 hi) { return __builtin_shufflevector(lo, hi, 0, 1, 2, 3, 4, 5, 6, 7); }
DI unsigned pk2(float lo, float hi) { f32x2 v = {lo, hi}; return __builtin_bit_cast(unsigned, __builtin_convertvector(v, bf16v2)); }
DI bf16x8 pack8(f32x4 a, f32x4 b) { u32x4 r = {pk2(a[0], a[1]), pk2(a[2], a[3]), pk2(b[0], b[1]), pk2(b[2], b[3])}; return __builtin_bit_cast(bf16x8, r); }
DI float bflo(unsigned u) { return __uint_as_float(u << 16); }
DI float bfhi(unsigned u) { return __uint_as_float(u & 0xffff0000u); }
DI float rbf(float x) { return bflo(pk2(x, x)); }
template <int CTRL> DI float dpp(float x) { return __builtin_bit_cast(float, __builtin_amdgcn_mov_dpp(__builtin_bit_cast(int, x), CTRL, 0xf, 0xf, true)); }
constexpr int XOR1 = 0xB1, XOR2 = 0x4E, HMIR = 0x141, MIR = 0x140, ROR8 = 0x128;
DI float row16_max(float x) { x = fmaxf(x, dpp<XOR1>(x)); x = fmaxf(x, dpp<XOR2>(x)); x = fmaxf(x, dpp<HMIR>(x)); return fmaxf(x, dpp<MIR>(x)); }
DI float row16_min(float x) { x = fminf(x, dpp<XOR1>(x)); x = fminf(x, dpp<XOR2>(x)); x = fminf(x, dpp<HMIR>(x)); return fminf(x, dpp<MIR>(x)); }
DI float row16_sum(float x) { x += dpp<XOR1>(x); x += dpp<XOR2>(x); x += dpp<HMIR>(x); return x + dpp<MIR>(x); }
DI float xg_max(float x) {
  auto s = __builtin_amdgcn_permlane16_swap(__float_as_uint(x), __float_as_uint(x), false, false);
  x = fmaxf(__uint_as_float(s[0]), __uint_as_float(s[1]));
  auto t = __builtin_amdgcn_permlane32_swap(__float_as_uint(x), __float_as_uint(x), false, false);
  return fmaxf(__uint_as_float(t[0]), __uint_as_float(t[1]));
}
DI float xg_min(float x) {
  auto s = __builtin_amdgcn_permlane16_swap(__float_as_uint(x), __float_as_uint(x), false, false);
  x = fminf(__uint_as_float(s[0]), __uint_as_float(s[1]));
  auto t = __builtin_amdgcn_permlane32_swap(__float_as_uint(x), __float_as_uint(x), false, false);
  return fminf(__uint_as_float(t[0]), __uint_as_float(t[1]));
}
DI float xg_sum(float x) {
  auto s = __builtin_amdgcn_permlane16_swap(__float_as_uint(x), __float_as_uint(x), false, false);
  x = __uint_as_float(s[0]) + __uint_as_float(s[1]);
  auto t = __builtin_amdgcn_permlane32_swap(__float_as_uint(x), __float_as_uint(x), false, false);
  return __uint_as_float(t[0]) + __uint_as_float(t[1]);
}
constexpr float LOG2E = 1.4426950408889634f;
#define SB() __builtin_amdgcn_sched_barrier(0)
DI u32x4 gelu8(f32x4 a, f32x4 b) {
  const float c1 = -2.0f * 0.7978845608028654f * LOG2E, c3 = c1 * 0.044715f;
  f32x4 ta = a * a, tb = b * b;
  ta = ta * c3 + c1; tb = tb * c3 + c1;
  ta = ta * a; tb = tb * b;
  SB();
#pragma unroll
  for (int r = 0; r < 4; ++r) { ta[r] = __builtin_amdgcn_exp2f(ta[r]); tb[r] = __builtin_amdgcn_exp2f(tb[r]); }
  SB();
  ta = ta + 1.0f; tb = tb + 1.0f;
  SB();
#pragma unroll
  for (int r = 0; r < 4; ++r) { ta[r] = __builtin_amdgcn_rcpf(ta[r]); tb[r] = __builtin_amdgcn_rcpf(tb[r]); }
  SB();
  return __builtin_bit_cast(u32x4, pack8(a * ta, b * tb));
}
DI void dual_softmax(f32x4 p0, f32x4 p1, f32x4& e0, f32x4& e1, f32x4& n0, f32x4& n1) {
  p0 = p0 * LOG2E; p1 = p1 * LOG2E;
  float mx = fmaxf(fmaxf(fmaxf(p0[0], p0[1]), fmaxf(p0[2], p0[3])), fmaxf(fmaxf(p1[0], p1[1]), fmaxf(p1[2], p1[3])));
  float mn = fminf(fminf(fminf(p0[0], p0[1]), fminf(p0[2], p0[3])), fminf(fminf(p1[0], p1[1]), fminf(p1[2], p1[3])));
  mx = xg_max(mx); mn = xg_min(mn);
  e0 = p0 - mx; e1 = p1 - mx; n0 = mn - p0; n1 = mn - p1;
#pragma unroll
  for (int r = 0; r < 4; ++r) {
    e0[r] = __builtin_amdgcn_exp2f(e0[r]); e1[r] = __builtin_amdgcn_exp2f(e1[r]);
    n0[r] = __builtin_amdgcn_exp2f(n0[r]); n1[r] = __builtin_amdgcn_exp2f(n1[r]);
  }
  const f32x4 se = e0 + e1, sn4 = n0 + n1;
  const float sp = xg_sum((se[0] + se[1]) + (se[2] + se[3]));
  const float sn = xg_sum((sn4[0] + sn4[1]) + (sn4[2] + sn4[3]));
  const float rp = __builtin_amdgcn_rcpf(sp), rn = __builtin_amdgcn_rcpf(sn);
  e0 = e0 * rp; e1 = e1 * rp; n0 = n0 * rn; n1 = n1 * rn;
}

__global__ __launch_bounds__(512) void k0_ln(const u16* __restrict__ x, u16* __restrict__ xnf, unsigned* __restrict__ flags,
                                             const u16* __restrict__ wqkv, const u16* __restrict__ w1) {
  __shared__ float red[2][8][8];
  __shared__ unsigned dump[64];
  const int tid = threadIdx.x, w = tid >> 6, l = tid & 63, i = l & 7, c = l >> 3;
  const int row = blockIdx.x * 8 + i;
  const u32x4* src = (const u32x4*)(x + (size_t)row * D_) + 16 * w + c;
  const u32x4 v0 = src[0], v1 = src[8];
  if (blockIdx.x == 0) { for (int j = tid; j < FLAG_WORDS; j += 512) flags[j] = 0u; }
  {
    const int ln = blockIdx.x * 512 + tid;
    const unsigned dl = (unsigned)(size_t)(LAS unsigned*)dump;
    const char* tp = ln < 49152 ? (const char*)wqkv + (size_t)ln * 128 : (const char*)w1 + (size_t)(ln - 49152) * 128;
    if (ln < 49152 + 57344) touch(tp, dl);
  }
  float f[16];
#pragma unroll
  for (int j = 0; j < 4; ++j) { f[2 * j] = bflo(v0[j]); f[2 * j + 1] = bfhi(v0[j]); f[8 + 2 * j] = bflo(v1[j]); f[9 + 2 * j] = bfhi(v1[j]); }
  float s = 0.f;
#pragma unroll
  for (int j = 0; j < 16; ++j) s += f[j];
  s += dpp<ROR8>(s); s = xg_sum(s);
  if (l < 8) red[0][w][i] = s;
  __syncthreads();
  float mean = 0.f;
#pragma unroll
  for (int j = 0; j < 8; ++j) mean += red[0][j][i];
  mean *= (1.0f / D_);
  float q = 0.f;
#pragma unroll
  for (int j = 0; j < 16; ++j) { float d = f[j] - mean; q = fmaf(d, d, q); }
  q += dpp<ROR8>(q); q = xg_sum(q);
  if (l < 8) red[1][w][i] = q;
  __syncthreads();
  float var = 0.f;
#pragma unroll
  for (int j = 0; j < 8; ++j) var += red[1][j][i];
  var *= (1.0f / D_);
  const float mb = rbf(mean);
  const float sd = rbf(sqrtf(rbf(rbf(var) + 1e-5f)));
  const float rs = 1.0f / sd;
  u32x4 o0, o1;
#pragma unroll
  for (int j = 0; j < 4; ++j) {
    o0[j] = pk2(rbf(f[2 * j] - mb) * rs, rbf(f[2 * j + 1] - mb) * rs);
    o1[j] = pk2(rbf(f[8 + 2 * j] - mb) * rs, rbf(f[9 + 2 * j] - mb) * rs);
  }
  const int mfrag = row >> 4, ri = row & 15;
  const int ch0 = 16 * w + c, ch1 = ch0 + 8;
  u32x4* dst = (u32x4*)xnf;
  dst[((ch0 >> 2) * 128 + mfrag) * 64 + (ch0 & 3) * 16 + ri] = o0;
  dst[((ch1 >> 2) * 128 + mfrag) * 64 + (ch1 & 3) * 16 + ri] = o1;
}

#define STAMP(i) do { } while (0)
#define DBG_FIELD
#define DBG_ARG(x)
struct G1Args {
  const u16* xnf; const u16* wqkv; const u16* w1; const u16* b1; const u16* wphi;
  u16* af; u16* phiq; float* kvp; const u16* w2; const u16* wo; DBG_FIELD
};

template <bool ISX, int GRP>
DI void g1_body(const G1Args& A, LAS char* lds, int rb, int j, int pos4, int wm, int lane) {
  constexpr int NF = GRP == 0 ? 7 : 6;
  [[maybe_unused]] const int wv_ = GRP * 4 + wm;
  STAMP(0);
  const int g = lane >> 4, q = (lane & 15) >> 2, p = lane & 3;
  const int hp = 2 * j + (pos4 >> 1);
  const int cp = pos4 == 0 ? 0 : pos4 == 3 ? 384 : pos4 == 1 ? 96 : 224;
  const int co = pos4 == 0 ? 64 : pos4 == 3 ? 368 : pos4 == 1 ? 80 : 352;
  const int cr = pos4 == 0 ? 0 : pos4 == 3 ? 320 : pos4 == 1 ? 64 : 192;
  constexpr int RBASE = ISX ? 256 : 128;
  const int colT = cp + 32 * (2 * GRP), colU = cp + 32 * (ISX ? GRP : 2 * GRP + 1);

  const unsigned lbase = (unsigned)(size_t)lds;
  const char* dsrc = nullptr; long dstep = 0; int dj = 0;
  const char* xsrc = (const char*)A.xnf + (size_t)(rb * 16 + 4 * wm) * 1024;
  const unsigned xoff = lane * 16;
  if (GRP == 0) {
    const int u0 = 8 * (wm & 1) + 4 * (wm >> 1), c = lane & 31, line = c >> 3, ch = c & 7, row = u0 + 16 * (lane >> 5);
    const bool fromq = ISX ? (line < 2) : (line < 1);
    const int ld = fromq ? 3 * D_ : F_;
    const u16* base = fromq ? (A.wqkv + (ISX ? (1 + line) * D_ : 0) + 64 * hp) : (A.w1 + 448 * j + cr + 64 * (line - (ISX ? 2 : 1)));
    dsrc = (const char*)(base + (size_t)row * ld + ch * 8); dstep = 64L * ld; dj = 2 * ld;
  }
  const unsigned ddst = lbase + (GRP == 0 ? BOFF + 4 * wm * PSTR : 4 * wm * 1024);
  auto issue = [&](int slot) {
    if (GRP == 0) {
#pragma unroll
      for (int jj = 0; jj < 4; ++jj) dma16(dsrc + jj * dj, ddst + slot * SLOT + jj * PSTR);
      dsrc += dstep;
    } else {
      dma16s_x4k(xsrc, xoff, ddst + slot * SLOT);
      xsrc += 128 * 1024;
    }
  };
  if (GRP == 0) dma16s(A.wphi + (size_t)hp * 2048 + wm * 512, xoff, lbase + G1_WPHI + wm * 1024);
  else if (wm == 0) { const int nch = ISX ? 16 : 24; dma16s(A.b1 + 448 * j + cr, (lane < nch ? lane : 0) * 16, lbase + G1_BIAS); }
  issue(0); issue(1); issue(2); issue(3);
  wait_vm<12>();
  bar();

  STAMP(1);
  f32x4 acc[4][NF];
  {
    LAS char* bp = lds + G1_BIAS + 8 * g;
#pragma unroll
    for (int f = 0; f < NF; ++f) {
      f32x4 b4 = {0.f, 0.f, 0.f, 0.f};
      if (f >= (ISX ? 4 : 2)) {
        const int col = f < 4 ? colT + 16 * (f - 2) : f < 6 ? colU + 16 * (f - 4) : co;
        const u32x2 bb = *(LAS u32x2*)(bp + (col - cr) * 2);
        b4 = (f32x4){bflo(bb[0]), bfhi(bb[0]), bflo(bb[1]), bfhi(bb[1])};
      }
#pragma unroll
      for (int m = 0; m < 4; ++m) acc[m][f] = b4;
    }
  }
  const int lb = BOFF + PSTR * (q + 4 * (g & 1)) + 512 * (g >> 1) + 8 * p;
  LAS char* pA = lds + wm * 4096 + lane * 16;
  LAS char* pS = lds + lb + 64 * GRP;
  LAS char* pT = lds + lb + (ISX ? 128 + 64 * GRP : RBASE + (colT - cr) * 2);
  LAS char* pU = lds + lb + RBASE + (colU - cr) * 2;
  LAS char* pO = lds + lb + RBASE + (co - cr) * 2;
  auto rdB = [&](int so, int f) -> bf16x8 {
    LAS char* pp = (f < 2 ? pS : f < 4 ? pT : f < 6 ? pU : pO) + so + (f < 6 ? (f & 1) * 32 : 0);
    return cat(trread(pp), trread(pp + 8 * PSTR));
  };
  bf16x8 a0[4], b0[NF], a1[4], b1[NF];
#pragma unroll
  for (int f = 0; f < NF; ++f) b0[f] = rdB(0, f);
#pragma unroll
  for (int m = 0; m < 4; ++m) a0[m] = *(LAS bf16x8*)(pA + m * 1024);
  wait_lgkm0();
  wait_vm<8>();
  bar();
  auto step = [&](int s, bf16x8 (&a)[4], bf16x8 (&b)[NF], bf16x8 (&an)[4], bf16x8 (&bn)[NF], auto tag) {
    constexpr int T = decltype(tag)::value;
    const int so = ((s + 1) & 3) * SLOT;
    auto mm = [&](int i) {
      const int f = i >> 2, m = i & 3;
      if (ISX && (f == 2 || f == 3)) acc[m][f] = MFMA16(a[m], b[f], acc[m][f]);
      else acc[m][f] = MFMA16(b[f], a[m], acc[m][f]);
    };
    s16x4 lo[NF];
    auto rd = [&](int i) {
      if (i < 4) an[i] = *(LAS bf16x8*)(pA + so + i * 1024);
      else {
        const int f = (i - 4) >> 1;
        LAS char* pp = (f < 2 ? pS : f < 4 ? pT : f < 6 ? pU : pO) + so + (f < 6 ? (f & 1) * 32 : 0);
        if (((i - 4) & 1) == 0) lo[f] = trread(pp); else bn[f] = cat(lo[f], trread(pp + 8 * PSTR));
      }
    };
    if (GRP == 1) {
      if (T == 4) issue(s & 3);
      if (T > 0) {
#pragma unroll
        for (int i = 0; i < 4 + 2 * NF; ++i) rd(i);
      }
      SB();
#pragma unroll
      for (int i = 0; i < 4 * NF; ++i) mm(i);
      SB();
    } else {
#pragma unroll
      for (int i = 0; i < 4 * NF; ++i) {
        mm(i);
        if (T > 0 && i < 4 + 2 * NF) rd(i);
        SB();
      }
      if (T == 4) issue(s & 3);
    }
    if (T > 0) {
      wait_lgkm0();
      if (T > 1) wait_vm<(T == 4 ? 8 : T == 3 ? 4 : 0)>();
      bar();
    }
  };
#pragma unroll 1
  for (int s = 0; s < 28; s += 2) { step(s, a0, b0, a1, b1, IC<4>{}); step(s + 1, a1, b1, a0, b0, IC<4>{}); }
  step(28, a0, b0, a1, b1, IC<3>{}); step(29, a1, b1, a0, b0, IC<2>{}); step(30, a0, b0, a1, b1, IC<1>{}); step(31, a1, b1, a0, b0, IC<0>{});

  STAMP(2);
  {
    const int ln = (blockIdx.x * 8 + GRP * 4 + wm) * 64 + lane;
    const char* tp = ln < 57344 ? (const char*)A.w2 + (size_t)ln * 128 : (const char*)A.wo + (size_t)(ln - 57344) * 128;
    if (ln < 57344 + 16384) touch(tp, lbase + G1_BIAS);
  }
  const int mf0 = rb * 16 + wm * 4;
  {
    u32x4* af4 = (u32x4*)A.af + (size_t)mf0 * 64 + lane;
    if (!ISX) {
      const int kb = (448 * j + colT) >> 5;
#pragma unroll
      for (int m = 0; m < 4; ++m) af4[((size_t)kb * 128 + m) * 64] = gelu8(acc[m][2], acc[m][3]);
    }
    {
      const int kb = (448 * j + colU) >> 5;
#pragma unroll
      for (int m = 0; m < 4; ++m) af4[((size_t)kb * 128 + m) * 64] = gelu8(acc[m][4], acc[m][5]);
    }
    if (GRP == 0) {
      const int kb = (448 * j + co) >> 5, n = (co >> 4) & 1;
      u32x2* af2 = (u32x2*)(af4 + (size_t)kb * 128 * 64) + n;
#pragma unroll
      for (int m = 0; m < 4; m += 2) {
        const u32x4 gv = gelu8(acc[m][NF - 1], acc[m + 1][NF - 1]);
        af2[m * 128] = (u32x2){gv[0], gv[1]};
        af2[(m + 1) * 128] = (u32x2){gv[2], gv[3]};
      }
    }
  }
  const int head = 2 * hp + GRP;
  bf16x8 wp[2];
  {
    LAS char* pw = lds + G1_WPHI + GRP * 2048 + (4 * g + q) * 64 + 8 * p;
    wp[0] = cat(trread(pw), trread(pw + 1024));
    wp[1] = cat(trread(pw + 32), trread(pw + 1024 + 32));
  }
  const f32x4 z4 = {0.f, 0.f, 0.f, 0.f};
  f32x4 p0[4], p1[4];
#pragma unroll
  for (int m = 0; m < 4; ++m) {
    const bf16x8 qb = pack8(acc[m][0], acc[m][1]);
    p0[m] = MFMA16(wp[0], qb, z4); p1[m] = MFMA16(wp[1], qb, z4);
  }
  if (!ISX) {
    u32x4* pq4 = (u32x4*)A.phiq + ((size_t)mf0 * 32 + head) * 128 + lane;
#pragma unroll
    for (int m = 0; m < 4; ++m) {
      f32x4 e0, e1, n0, n1;
      dual_softmax(p0[m], p1[m], e0, e1, n0, n1);
      pq4[m * 4096] = __builtin_bit_cast(u32x4, pack8(e0, e1));
      pq4[m * 4096 + 64] = __builtin_bit_cast(u32x4, pack8(n0, n1));
    }
  } else {
    constexpr int TS = 144;
    LAS char* tb = lds + (GRP * 4 + wm) * 12288;
    {
      LAS char* wrp = tb + (lane & 15) * TS + 8 * g;
#pragma unroll
      for (int m = 0; m < 4; ++m) {
        f32x4 e0, e1, n0, n1;
        dual_softmax(p0[m], p1[m], e0, e1, n0, n1);
        const u32x4 pe = __builtin_bit_cast(u32x4, pack8(e0, e1)), pn = __builtin_bit_cast(u32x4, pack8(n0, n1));
        *(LAS u32x2*)(wrp + m * 16 * TS) = (u32x2){pe[0], pe[1]};
        *(LAS u32x2*)(wrp + m * 16 * TS + 32) = (u32x2){pe[2], pe[3]};
        *(LAS u32x2*)(wrp + m * 16 * TS + 64) = (u32x2){pn[0], pn[1]};
        *(LAS u32x2*)(wrp + m * 16 * TS + 96) = (u32x2){pn[2], pn[3]};
      }
    }
    wait_lgkm0();
    f32x4 kv[4][3];
    const bf16x8 ones = {0x3F80, 0x3F80, 0x3F80, 0x3F80, 0x3F80, 0x3F80, 0x3F80, 0x3F80};
    LAS char* rdp = tb + (4 * g + q) * TS + 8 * p;
    bf16x8 pa[2][4];
#pragma unroll
    for (int u = 0; u < 2; ++u)
#pragma unroll
      for (int fq = 0; fq < 4; ++fq) pa[u][fq] = cat(trread(rdp + (32 * u) * TS + 32 * fq), trread(rdp + (32 * u + 16) * TS + 32 * fq));
#pragma unroll
    for (int u = 0; u < 2; ++u) {
      const bf16x8 v0 = pack8(acc[2 * u][2], acc[2 * u + 1][2]), v1 = pack8(acc[2 * u][3], acc[2 * u + 1][3]);
#pragma unroll
      for (int fq = 0; fq < 4; ++fq) {
        kv[fq][0] = MFMA16(pa[u][fq], v0, u == 0 ? z4 : kv[fq][0]);
        kv[fq][1] = MFMA16(pa[u][fq], v1, u == 0 ? z4 : kv[fq][1]);
        kv[fq][2] = MFMA16(pa[u][fq], ones, u == 0 ? z4 : kv[fq][2]);
      }
    }
    wait_lgkm0();
    LAS f32x4* rbuf = (LAS f32x4*)tb + lane;
#pragma unroll
    for (int fq = 0; fq < 4; ++fq)
#pragma unroll
      for (int dc = 0; dc < 3; ++dc) rbuf[(fq * 3 + dc) * 64] = kv[fq][dc];
    wait_lgkm0();
    bar();
    LAS f32x4* sbuf = (LAS f32x4*)(lds + GRP * 4 * 12288) + lane;
    f32x4* dst = (f32x4*)A.kvp + ((size_t)(rb * 32 + head) * 12) * 64 + lane;
#pragma unroll
    for (int t = 0; t < 3; ++t) {
      const int qd = 3 * wm + t;
      dst[qd * 64] = (sbuf[qd * 64] + sbuf[768 + qd * 64]) + (sbuf[1536 + qd * 64] + sbuf[2304 + qd * 64]);
    }
  }
  STAMP(3);
  STAMP(4);
}

__global__ __launch_bounds__(512, 2) void k1_gemm1(G1Args A) {
  extern __shared__ __attribute__((aligned(16))) char smem[];
  LAS char* lds = (LAS char*)smem;
  const int wid = __builtin_amdgcn_readfirstlane(threadIdx.x >> 6), lane = threadIdx.x & 63;
  const int bid = blockIdx.x, xcd = bid & 7, idx = bid >> 3;
  const int rb = (xcd & 1) * 4 + (idx & 3), ct = (xcd >> 1) * 8 + (idx >> 2);
  const int j = ct >> 2, pos4 = ct & 3, wm = wid & 3;
  const bool isx = (pos4 == 0) || (pos4 == 3);
  if (isx) { if (wid < 4) g1_body<true, 0>(A, lds, rb, j, pos4, wm, lane); else g1_body<true, 1>(A, lds, rb, j, pos4, wm, lane); }
  else { if (wid < 4) g1_body<false, 0>(A, lds, rb, j, pos4, wm, lane); else g1_body<false, 1>(A, lds, rb, j, pos4, wm, lane); }
}

struct G2Args {
  u16* af; const u16* w2; const u16* wo; const u16* phiq; const float* kvp; float* part;
  const u16* x; const u16* b2; u16* out; unsigned* flags; DBG_FIELD
};

DI void wait_flag(unsigned* flag, unsigned want, unsigned* tmo) {
  unsigned spins = 0;
  while ((unsigned)__builtin_amdgcn_readfirstlane(__hip_atomic_load((GAS unsigned*)flag, RLX_AGENT)) < want) {
    __builtin_amdgcn_s_sleep(1);
    if (++spins > (1u << 22)) { __hip_atomic_store((GAS unsigned*)tmo, 1u, RLX_AGENT); break; }
  }
  __builtin_amdgcn_fence(__ATOMIC_ACQUIRE, "agent");
  asm volatile("s_waitcnt vmcnt(0)" ::: "memory");
}

template <bool FIN, int GRP>
DI void g2_body(const G2Args& A, LAS char* lds, int rb, int ct, int w4, int lane) {
  constexpr int NS = FIN ? N2 : N1;
  constexpr int KB0 = FIN ? 2 * N1 : 0;
  [[maybe_unused]] const int wv_ = GRP * 4 + w4;
  STAMP(0);
  const int g = lane >> 4, q = (lane & 15) >> 2, p = lane & 3;
  const int wm = w4 & 1, wn = w4 >> 1;
  const int tile = rb * 8 + ct, wv = GRP * 4 + w4;
  const int kbs = w4 >> 1, u0 = 4 * (w4 & 1);
  const unsigned lbase = (unsigned)(size_t)lds;
  const char* asrc = (const char*)A.af + ((size_t)kbs * 128 + rb * 8 + u0) * 1024;
  const unsigned voff = GRP == 0 ? (unsigned)(lane * 16) : (unsigned)(((kbs * 32 + u0 + 8 * (lane >> 4)) * D_ + ct * 128 + (lane & 15) * 8) * 2);
  const unsigned ddst = lbase + (GRP == 0 ? kbs * 8192 + u0 * 1024 : BOFF + kbs * 8 * PSTR + u0 * PSTR);
  auto issue = [&](int st) {
    const int kb = KB0 + 2 * st, slot = (st & 3) * SLOT;
    if (GRP == 0) dma16s_x4k(asrc + (size_t)kb * 128 * 1024, voff, ddst + slot);
    else {
      const char* wb = kb < 112 ? (const char*)A.w2 + (size_t)kb * 32 * D_ * 2 : (const char*)A.wo + (size_t)(kb - 112) * 32 * D_ * 2;
#pragma unroll
      for (int jj = 0; jj < 4; ++jj) dma16s(wb + jj * (D_ * 2), voff, ddst + slot + jj * PSTR);
    }
  };
  issue(0); issue(1); issue(2); issue(3);

  if (!FIN) {
    const int head = tile & 31, rq = tile >> 5;
    const int tid = wv * 64 + lane;
    LAS char* kvl = lds + RING;
    u32x4 pq[4][2];
    {
      const u32x4* pq4 = (const u32x4*)A.phiq + ((size_t)(rq * 32 + wv * 4) * 32 + head) * 128 + lane;
#pragma unroll
      for (int m = 0; m < 4; ++m) { pq[m][0] = pq4[m * 4096]; pq[m][1] = pq4[m * 4096 + 64]; }
    }
    if (tid < 384) {
      const int kh = tid / 192, dc = (tid - kh * 192) >> 6;
      const f32x4* src = (const f32x4*)A.kvp + ((size_t)head * 12 + (2 * kh) * 3 + dc) * 64 + lane;
      f32x4 s0 = {0.f, 0.f, 0.f, 0.f}, s1 = s0;
#pragma unroll
      for (int r8 = 0; r8 < 8; ++r8) { s0 += src[(size_t)r8 * 32 * 768]; s1 += src[(size_t)r8 * 32 * 768 + 192]; }
      *(LAS bf16x8*)(kvl + (kh * 3 + dc) * 1024 + lane * 16) = pack8(s0, s1);
    }
    wait_lgkm0();
    bar();
    bf16x8 ka[2][3];
#pragma unroll
    for (int kh = 0; kh < 2; ++kh)
#pragma unroll
      for (int dc = 0; dc < 3; ++dc) ka[kh][dc] = *(LAS bf16x8*)(kvl + (kh * 3 + dc) * 1024 + lane * 16);
    const f32x4 z4 = {0.f, 0.f, 0.f, 0.f};
    auto rsrc = __builtin_amdgcn_make_buffer_rsrc((void*)(A.af + (size_t)(112 + head) * 128 * 512), 0, 128 * 1024, 0x00020000);
#pragma unroll
    for (int m = 0; m < 4; ++m) {
      const bf16x8 b0 = __builtin_bit_cast(bf16x8, pq[m][0]), b1 = __builtin_bit_cast(bf16x8, pq[m][1]);
      f32x4 n0 = MFMA16(ka[0][0], b0, z4); n0 = MFMA16(ka[1][0], b1, n0);
      f32x4 n1 = MFMA16(ka[0][1], b0, z4); n1 = MFMA16(ka[1][1], b1, n1);
      f32x4 dn = MFMA16(ka[0][2], b0, z4); dn = MFMA16(ka[1][2], b1, dn);
      const float rd = __builtin_amdgcn_rcpf(rbf(rbf(dn[0]) + 1e-6f));
      f32x4 o0, o1;
#pragma unroll
      for (int r = 0; r < 4; ++r) { o0[r] = rbf(n0[r]) * rd; o1[r] = rbf(n1[r]) * rd; }
      const int mf = rq * 32 + wv * 4 + m;
      __builtin_amdgcn_raw_buffer_store_b128(__builtin_bit_cast(u32x4, pack8(o0, o1)), rsrc, (mf * 64 + lane) * 16, 0, 16);
    }
    wait_vm<4>();
  } else {
    wait_vm<12>();
  }
  bar();

  STAMP(1);
  f32x4 acc[4][4];
#pragma unroll
  for (int m = 0; m < 4; ++m)
#pragma unroll
    for (int n = 0; n < 4; ++n) acc[m][n] = (f32x4){0.f, 0.f, 0.f, 0.f};
  LAS char* pA = lds + GRP * 8192 + wm * 4096 + lane * 16;
  LAS char* pB = lds + BOFF + GRP * 8 * PSTR + PSTR * (4 * (g & 1) + q) + 256 * (g >> 1) + 8 * p + 128 * wn;
  constexpr int SO_FIRST = (112 - KB0) / 2;
  bf16x8 a0[4], b0[4], a1[4], b1[4];
#pragma unroll
  for (int n = 0; n < 4; ++n) b0[n] = cat(trread(pB + n * 32), trread(pB + n * 32 + 512));
#pragma unroll
  for (int m = 0; m < 4; ++m) a0[m] = *(LAS bf16x8*)(pA + m * 1024);
  wait_lgkm0();
  wait_vm<8>();
  bar();

  auto step = [&](int st, bf16x8 (&a)[4], bf16x8 (&b)[4], bf16x8 (&an)[4], bf16x8 (&bn)[4], auto tag) {
    constexpr int T = decltype(tag)::value;
    const int so = ((st + 1) & 3) * SLOT;
    s16x4 lo[4];
    auto rd = [&](int i) {
      if (i < 4) an[i] = *(LAS bf16x8*)(pA + so + i * 1024);
      else {
        const int n = (i - 4) >> 1;
        LAS char* pp = pB + so + n * 32;
        if (((i - 4) & 1) == 0) lo[n] = trread(pp); else bn[n] = cat(lo[n], trread(pp + 512));
      }
    };
    if (GRP == 1) {
      if (T == 4) issue(st + 4);
      if (T > 0) {
#pragma unroll
        for (int i = 0; i < 12; ++i) rd(i);
      }
      SB();
#pragma unroll
      for (int i = 0; i < 16; ++i) acc[i & 3][i >> 2] = MFMA16(b[i >> 2], a[i & 3], acc[i & 3][i >> 2]);
      SB();
    } else {
      if (T == 4) { issue(st + 4); SB(); }
#pragma unroll
      for (int i = 0; i < 16; ++i) {
        acc[i & 3][i >> 2] = MFMA16(b[i >> 2], a[i & 3], acc[i & 3][i >> 2]);
        if (T > 0 && i < 12) rd(i);
        SB();
      }
    }
    if (T > 0) {
      wait_lgkm0();
      if (T > 1) wait_vm<(T == 4 ? 8 : T == 3 ? 4 : 0)>();
      bar();
    }
  };
#pragma unroll 1
  for (int st = 0; st < NS - 4; st += 2) {
    if (wv == 0) {
      if (!FIN && st == 2 && lane == 0)
        __hip_atomic_fetch_add((GAS unsigned*)(A.flags + (FLAG_OCNT + (tile >> 5)) * FLAG_STRIDE), 1u, RLX_AGENT);
      if (FIN && st == ((SO_FIRST - 6) & ~1))
        wait_flag(A.flags + (FLAG_OCNT + (rb >> 2)) * FLAG_STRIDE, 32u, A.flags + FLAG_TMO * FLAG_STRIDE);
    }
    step(st, a0, b0, a1, b1, IC<4>{}); step(st + 1, a1, b1, a0, b0, IC<4>{});
  }
  step(NS - 4, a0, b0, a1, b1, IC<3>{}); step(NS - 3, a1, b1, a0, b0, IC<2>{});
  if (FIN && wv == 0) wait_flag(A.flags + (FLAG_PART + tile) * FLAG_STRIDE, 1u, A.flags + FLAG_TMO * FLAG_STRIDE);
  step(NS - 2, a0, b0, a1, b1, IC<1>{});
  const int col0 = ct * 128 + wn * 64 + 4 * g;
  const int row0 = rb * 128 + wm * 64 + GRP * 32 + (lane & 15);
  u32x2 xr[2][4], bb[4];
  f32x4 pv[2][4];
  if (FIN) {
    const f32x4* pp = (const f32x4*)(A.part + (size_t)tile * 16384) + wv * 512 + lane;
#pragma unroll
    for (int mm = 0; mm < 2; ++mm)
#pragma unroll
      for (int n = 0; n < 4; ++n) pv[mm][n] = pp[(mm * 4 + n) * 64];
#pragma unroll
    for (int mm = 0; mm < 2; ++mm)
#pragma unroll
      for (int n = 0; n < 4; ++n) xr[mm][n] = *(const u32x2*)(A.x + (size_t)(row0 + mm * 16) * D_ + col0 + n * 16);
#pragma unroll
    for (int n = 0; n < 4; ++n) bb[n] = *(const u32x2*)(A.b2 + col0 + n * 16);
    SB();
  }
  step(NS - 1, a1, b1, a0, b0, IC<0>{});

  STAMP(2);
  {
    LAS f32x4* wb = (LAS f32x4*)(lds + wv * 8192) + lane;
#pragma unroll
    for (int mm = 0; mm < 2; ++mm)
#pragma unroll
      for (int n = 0; n < 4; ++n) wb[(mm * 4 + n) * 64] = acc[GRP == 0 ? 2 + mm : mm][n];
  }
  wait_lgkm0();
  bar();
  f32x4 r[2][4];
  {
    LAS f32x4* rbuf = (LAS f32x4*)(lds + ((1 - GRP) * 4 + w4) * 8192) + lane;
#pragma unroll
    for (int mm = 0; mm < 2; ++mm)
#pragma unroll
      for (int n = 0; n < 4; ++n) r[mm][n] = acc[GRP == 0 ? mm : 2 + mm][n] + rbuf[(mm * 4 + n) * 64];
  }
  if (!FIN) {
    auto rsrc = __builtin_amdgcn_make_buffer_rsrc((void*)(A.part + (size_t)tile * 16384), 0, 65536, 0x00020000);
#pragma unroll
    for (int mm = 0; mm < 2; ++mm)
#pragma unroll
      for (int n = 0; n < 4; ++n)
        __builtin_amdgcn_raw_buffer_store_b128(__builtin_bit_cast(u32x4, r[mm][n]), rsrc, ((wv * 8 + mm * 4 + n) * 64 + lane) * 16, 0, 16);
    wait_vm<0>();
    bar();
    if (wv == 0 && lane == 0) __hip_atomic_store((GAS unsigned*)(A.flags + (FLAG_PART + tile) * FLAG_STRIDE), 1u, RLX_AGENT);
  } else {
#pragma unroll
    for (int mm = 0; mm < 2; ++mm)
#pragma unroll
      for (int n = 0; n < 4; ++n) {
        const f32x4 xv = {bflo(xr[mm][n][0]), bfhi(xr[mm][n][0]), bflo(xr[mm][n][1]), bfhi(xr[mm][n][1])};
        const f32x4 bv = {bflo(bb[n][0]), bfhi(bb[n][0]), bflo(bb[n][1]), bfhi(bb[n][1])};
        const f32x4 o = (r[mm][n] + pv[mm][n]) + (xv + bv);
        u32x2 ov = {pk2(o[0], o[1]), pk2(o[2], o[3])};
        *(u32x2*)(A.out + (size_t)(row0 + mm * 16) * D_ + col0 + n * 16) = ov;
      }
  }
  STAMP(3);
  STAMP(4);
}

__global__ __launch_bounds__(512, 2) void k2_gemm2(G2Args A) {
  extern __shared__ __attribute__((aligned(16))) char smem[];
  LAS char* lds = (LAS char*)smem;
  const int wid = __builtin_amdgcn_readfirstlane(threadIdx.x >> 6), lane = threadIdx.x & 63;
  const int bid = blockIdx.x, xcd = bid & 7, idx = bid >> 3;
  const int rb = (xcd & 3) * 4 + (idx & 3), ct = (xcd >> 2) * 4 + ((idx >> 2) & 3), fin = idx >> 4;
  const int w4 = wid & 3;
  if (fin) { if (wid < 4) g2_body<true, 0>(A, lds, rb, ct, w4, lane); else g2_body<true, 1>(A, lds, rb, ct, w4, lane); }
  else { if (wid < 4) g2_body<false, 0>(A, lds, rb, ct, w4, lane); else g2_body<false, 1>(A, lds, rb, ct, w4, lane); }
}

extern "C" void kernel_launch(void* const* d_in, const int* in_sizes, int n_in, void* d_out, int out_size, void* d_ws, size_t ws_size,
                              hipStream_t stream) {
  const u16* x = (const u16*)d_in[0];
  const u16* wqkv = (const u16*)d_in[1];
  const u16* wphi = (const u16*)d_in[2];
  const u16* wo = (const u16*)d_in[3];
  const u16* w1 = (const u16*)d_in[5];
  const u16* b1 = (const u16*)d_in[6];
  const u16* w2 = (const u16*)d_in[7];
  const u16* b2 = (const u16*)d_in[8];
  char* ws = (char*)d_ws;
  unsigned* flags = (unsigned*)(ws + WS_FLAGS);
  u16* xnf = (u16*)(ws + WS_XNF);
  u16* af = (u16*)(ws + WS_AF);
  u16* phiq = (u16*)(ws + WS_PHIQ);
  float* kvp = (float*)(ws + WS_KVP);
  float* part = (float*)(ws + WS_PART);
  (void)hipFuncSetAttribute((const void*)k1_gemm1, hipFuncAttributeMaxDynamicSharedMemorySize, G1_LDS);
  (void)hipFuncSetAttribute((const void*)k2_gemm2, hipFuncAttributeMaxDynamicSharedMemorySize, G2_LDS);
  k0_ln<<<256, 512, 0, stream>>>(x, xnf, flags, wqkv, w1);
  G1Args a1 = {xnf, wqkv, w1, b1, wphi, af, phiq, kvp, w2, wo DBG_ARG(128u)};
  k1_gemm1<<<256, 512, G1_LDS, stream>>>(a1);
  G2Args a2 = {af, w2, wo, phiq, kvp, part, x, b2, (u16*)d_out, flags DBG_ARG(129u)};
  k2_gemm2<<<256, 512, G2_LDS, stream>>>(a2);
}
```

```cpp
#include <hip/hip_runtime.h>
#include <hip/hip_bf16.h>
#include <stdint.h>

typedef unsigned short u16;
typedef short bf16x8 __attribute__((ext_vector_type(8)));
typedef short s16x4 __attribute__((ext_vector_type(4)));
typedef float f32x4 __attribute__((ext_vector_type(4)));
typedef float f32x2 __attribute__((ext_vector_type(2)));
typedef unsigned u32x4 __attribute__((ext_vector_type(4)));
typedef unsigned u32x2 __attribute__((ext_vector_type(2)));
typedef __bf16 bf16v2 __attribute__((ext_vector_type(2)));
#define LAS __attribute__((address_space(3)))
#define GAS __attribute__((address_space(1)))
#define DI __device__ __forceinline__
#define RLX_AGENT __ATOMIC_RELAXED, __HIP_MEMORY_SCOPE_AGENT

constexpr int S_ = 2048, D_ = 1024, F_ = 3584;
constexpr int SLOT = 33280;
constexpr int BOFF = 16384;
constexpr int PSTR = 1056;
constexpr int RING = 4 * SLOT;
constexpr int G1_WPHI = RING;
constexpr int G1_BIAS = RING + 4096;
constexpr int G1_DUMP = RING + 5120;
constexpr int G1_LDS = RING + 5120 + 256;
constexpr int G2_LDS = RING + 6144 + 256;
constexpr int NKB = 144;
constexpr int N1 = 30;
constexpr int N2 = 72 - N1;
constexpr int XSLOT = (N2 - 4) & 3;
constexpr int XPSTR = 1040;
constexpr int CBASE = XSLOT >= 2 ? 0 : 2 * SLOT;
static_assert(32 * XPSTR <= SLOT && N1 % 2 == 0, "");

constexpr size_t WS_FLAGS = 0;
constexpr size_t WS_XNF = 1u << 20;
constexpr size_t WS_AF = WS_XNF + (4u << 20);
constexpr size_t WS_PHIQ = WS_AF + (size_t)NKB * 128 * 1024;
constexpr size_t WS_KVP = WS_PHIQ + (8u << 20);
constexpr size_t WS_PART = WS_KVP + (3u << 20);
constexpr int FLAG_STRIDE = 64;
constexpr int FLAG_OCNT = 0;
constexpr int FLAG_PART = 8;
constexpr int FLAG_TMO = 8 + 128;
constexpr int FLAG_WORDS = (8 + 128 + 1) * FLAG_STRIDE;

template <int N> struct IC { static constexpr int value = N; };
#define ST_AUX 16
#define K0_PF 1
#define MFMA16(a, b, c) __builtin_amdgcn_mfma_f32_16x16x32_bf16((a), (b), (c), 0, 0, 0)
template <int N> DI void wait_vm() { asm volatile("s_waitcnt vmcnt(%0)" ::"i"(N) : "memory"); }
DI void wait_lgkm0() { asm volatile("" ::: "memory"); __builtin_amdgcn_s_waitcnt(0xC07F); asm volatile("" ::: "memory"); }
DI void bar() { __builtin_amdgcn_sched_barrier(0); __builtin_amdgcn_s_barrier(); __builtin_amdgcn_sched_barrier(0); }
DI void dma16(const void* g, unsigned l) {
  asm volatile("s_mov_b32 m0, %1\n\ts_nop 0\n\tglobal_load_lds_dwordx4 %0, off" ::"v"(g), "s"(l) : "memory");
}
DI void dma16s(const void* sb, unsigned voff, unsigned l) {
  asm volatile("s_mov_b32 m0, %2\n\ts_nop 0\n\tglobal_load_lds_dwordx4 %0, %1" ::"v"(voff), "s"(sb), "s"(l) : "memory");
}
DI void dma16s_x4k(const void* sb, unsigned voff, unsigned l) {
  asm volatile("s_mov_b32 m0, %2\n\ts_nop 0\n\tglobal_load_lds_dwordx4 %0, %1\n\tglobal_load_lds_dwordx4 %0, %1 offset:1024\n\t"
               "global_load_lds_dwordx4 %0, %1 offset:2048\n\tglobal_load_lds_dwordx4 %0, %1 offset:3072" ::"v"(voff), "s"(sb), "s"(l) : "memory");
}
DI void dma16s_x4k_sc1(const void* sb, unsigned voff, unsigned l) {
  asm volatile("s_mov_b32 m0, %2\n\ts_nop 0\n\tglobal_load_lds_dwordx4 %0, %1 sc1\n\tglobal_load_lds_dwordx4 %0, %1 offset:1024 sc1\n\t"
               "global_load_lds_dwordx4 %0, %1 offset:2048 sc1\n\tglobal_load_lds_dwordx4 %0, %1 offset:3072 sc1" ::"v"(voff), "s"(sb), "s"(l) : "memory");
}
DI void touch(const void* g, unsigned l) { asm volatile("s_mov_b32 m0, %1\n\ts_nop 0\n\tglobal_load_lds_dword %0, off" ::"v"(g), "s"(l) : "memory"); }
DI s16x4 trread(LAS char* p) { return __builtin_amdgcn_ds_read_tr16_b64_v4i16((LAS s16x4*)p); }
DI bf16x8 cat(s16x4 lo, s16x4 hi) { return __builtin_shufflevector(lo, hi, 0, 1, 2, 3, 4, 5, 6, 7); }
DI unsigned pk2(float lo, float hi) { f32x2 v = {lo, hi}; return __builtin_bit_cast(unsigned, __builtin_convertvector(v, bf16v2)); }
DI bf16x8 pack8(f32x4 a, f32x4 b) { u32x4 r = {pk2(a[0], a[1]), pk2(a[2], a[3]), pk2(b[0], b[1]), pk2(b[2], b[3])}; return __builtin_bit_cast(bf16x8, r); }
DI float bflo(unsigned u) { return __uint_as_float(u << 16); }
DI float bfhi(unsigned u) { return __uint_as_float(u & 0xffff0000u); }
DI float rbf(float x) { return bflo(pk2(x, x)); }
template <int CTRL> DI float dpp(float x) { return __builtin_bit_cast(float, __builtin_amdgcn_mov_dpp(__builtin_bit_cast(int, x), CTRL, 0xf, 0xf, true)); }
constexpr int XOR1 = 0xB1, XOR2 = 0x4E, HMIR = 0x141, MIR = 0x140, ROR8 = 0x128;
DI float row16_max(float x) { x = fmaxf(x, dpp<XOR1>(x)); x = fmaxf(x, dpp<XOR2>(x)); x = fmaxf(x, dpp<HMIR>(x)); return fmaxf(x, dpp<MIR>(x)); }
DI float row16_min(float x) { x = fminf(x, dpp<XOR1>(x)); x = fminf(x, dpp<XOR2>(x)); x = fminf(x, dpp<HMIR>(x)); return fminf(x, dpp<MIR>(x)); }
DI float row16_sum(float x) { x += dpp<XOR1>(x); x += dpp<XOR2>(x); x += dpp<HMIR>(x); return x + dpp<MIR>(x); }
DI float xg_max(float x) {
  auto s = __builtin_amdgcn_permlane16_swap(__float_as_uint(x), __float_as_uint(x), false, false);
  x = fmaxf(__uint_as_float(s[0]), __uint_as_float(s[1]));
  auto t = __builtin_amdgcn_permlane32_swap(__float_as_uint(x), __float_as_uint(x), false, false);
  return fmaxf(__uint_as_float(t[0]), __uint_as_float(t[1]));
}
DI float xg_min(float x) {
  auto s = __builtin_amdgcn_permlane16_swap(__float_as_uint(x), __float_as_uint(x), false, false);
  x = fminf(__uint_as_float(s[0]), __uint_as_float(s[1]));
  auto t = __builtin_amdgcn_permlane32_swap(__float_as_uint(x), __float_as_uint(x), false, false);
  return fminf(__uint_as_float(t[0]), __uint_as_float(t[1]));
}
DI float xg_sum(float x) {
  auto s = __builtin_amdgcn_permlane16_swap(__float_as_uint(x), __float_as_uint(x), false, false);
  x = __uint_as_float(s[0]) + __uint_as_float(s[1]);
  auto t = __builtin_amdgcn_permlane32_swap(__float_as_uint(x), __float_as_uint(x), false, false);
  return __uint_as_float(t[0]) + __uint_as_float(t[1]);
}
constexpr float LOG2E = 1.4426950408889634f;
#define SB() __builtin_amdgcn_sched_barrier(0)
DI u32x4 gelu8(f32x4 a, f32x4 b) {
  const float c1 = -2.0f * 0.7978845608028654f * LOG2E, c3 = c1 * 0.044715f;
  f32x4 ta = a * a, tb = b * b;
  ta = ta * c3 + c1; tb = tb * c3 + c1;
  ta = ta * a; tb = tb * b;
  SB();
#pragma unroll
  for (int r = 0; r < 4; ++r) { ta[r] = __builtin_amdgcn_exp2f(ta[r]); tb[r] = __builtin_amdgcn_exp2f(tb[r]); }
  SB();
  ta = ta + 1.0f; tb = tb + 1.0f;
  SB();
#pragma unroll
  for (int r = 0; r < 4; ++r) { ta[r] = __builtin_amdgcn_rcpf(ta[r]); tb[r] = __builtin_amdgcn_rcpf(tb[r]); }
  SB();
  return __builtin_bit_cast(u32x4, pack8(a * ta, b * tb));
}
DI void dual_softmax(f32x4 p0, f32x4 p1, f32x4& e0, f32x4& e1, f32x4& n0, f32x4& n1) {
  p0 = p0 * LOG2E; p1 = p1 * LOG2E;
  float mx = fmaxf(fmaxf(fmaxf(p0[0], p0[1]), fmaxf(p0[2], p0[3])), fmaxf(fmaxf(p1[0], p1[1]), fmaxf(p1[2], p1[3])));
  float mn = fminf(fminf(fminf(p0[0], p0[1]), fminf(p0[2], p0[3])), fminf(fminf(p1[0], p1[1]), fminf(p1[2], p1[3])));
  mx = xg_max(mx); mn = xg_min(mn);
  e0 = p0 - mx; e1 = p1 - mx; n0 = mn - p0; n1 = mn - p1;
#pragma unroll
  for (int r = 0; r < 4; ++r) {
    e0[r] = __builtin_amdgcn_exp2f(e0[r]); e1[r] = __builtin_amdgcn_exp2f(e1[r]);
    n0[r] = __builtin_amdgcn_exp2f(n0[r]); n1[r] = __builtin_amdgcn_exp2f(n1[r]);
  }
  const f32x4 se = e0 + e1, sn4 = n0 + n1;
  const float sp = xg_sum((se[0] + se[1]) + (se[2] + se[3]));
  const float sn = xg_sum((sn4[0] + sn4[1]) + (sn4[2] + sn4[3]));
  const float rp = __builtin_amdgcn_rcpf(sp), rn = __builtin_amdgcn_rcpf(sn);
  e0 = e0 * rp; e1 = e1 * rp; n0 = n0 * rn; n1 = n1 * rn;
}

#define STAMP(i) do { } while (0)
#define DBG_FIELD
#define DBG_ARG(x)
__global__ __launch_bounds__(512) void k0_ln(const u16* __restrict__ x, u16* __restrict__ xnf, unsigned* __restrict__ flags,
                                             const u16* __restrict__ wqkv, const u16* __restrict__ w1
                                             ) {
  __shared__ float red[2][8][8];
  __shared__ __attribute__((aligned(16))) unsigned dump[256];
  const int tid = threadIdx.x, w = tid >> 6, l = tid & 63, i = l & 7, c = l >> 3;
  const int row = blockIdx.x * 8 + i;
  const u32x4* src = (const u32x4*)(x + (size_t)row * D_) + 16 * w + c;
  const u32x4 v0 = src[0], v1 = src[8];
  if (blockIdx.x == 0) { for (int j = tid; j < FLAG_WORDS; j += 512) flags[j] = 0u; }
  {
    const unsigned dl = (unsigned)(size_t)(LAS unsigned*)dump;
    const int c0 = (blockIdx.x * 8 + __builtin_amdgcn_readfirstlane(w)) * 7;
#pragma unroll
    for (int t = 0; t < 7; ++t) {
      const int ck = c0 + t;
      if (ck < 13312) dma16s(ck < 6144 ? (const char*)wqkv + (size_t)ck * 1024 : (const char*)w1 + (size_t)(ck - 6144) * 1024, l * 16, dl);
    }
  }
  float f[16];
#pragma unroll
  for (int j = 0; j < 4; ++j) { f[2 * j] = bflo(v0[j]); f[2 * j + 1] = bfhi(v0[j]); f[8 + 2 * j] = bflo(v1[j]); f[9 + 2 * j] = bfhi(v1[j]); }
  float s = 0.f;
#pragma unroll
  for (int j = 0; j < 16; ++j) s += f[j];
  s += dpp<ROR8>(s); s = xg_sum(s);
  if (l < 8) red[0][w][i] = s;
  __syncthreads();
  float mean = 0.f;
#pragma unroll
  for (int j = 0; j < 8; ++j) mean += red[0][j][i];
  mean *= (1.0f / D_);
  float q = 0.f;
#pragma unroll
  for (int j = 0; j < 16; ++j) { float d = f[j] - mean; q = fmaf(d, d, q); }
  q += dpp<ROR8>(q); q = xg_sum(q);
  if (l < 8) red[1][w][i] = q;
  __syncthreads();
  float var = 0.f;
#pragma unroll
  for (int j = 0; j < 8; ++j) var += red[1][j][i];
  var *= (1.0f / D_);
  const float mb = rbf(mean);
  const float sd = rbf(sqrtf(rbf(rbf(var) + 1e-5f)));
  const float rs = 1.0f / sd;
  u32x4 o0, o1;
#pragma unroll
  for (int j = 0; j < 4; ++j) {
    o0[j] = pk2(rbf(f[2 * j] - mb) * rs, rbf(f[2 * j + 1] - mb) * rs);
    o1[j] = pk2(rbf(f[8 + 2 * j] - mb) * rs, rbf(f[9 + 2 * j] - mb) * rs);
  }
  const int mfrag = row >> 4, ri = row & 15;
  const int ch0 = 16 * w + c, ch1 = ch0 + 8;
  auto rsx = __builtin_amdgcn_make_buffer_rsrc((void*)xnf, 0, 4 << 20, 0x00020000);
  __builtin_amdgcn_raw_buffer_store_b128(o0, rsx, (((ch0 >> 2) * 128 + mfrag) * 64 + (ch0 & 3) * 16 + ri) * 16, 0, ST_AUX);
  __builtin_amdgcn_raw_buffer_store_b128(o1, rsx, (((ch1 >> 2) * 128 + mfrag) * 64 + (ch1 & 3) * 16 + ri) * 16, 0, ST_AUX);
}

struct G1Args {
  const u16* xnf; const u16* wqkv; const u16* w1; const u16* b1; const u16* wphi;
  u16* af; u16* phiq; float* kvp; const u16* w2; const u16* wo; DBG_FIELD
};

template <bool ISX, int GRP>
DI void g1_body(const G1Args& A, LAS char* lds, int rb, int j, int pos4, int wm, int lane) {
  constexpr int NF = GRP == 0 ? 7 : 6;
  [[maybe_unused]] const int wv_ = GRP * 4 + wm;
  STAMP(0);
  const int g = lane >> 4, q = (lane & 15) >> 2, p = lane & 3;
  const int hp = 2 * j + (pos4 >> 1);
  const int cp = pos4 == 0 ? 0 : pos4 == 3 ? 384 : pos4 == 1 ? 96 : 224;
  const int co = pos4 == 0 ? 64 : pos4 == 3 ? 368 : pos4 == 1 ? 80 : 352;
  const int cr = pos4 == 0 ? 0 : pos4 == 3 ? 320 : pos4 == 1 ? 64 : 192;
  constexpr int RBASE = ISX ? 256 : 128;
  const int colT = cp + 32 * (2 * GRP), colU = cp + 32 * (ISX ? GRP : 2 * GRP + 1);

  const unsigned lbase = (unsigned)(size_t)lds;
  const char* dsrc = nullptr; long dstep = 0; int dj = 0;
  const char* xsrc = (const char*)A.xnf + (size_t)(rb * 16 + 4 * wm) * 1024;
  const unsigned xoff = lane * 16;
  if (GRP == 0) {
    const int u0 = 8 * (wm & 1) + 4 * (wm >> 1), c = lane & 31, line = c >> 3, ch = c & 7, row = u0 + 16 * (lane >> 5);
    const bool fromq = ISX ? (line < 2) : (line < 1);
    const int ld = fromq ? 3 * D_ : F_;
    const u16* base = fromq ? (A.wqkv + (ISX ? (1 + line) * D_ : 0) + 64 * hp) : (A.w1 + 448 * j + cr + 64 * (line - (ISX ? 2 : 1)));
    dsrc = (const char*)(base + (size_t)row * ld + ch * 8); dstep = 64L * ld; dj = 2 * ld;
  }
  const unsigned ddst = lbase + (GRP == 0 ? BOFF + 4 * wm * PSTR : 4 * wm * 1024);
  auto issue = [&](int slot) {
    if (GRP == 0) {
#pragma unroll
      for (int jj = 0; jj < 4; ++jj) dma16(dsrc + jj * dj, ddst + slot * SLOT + jj * PSTR);
      dsrc += dstep;
    } else {
      dma16s_x4k(xsrc, xoff, ddst + slot * SLOT);
      xsrc += 128 * 1024;
    }
  };
  if (GRP == 0) dma16s(A.wphi + (size_t)hp * 2048 + wm * 512, xoff, lbase + G1_WPHI + wm * 1024);
  else if (wm == 0) { const int nch = ISX ? 16 : 24; dma16s(A.b1 + 448 * j + cr, (lane < nch ? lane : 0) * 16, lbase + G1_BIAS); }
  issue(0); issue(1); issue(2); issue(3);
  wait_vm<12>();
  bar();

  STAMP(1);
  f32x4 acc[4][NF];
  {
    LAS char* bp = lds + G1_BIAS + 8 * g;
#pragma unroll
    for (int f = 0; f < NF; ++f) {
      f32x4 b4 = {0.f, 0.f, 0.f, 0.f};
      if (f >= (ISX ? 4 : 2)) {
        const int col = f < 4 ? colT + 16 * (f - 2) : f < 6 ? colU + 16 * (f - 4) : co;
        const u32x2 bb = *(LAS u32x2*)(bp + (col - cr) * 2);
        b4 = (f32x4){bflo(bb[0]), bfhi(bb[0]), bflo(bb[1]), bfhi(bb[1])};
      }
#pragma unroll
      for (int m = 0; m < 4; ++m) acc[m][f] = b4;
    }
  }
  const int lb = BOFF + PSTR * (q + 4 * (g & 1)) + 512 * (g >> 1) + 8 * p;
  LAS char* pA = lds + wm * 4096 + lane * 16;
  LAS char* pS = lds + lb + 64 * GRP;
  LAS char* pT = lds + lb + (ISX ? 128 + 64 * GRP : RBASE + (colT - cr) * 2);
  LAS char* pU = lds + lb + RBASE + (colU - cr) * 2;
  LAS char* pO = lds + lb + RBASE + (co - cr) * 2;
  auto rdB = [&](int so, int f) -> bf16x8 {
    LAS char* pp = (f < 2 ? pS : f < 4 ? pT : f < 6 ? pU : pO) + so + (f < 6 ? (f & 1) * 32 : 0);
    return cat(trread(pp), trread(pp + 8 * PSTR));
  };
  bf16x8 a0[4], b0[NF], a1[4], b1[NF];
#pragma unroll
  for (int f = 0; f < NF; ++f) b0[f] = rdB(0, f);
#pragma unroll
  for (int m = 0; m < 4; ++m) a0[m] = *(LAS bf16x8*)(pA + m * 1024);
  wait_lgkm0();
  wait_vm<8>();
  bar();
  auto step = [&](int s, bf16x8 (&a)[4], bf16x8 (&b)[NF], bf16x8 (&an)[4], bf16x8 (&bn)[NF], auto tag) {
    constexpr int T = decltype(tag)::value;
    const int so = ((s + 1) & 3) * SLOT;
    auto mm = [&](int i) {
      const int f = i >> 2, m = i & 3;
      if (ISX && (f == 2 || f == 3)) acc[m][f] = MFMA16(a[m], b[f], acc[m][f]);
      else acc[m][f] = MFMA16(b[f], a[m], acc[m][f]);
    };
    s16x4 lo[NF];
    auto rd = [&](int i) {
      if (i < 4) an[i] = *(LAS bf16x8*)(pA + so + i * 1024);
      else {
        const int f = (i - 4) >> 1;
        LAS char* pp = (f < 2 ? pS : f < 4 ? pT : f < 6 ? pU : pO) + so + (f < 6 ? (f & 1) * 32 : 0);
        if (((i - 4) & 1) == 0) lo[f] = trread(pp); else bn[f] = cat(lo[f], trread(pp + 8 * PSTR));
      }
    };
    if (GRP == 1) {
      if (T == 4) issue(s & 3);
      if (T > 0) {
#pragma unroll
        for (int i = 0; i < 4 + 2 * NF; ++i) rd(i);
      }
      SB();
#pragma unroll
      for (int i = 0; i < 4 * NF; ++i) mm(i);
      SB();
    } else {
#pragma unroll
      for (int i = 0; i < 4 * NF; ++i) {
        mm(i);
        if (T > 0 && i < 4 + 2 * NF) rd(i);
        SB();
      }
      if (T == 4) issue(s & 3);
    }
    if (T > 0) {
      wait_lgkm0();
      if (T > 1) wait_vm<(T == 4 ? 8 : T == 3 ? 4 : 0)>();
      bar();
    }
  };
#pragma unroll 1
  for (int s = 0; s < 28; s += 2) { step(s, a0, b0, a1, b1, IC<4>{}); step(s + 1, a1, b1, a0, b0, IC<4>{}); }
  step(28, a0, b0, a1, b1, IC<3>{}); step(29, a1, b1, a0, b0, IC<2>{}); step(30, a0, b0, a1, b1, IC<1>{}); step(31, a1, b1, a0, b0, IC<0>{});

  STAMP(2);
  if (!ISX) {
    const int yw = ((rb * 16 + hp) * 8 + GRP * 4 + wm) * 72;
#pragma unroll
    for (int t = 0; t < 2; ++t) {
      const int ln = yw + t * 36 + (lane < 36 ? lane : 0);
      touch(ln < 57344 ? (const char*)A.w2 + (size_t)ln * 128 : (const char*)A.wo + (size_t)(ln - 57344) * 128, lbase + G1_DUMP);
    }
  }
  const int mf0 = rb * 16 + wm * 4;
  {
    auto rs = __builtin_amdgcn_make_buffer_rsrc((void*)A.af, 0, NKB * 128 * 1024, 0x00020000);
    const int o0 = (mf0 * 64 + lane) * 16;
    if (!ISX) {
      const int kb = (448 * j + colT) >> 5;
#pragma unroll
      for (int m = 0; m < 4; ++m) __builtin_amdgcn_raw_buffer_store_b128(gelu8(acc[m][2], acc[m][3]), rs, o0 + kb * 131072 + m * 1024, 0, ST_AUX);
    }
    {
      const int kb = (448 * j + colU) >> 5;
#pragma unroll
      for (int m = 0; m < 4; ++m) __builtin_amdgcn_raw_buffer_store_b128(gelu8(acc[m][4], acc[m][5]), rs, o0 + kb * 131072 + m * 1024, 0, ST_AUX);
    }
    if (GRP == 0) {
      const int kb = (448 * j + co) >> 5, n = (co >> 4) & 1;
#pragma unroll
      for (int m = 0; m < 4; m += 2) {
        const u32x4 gv = gelu8(acc[m][NF - 1], acc[m + 1][NF - 1]);
        __builtin_amdgcn_raw_buffer_store_b64((u32x2){gv[0], gv[1]}, rs, o0 + kb * 131072 + n * 8 + m * 1024, 0, ST_AUX);
        __builtin_amdgcn_raw_buffer_store_b64((u32x2){gv[2], gv[3]}, rs, o0 + kb * 131072 + n * 8 + (m + 1) * 1024, 0, ST_AUX);
      }
    }
  }
  const int head = 2 * hp + GRP;
  if (!ISX) {
    auto rs = __builtin_amdgcn_make_buffer_rsrc((void*)A.phiq, 0, 4 << 20, 0x00020000);
#pragma unroll
    for (int m = 0; m < 4; ++m)
      __builtin_amdgcn_raw_buffer_store_b128(__builtin_bit_cast(u32x4, pack8(acc[m][0], acc[m][1])), rs, lane * 16 + ((mf0 + m) * 32 + head) * 1024, 0, ST_AUX);
  } else {
    bf16x8 wp[2];
    {
      LAS char* pw = lds + G1_WPHI + GRP * 2048 + (4 * g + q) * 64 + 8 * p;
      wp[0] = cat(trread(pw), trread(pw + 1024));
      wp[1] = cat(trread(pw + 32), trread(pw + 1024 + 32));
    }
    const f32x4 z4 = {0.f, 0.f, 0.f, 0.f};
    f32x4 p0[4], p1[4];
#pragma unroll
    for (int m = 0; m < 4; ++m) {
      const bf16x8 qb = pack8(acc[m][0], acc[m][1]);
      p0[m] = MFMA16(wp[0], qb, z4); p1[m] = MFMA16(wp[1], qb, z4);
    }
    constexpr int TS = 144;
    LAS char* tb = lds + (GRP * 4 + wm) * 12288;
    {
      LAS char* wrp = tb + (lane & 15) * TS + 8 * g;
#pragma unroll
      for (int m = 0; m < 4; ++m) {
        f32x4 e0, e1, n0, n1;
        dual_softmax(p0[m], p1[m], e0, e1, n0, n1);
        const u32x4 pe = __builtin_bit_cast(u32x4, pack8(e0, e1)), pn = __builtin_bit_cast(u32x4, pack8(n0, n1));
        *(LAS u32x2*)(wrp + m * 16 * TS) = (u32x2){pe[0], pe[1]};
        *(LAS u32x2*)(wrp + m * 16 * TS + 32) = (u32x2){pe[2], pe[3]};
        *(LAS u32x2*)(wrp + m * 16 * TS + 64) = (u32x2){pn[0], pn[1]};
        *(LAS u32x2*)(wrp + m * 16 * TS + 96) = (u32x2){pn[2], pn[3]};
      }
    }
    wait_lgkm0();
    f32x4 kv[4][3];
    const bf16x8 ones = {0x3F80, 0x3F80, 0x3F80, 0x3F80, 0x3F80, 0x3F80, 0x3F80, 0x3F80};
    LAS char* rdp = tb + (4 * g + q) * TS + 8 * p;
    bf16x8 pa[2][4];
#pragma unroll
    for (int u = 0; u < 2; ++u)
#pragma unroll
      for (int fq = 0; fq < 4; ++fq) pa[u][fq] = cat(trread(rdp + (32 * u) * TS + 32 * fq), trread(rdp + (32 * u + 16) * TS + 32 * fq));
#pragma unroll
    for (int u = 0; u < 2; ++u) {
      const bf16x8 v0 = pack8(acc[2 * u][2], acc[2 * u + 1][2]), v1 = pack8(acc[2 * u][3], acc[2 * u + 1][3]);
#pragma unroll
      for (int fq = 0; fq < 4; ++fq) {
        kv[fq][0] = MFMA16(pa[u][fq], v0, u == 0 ? z4 : kv[fq][0]);
        kv[fq][1] = MFMA16(pa[u][fq], v1, u == 0 ? z4 : kv[fq][1]);
        kv[fq][2] = MFMA16(pa[u][fq], ones, u == 0 ? z4 : kv[fq][2]);
      }
    }
    wait_lgkm0();
    LAS f32x4* rbuf = (LAS f32x4*)tb + lane;
#pragma unroll
    for (int fq = 0; fq < 4; ++fq)
#pragma unroll
      for (int dc = 0; dc < 3; ++dc) rbuf[(fq * 3 + dc) * 64] = kv[fq][dc];
    wait_lgkm0();
    bar();
    LAS f32x4* sbuf = (LAS f32x4*)(lds + GRP * 4 * 12288) + lane;
    auto rs = __builtin_amdgcn_make_buffer_rsrc((void*)A.kvp, 0, 3 << 20, 0x00020000);
#pragma unroll
    for (int t = 0; t < 3; ++t) {
      const int qd = 3 * wm + t;
      const f32x4 sv = (sbuf[qd * 64] + sbuf[768 + qd * 64]) + (sbuf[1536 + qd * 64] + sbuf[2304 + qd * 64]);
      __builtin_amdgcn_raw_buffer_store_b128(__builtin_bit_cast(u32x4, sv), rs, lane * 16 + ((rb * 32 + head) * 12 + qd) * 1024, 0, ST_AUX);
    }
  }
  STAMP(3);
  STAMP(4);
}

__global__ __launch_bounds__(512, 2) void k1_gemm1(G1Args A) {
  extern __shared__ __attribute__((aligned(16))) char smem[];
  LAS char* lds = (LAS char*)smem;
  const int wid = __builtin_amdgcn_readfirstlane(threadIdx.x >> 6), lane = threadIdx.x & 63;
  const int bid = blockIdx.x, xcd = bid & 7, idx = bid >> 3;
  const int rb = (xcd & 1) * 4 + (idx & 3), ct = (xcd >> 1) * 8 + (idx >> 2);
  const int j = ct >> 2, pos4 = ct & 3, wm = wid & 3;
  const bool isx = (pos4 == 0) || (pos4 == 3);
  if (isx) { if (wid < 4) g1_body<true, 0>(A, lds, rb, j, pos4, wm, lane); else g1_body<true, 1>(A, lds, rb, j, pos4, wm, lane); }
  else { if (wid < 4) g1_body<false, 0>(A, lds, rb, j, pos4, wm, lane); else g1_body<false, 1>(A, lds, rb, j, pos4, wm, lane); }
}

struct G2Args {
  u16* af; const u16* w2; const u16* wo; const u16* phiq; const float* kvp; float* part;
  const u16* x; const u16* b2; u16* out; unsigned* flags; const u16* wphi; DBG_FIELD
};

DI unsigned peek_flag(unsigned* flag) { return __hip_atomic_load((GAS unsigned*)flag, RLX_AGENT); }
DI void await_flag(unsigned seen, unsigned* flag, unsigned want, unsigned* tmo) {
  unsigned spins = 0;
  while ((unsigned)__builtin_amdgcn_readfirstlane(seen) < want) {
    __builtin_amdgcn_s_sleep(1);
    if (++spins > (1u << 22)) { __hip_atomic_store((GAS unsigned*)tmo, 1u, RLX_AGENT); break; }
    seen = peek_flag(flag);
  }
  __builtin_amdgcn_fence(__ATOMIC_ACQUIRE, "workgroup");
}

template <bool FIN, int GRP>
DI void g2_body(const G2Args& A, LAS char* lds, int rb, int ct, int w4, int lane) {
  constexpr int NS = FIN ? N2 : N1;
  constexpr int KB0 = FIN ? 2 * N1 : 0;
  [[maybe_unused]] const int wv_ = GRP * 4 + w4;
  STAMP(0);
  const int g = lane >> 4, q = (lane & 15) >> 2, p = lane & 3;
  const int wm = w4 & 1, wn = w4 >> 1;
  const int tile = rb * 8 + ct, wv = GRP * 4 + w4;
  const int col0 = ct * 128 + wn * 64 + 4 * g;
  u32x2 bb[4];
  if (FIN && GRP == 0) {
#pragma unroll
    for (int n = 0; n < 4; ++n) bb[n] = *(const u32x2*)(A.b2 + col0 + n * 16);
  }
  if (!FIN) {
    const int head = tile & 31, rq = tile >> 5;
    LAS char* kvl = lds + RING;
    u32x4 qf[4];
    {
      const u32x4* q4 = (const u32x4*)A.phiq + ((size_t)(rq * 32 + wv * 4) * 32 + head) * 64 + lane;
#pragma unroll
      for (int m = 0; m < 4; ++m) qf[m] = q4[m * 2048];
    }
    u16 wraw[2][8];
    {
      const u16* wsrc = A.wphi + head * 1024 + 4 * g * 32 + (lane & 15);
#pragma unroll
      for (int fc = 0; fc < 2; ++fc)
#pragma unroll
        for (int e = 0; e < 8; ++e) wraw[fc][e] = wsrc[((e & 3) + 16 * (e >> 2)) * 32 + 16 * fc];
    }
    f32x4 kp[8][2];
    const int kh = wv < 4 ? wv >> 1 : (lane >> 2) & 1, dc = wv < 4 ? wv & 1 : 2, sl = wv < 4 ? lane : (lane & 3) * 16;
    const bool kact = wv < 4 || (wv == 4 && lane < 8);
    if (kact) {
      const f32x4* src = (const f32x4*)A.kvp + ((size_t)head * 12 + (2 * kh) * 3 + dc) * 64 + sl;
#pragma unroll
      for (int r8 = 0; r8 < 8; ++r8) { kp[r8][0] = src[(size_t)r8 * 32 * 768]; kp[r8][1] = src[(size_t)r8 * 32 * 768 + 192]; }
    }
    bar();
    const f32x4 z4 = {0.f, 0.f, 0.f, 0.f};
    bf16x8 pq[4][2];
    {
      bf16x8 wp[2];
#pragma unroll
      for (int fc = 0; fc < 2; ++fc)
#pragma unroll
        for (int e = 0; e < 8; ++e) wp[fc][e] = (short)wraw[fc][e];
#pragma unroll
      for (int m = 0; m < 4; ++m) {
        const bf16x8 qb = __builtin_bit_cast(bf16x8, qf[m]);
        f32x4 e0, e1, n0, n1;
        dual_softmax(MFMA16(wp[0], qb, z4), MFMA16(wp[1], qb, z4), e0, e1, n0, n1);
        pq[m][0] = pack8(e0, e1); pq[m][1] = pack8(n0, n1);
      }
    }
    if (kact) {
      f32x4 s0 = kp[0][0], s1 = kp[0][1];
#pragma unroll
      for (int r8 = 1; r8 < 8; ++r8) { s0 += kp[r8][0]; s1 += kp[r8][1]; }
      *(LAS bf16x8*)(kvl + (kh * 3 + dc) * 1024 + (wv < 4 ? lane : lane & 3) * 16) = pack8(s0, s1);
    }
    STAMP(13);
    wait_lgkm0();
    bar();
    STAMP(14);
    bf16x8 ka[2][3];
#pragma unroll
    for (int k2 = 0; k2 < 2; ++k2)
#pragma unroll
      for (int d2 = 0; d2 < 3; ++d2) ka[k2][d2] = *(LAS bf16x8*)(kvl + (k2 * 3 + d2) * 1024 + (d2 < 2 ? lane : g) * 16);
    auto rsrc = __builtin_amdgcn_make_buffer_rsrc((void*)(A.af + (size_t)(112 + head) * 128 * 512), 0, 128 * 1024, 0x00020000);
#pragma unroll
    for (int m = 0; m < 4; ++m) {
      const bf16x8 b0 = pq[m][0], b1 = pq[m][1];
      f32x4 n0 = MFMA16(ka[0][0], b0, z4); n0 = MFMA16(ka[1][0], b1, n0);
      f32x4 n1 = MFMA16(ka[0][1], b0, z4); n1 = MFMA16(ka[1][1], b1, n1);
      f32x4 dn = MFMA16(ka[0][2], b0, z4); dn = MFMA16(ka[1][2], b1, dn);
      const float rd = __builtin_amdgcn_rcpf(rbf(rbf(dn[0]) + 1e-6f));
      f32x4 o0, o1;
#pragma unroll
      for (int r = 0; r < 4; ++r) { o0[r] = rbf(n0[r]) * rd; o1[r] = rbf(n1[r]) * rd; }
      const int mf = rq * 32 + wv * 4 + m;
      __builtin_amdgcn_raw_buffer_store_b128(__builtin_bit_cast(u32x4, pack8(o0, o1)), rsrc, (mf * 64 + lane) * 16, 0, 16);
    }
    STAMP(15);
  }
  bar();

  STAMP(1);
  f32x4 acc[4][4];
#pragma unroll
  for (int m = 0; m < 4; ++m)
#pragma unroll
    for (int n = 0; n < 4; ++n) {
      if (FIN && GRP == 0) acc[m][n] = (f32x4){bflo(bb[n][0]), bfhi(bb[n][0]), bflo(bb[n][1]), bfhi(bb[n][1])};
      else acc[m][n] = (f32x4){0.f, 0.f, 0.f, 0.f};
    }
  LAS char* pA = lds + GRP * 8192 + wm * 4096 + lane * 16;
  LAS char* pB = lds + BOFF + GRP * 8 * PSTR + PSTR * (4 * (g & 1) + q) + 256 * (g >> 1) + 8 * p + 128 * wn;
  constexpr int SO_FIRST = (112 - KB0) / 2;
  bf16x8 a0[4], b0[4], a1[4], b1[4];
#pragma unroll
  for (int n = 0; n < 4; ++n) b0[n] = cat(trread(pB + n * 32), trread(pB + n * 32 + 512));
#pragma unroll
  for (int m = 0; m < 4; ++m) a0[m] = *(LAS bf16x8*)(pA + m * 1024);
  wait_lgkm0();
  bar();

  auto step = [&](int st, bf16x8 (&a)[4], bf16x8 (&b)[4], bf16x8 (&an)[4], bf16x8 (&bn)[4], auto tag) {
    constexpr int T = decltype(tag)::value;
    const int so = ((st + 1) & 3) * SLOT;
    s16x4 lo[4];
    auto rd = [&](int i) {
      if (i < 4) an[i] = *(LAS bf16x8*)(pA + so + i * 1024);
      else {
        const int n = (i - 4) >> 1;
        LAS char* pp = pB + so + n * 32;
        if (((i - 4) & 1) == 0) lo[n] = trread(pp); else bn[n] = cat(lo[n], trread(pp + 512));
      }
    };
    if (GRP == 1) {
      if (T > 0) {
#pragma unroll
        for (int i = 0; i < 12; ++i) rd(i);
      }
      SB();
#pragma unroll
      for (int i = 0; i < 16; ++i) acc[i & 3][i >> 2] = MFMA16(b[i >> 2], a[i & 3], acc[i & 3][i >> 2]);
      SB();
    } else {
#pragma unroll
      for (int i = 0; i < 16; ++i) {
        acc[i & 3][i >> 2] = MFMA16(b[i >> 2], a[i & 3], acc[i & 3][i >> 2]);
        if (T > 0 && i < 12) rd(i);
        SB();
      }
    }
    if (T > 0) {
      wait_lgkm0();
      bar();
    }
  };
  unsigned* const ocnt = A.flags + (FLAG_OCNT + (rb >> 2)) * FLAG_STRIDE;
  unsigned* const pflag = A.flags + (FLAG_PART + tile) * FLAG_STRIDE;
  unsigned* const tmo = A.flags + FLAG_TMO * FLAG_STRIDE;
  unsigned seen = 0;
#pragma unroll 1
  for (int st = 0; st < NS - 4; st += 2) {
    if (wv == 0) {
      if (!FIN && st == 2 && lane == 0)
        __hip_atomic_fetch_add((GAS unsigned*)(A.flags + (FLAG_OCNT + (tile >> 5)) * FLAG_STRIDE), 1u, RLX_AGENT);
      if (FIN) {
        constexpr int S0 = (SO_FIRST - 8) & ~1;
        if (st == S0) seen = peek_flag(ocnt);
        if (st == S0 + 2) await_flag(seen, ocnt, 32u, tmo);
      }
    }
    step(st, a0, b0, a1, b1, IC<4>{});
    if (!FIN && st == 0) wait_vm<0>();
    step(st + 1, a1, b1, a0, b0, IC<4>{});
  }
  if (FIN && wv == 0) seen = peek_flag(pflag);
  step(NS - 4, a0, b0, a1, b1, IC<3>{}); step(NS - 3, a1, b1, a0, b0, IC<2>{});
  if (FIN && wv == 0) await_flag(seen, pflag, 1u, tmo);
  step(NS - 2, a0, b0, a1, b1, IC<1>{});
  f32x4 pv[2][4];
  if (FIN) {
    auto rsrc = __builtin_amdgcn_make_buffer_rsrc((void*)(A.part + (size_t)tile * 16384), 0, 65536, 0x00020000);
#pragma unroll
    for (int mm = 0; mm < 2; ++mm)
#pragma unroll
      for (int n = 0; n < 4; ++n)
        pv[mm][n] = __builtin_bit_cast(f32x4, __builtin_amdgcn_raw_buffer_load_b128(rsrc, ((wv * 8 + mm * 4 + n) * 64 + lane) * 16, 0, 16));
    SB();
  }
  step(NS - 1, a1, b1, a0, b0, IC<0>{});

  STAMP(2);
  {
    LAS f32x4* wb = (LAS f32x4*)(lds + CBASE + wv * 8192) + lane;
#pragma unroll
    for (int mm = 0; mm < 2; ++mm)
#pragma unroll
      for (int n = 0; n < 4; ++n) wb[(mm * 4 + n) * 64] = acc[GRP == 0 ? 2 + mm : mm][n];
  }
  wait_lgkm0();
  bar();
  f32x4 r[2][4];
  {
    LAS f32x4* rbuf = (LAS f32x4*)(lds + CBASE + ((1 - GRP) * 4 + w4) * 8192) + lane;
#pragma unroll
    for (int mm = 0; mm < 2; ++mm)
#pragma unroll
      for (int n = 0; n < 4; ++n) r[mm][n] = acc[GRP == 0 ? mm : 2 + mm][n] + rbuf[(mm * 4 + n) * 64];
  }
  if (!FIN) {
    auto rsrc = __builtin_amdgcn_make_buffer_rsrc((void*)(A.part + (size_t)tile * 16384), 0, 65536, 0x00020000);
#pragma unroll
    for (int mm = 0; mm < 2; ++mm)
#pragma unroll
      for (int n = 0; n < 4; ++n)
        __builtin_amdgcn_raw_buffer_store_b128(__builtin_bit_cast(u32x4, r[mm][n]), rsrc, ((wv * 8 + mm * 4 + n) * 64 + lane) * 16, 0, 16);
    wait_vm<0>();
    bar();
    if (wv == 0 && lane == 0) __hip_atomic_store((GAS unsigned*)(A.flags + (FLAG_PART + tile) * FLAG_STRIDE), 1u, RLX_AGENT);
  } else {
    int lz = lane; asm volatile("" : "+v"(lz));
    const int rl = wm * 64 + GRP * 32 + (lz & 15), cl = wn * 64 + 4 * (lz >> 4);
    LAS char* xl = lds + XSLOT * SLOT + (rl >> 2) * XPSTR + (rl & 3) * 256 + cl * 2;
    u16* op = A.out + (size_t)(rb * 128 + rl) * D_ + ct * 128 + cl;
#pragma unroll
    for (int mm = 0; mm < 2; ++mm)
#pragma unroll
      for (int n = 0; n < 4; ++n) {
        const u32x2 xr = *(LAS u32x2*)(xl + mm * 4 * XPSTR + n * 32);
        const f32x4 xv = {bflo(xr[0]), bfhi(xr[0]), bflo(xr[1]), bfhi(xr[1])};
        const f32x4 o = (r[mm][n] + pv[mm][n]) + xv;
        u32x2 ov = {pk2(o[0], o[1]), pk2(o[2], o[3])};
        *(u32x2*)(op + (size_t)mm * 16 * D_ + n * 16) = ov;
      }
  }
  STAMP(3);
  STAMP(4);
}

template <bool FIN>
DI void g2_loader(const G2Args& A, LAS char* lds, int rb, int ct, int w4, int lane) {
  constexpr int NS = FIN ? N2 : N1;
  constexpr int KB0 = FIN ? 2 * N1 : 0;
  const int kbs = w4 >> 1, u0 = 4 * (w4 & 1);
  const unsigned lbase = (unsigned)(size_t)lds;
  const char* asrc = (const char*)A.af + ((size_t)kbs * 128 + rb * 8 + u0) * 1024;
  const unsigned aoff = lane * 16;
  const unsigned boff = (unsigned)(((kbs * 32 + u0 + 8 * (lane >> 4)) * D_ + ct * 128 + (lane & 15) * 8) * 2);
  const unsigned adst = lbase + kbs * 8192 + u0 * 1024, bdst = lbase + BOFF + kbs * 8 * PSTR + u0 * PSTR;
  auto issue = [&](int st) {
    const int kb = KB0 + 2 * st, slot = (st & 3) * SLOT;
    const char* wb = kb < 112 ? (const char*)A.w2 + (size_t)kb * 32 * D_ * 2 : (const char*)A.wo + (size_t)(kb - 112) * 32 * D_ * 2;
    if (FIN && kb >= 112) dma16s_x4k_sc1(asrc + (size_t)kb * 128 * 1024, aoff, adst + slot);
    else dma16s_x4k(asrc + (size_t)kb * 128 * 1024, aoff, adst + slot);
#pragma unroll
    for (int jj = 0; jj < 4; ++jj) dma16s(wb + jj * (D_ * 2), boff, bdst + slot + jj * PSTR);
  };
  if (!FIN) bar();
  issue(0); issue(1); issue(2); issue(3);
  if (!FIN) bar();
  wait_vm<24>();
  bar();
  wait_vm<16>();
  bar();
#pragma unroll 1
  for (int st = 0; st < NS - 4; ++st) { issue(st + 4); wait_vm<16>(); bar(); }
  if (FIN) {
    const char* xs = (const char*)A.x + ((size_t)(rb * 128 + w4 * 32) * D_ + ct * 128) * 2;
    const unsigned xo = (unsigned)((lane >> 4) * (D_ * 2) + (lane & 15) * 16);
#pragma unroll
    for (int jj = 0; jj < 8; ++jj) dma16s(xs + jj * 4 * (D_ * 2), xo, lbase + XSLOT * SLOT + (w4 * 8 + jj) * XPSTR);
    wait_vm<16>(); bar();
    wait_vm<8>(); bar();
    wait_vm<0>(); bar();
  } else {
    wait_vm<8>(); bar();
    wait_vm<0>(); bar();
  }
}

__global__ __launch_bounds__(768) void k2_gemm2(G2Args A) {
  extern __shared__ __attribute__((aligned(16))) char smem[];
  LAS char* lds = (LAS char*)smem;
  const int wid = __builtin_amdgcn_readfirstlane(threadIdx.x >> 6), lane = threadIdx.x & 63;
  const int bid = blockIdx.x, xcd = bid & 7, idx = bid >> 3;
  const int rb = (xcd & 3) * 4 + (idx & 3), ct = (xcd >> 2) * 4 + ((idx >> 2) & 3), fin = idx >> 4;
  const int w4 = wid & 3;
  if (wid >= 8) { if (fin) g2_loader<true>(A, lds, rb, ct, w4, lane); else g2_loader<false>(A, lds, rb, ct, w4, lane); return; }
  if (fin) { if (wid < 4) g2_body<true, 0>(A, lds, rb, ct, w4, lane); else g2_body<true, 1>(A, lds, rb, ct, w4, lane); }
  else { if (wid < 4) g2_body<false, 0>(A, lds, rb, ct, w4, lane); else g2_body<false, 1>(A, lds, rb, ct, w4, lane); }
}

extern "C" void kernel_launch(void* const* d_in, const int* in_sizes, int n_in, void* d_out, int out_size, void* d_ws, size_t ws_size,
                              hipStream_t stream) {
  const u16* x = (const u16*)d_in[0];
  const u16* wqkv = (const u16*)d_in[1];
  const u16* wphi = (const u16*)d_in[2];
  const u16* wo = (const u16*)d_in[3];
  const u16* w1 = (const u16*)d_in[5];
  const u16* b1 = (const u16*)d_in[6];
  const u16* w2 = (const u16*)d_in[7];
  const u16* b2 = (const u16*)d_in[8];
  char* ws = (char*)d_ws;
  unsigned* flags = (unsigned*)(ws + WS_FLAGS);
  u16* xnf = (u16*)(ws + WS_XNF);
  u16* af = (u16*)(ws + WS_AF);
  u16* phiq = (u16*)(ws + WS_PHIQ);
  float* kvp = (float*)(ws + WS_KVP);
  float* part = (float*)(ws + WS_PART);
  (void)hipFuncSetAttribute((const void*)k1_gemm1, hipFuncAttributeMaxDynamicSharedMemorySize, G1_LDS);
  (void)hipFuncSetAttribute((const void*)k2_gemm2, hipFuncAttributeMaxDynamicSharedMemorySize, G2_LDS);
  k0_ln<<<256, 512, 0, stream>>>(x, xnf, flags, wqkv, w1 DBG_ARG(130u));
  G1Args a1 = {xnf, wqkv, w1, b1, wphi, af, phiq, kvp, w2, wo DBG_ARG(128u)};
  k1_gemm1<<<256, 512, G1_LDS, stream>>>(a1);
  G2Args a2 = {af, w2, wo, phiq, kvp, part, x, b2, (u16*)d_out, flags, wphi DBG_ARG(129u)};
  k2_gemm2<<<256, 768, G2_LDS, stream>>>(a2);
}
```

```cpp
#include <hip/hip_runtime.h>
#include <hip/hip_bf16.h>
#include <stdint.h>

typedef unsigned short u16;
typedef short bf16x8 __attribute__((ext_vector_type(8)));
typedef short s16x4 __attribute__((ext_vector_type(4)));
typedef float f32x4 __attribute__((ext_vector_type(4)));
typedef float f32x2 __attribute__((ext_vector_type(2)));
typedef unsigned u32x4 __attribute__((ext_vector_type(4)));
typedef unsigned u32x2 __attribute__((ext_vector_type(2)));
typedef __bf16 bf16v2 __attribute__((ext_vector_type(2)));
#define LAS __attribute__((address_space(3)))
#define GAS __attribute__((address_space(1)))
#define DI __device__ __forceinline__
#define RLX_AGENT __ATOMIC_RELAXED, __HIP_MEMORY_SCOPE_AGENT

constexpr int S_ = 2048, D_ = 1024, F_ = 3584;
constexpr int SLOT = 33280;
constexpr int BOFF = 16384;
constexpr int PSTR = 1056;
constexpr int RING = 4 * SLOT;
constexpr int G1_WPHI = RING;
constexpr int G1_BIAS = RING + 4096;
constexpr int G1_DUMP = RING + 5120;
constexpr int G1_LDS = RING + 5120 + 256;
constexpr int G2_LDS = RING + 6144 + 256;
constexpr int NKB = 144;
constexpr int N1 = 30;
constexpr int N2 = 72 - N1;
constexpr int XSLOT = (N2 - 4) & 3;
constexpr int XPSTR = 1040;
constexpr int CBASE = XSLOT >= 2 ? 0 : 2 * SLOT;
static_assert(32 * XPSTR <= SLOT && N1 % 2 == 0, "");

constexpr size_t WS_FLAGS = 0;
constexpr size_t WS_XNF = 1u << 20;
constexpr size_t WS_AF = WS_XNF + (4u << 20);
constexpr size_t WS_PHIQ = WS_AF + (size_t)NKB * 128 * 1024;
constexpr size_t WS_KVP = WS_PHIQ + (8u << 20);
constexpr size_t WS_PART = WS_KVP + (3u << 20);
constexpr int FLAG_STRIDE = 64;
constexpr int FLAG_OCNT = 0;
constexpr int FLAG_PART = 8;
constexpr int FLAG_TMO = 8 + 128;
constexpr int FLAG_WORDS = (8 + 128 + 1) * FLAG_STRIDE;

template <int N> struct IC { static constexpr int value = N; };
#define ST_AUX 16
#define K0_PF 0
#define MFMA16(a, b, c) __builtin_amdgcn_mfma_f32_16x16x32_bf16((a), (b), (c), 0, 0, 0)
template <int N> DI void wait_vm() { asm volatile("s_waitcnt vmcnt(%0)" ::"i"(N) : "memory"); }
DI void wait_lgkm0() { asm volatile("" ::: "memory"); __builtin_amdgcn_s_waitcnt(0xC07F); asm volatile("" ::: "memory"); }
DI void bar() { __builtin_amdgcn_sched_barrier(0); __builtin_amdgcn_s_barrier(); __builtin_amdgcn_sched_barrier(0); }
DI void dma16(const void* g, unsigned l) {
  asm volatile("s_mov_b32 m0, %1\n\ts_nop 0\n\tglobal_load_lds_dwordx4 %0, off" ::"v"(g), "s"(l) : "memory");
}
DI void dma16s(const void* sb, unsigned voff, unsigned l) {
  asm volatile("s_mov_b32 m0, %2\n\ts_nop 0\n\tglobal_load_lds_dwordx4 %0, %1" ::"v"(voff), "s"(sb), "s"(l) : "memory");
}
DI void dma16s_x4k(const void* sb, unsigned voff, unsigned l) {
  asm volatile("s_mov_b32 m0, %2\n\ts_nop 0\n\tglobal_load_lds_dwordx4 %0, %1\n\tglobal_load_lds_dwordx4 %0, %1 offset:1024\n\t"
               "global_load_lds_dwordx4 %0, %1 offset:2048\n\tglobal_load_lds_dwordx4 %0, %1 offset:3072" ::"v"(voff), "s"(sb), "s"(l) : "memory");
}
DI void dma16s_x4k_sc1(const void* sb, unsigned voff, unsigned l) {
  asm volatile("s_mov_b32 m0, %2\n\ts_nop 0\n\tglobal_load_lds_dwordx4 %0, %1 sc1\n\tglobal_load_lds_dwordx4 %0, %1 offset:1024 sc1\n\t"
               "global_load_lds_dwordx4 %0, %1 offset:2048 sc1\n\tglobal_load_lds_dwordx4 %0, %1 offset:3072 sc1" ::"v"(voff), "s"(sb), "s"(l) : "memory");
}
DI void touch(const void* g, unsigned l) { asm volatile("s_mov_b32 m0, %1\n\ts_nop 0\n\tglobal_load_lds_dword %0, off" ::"v"(g), "s"(l) : "memory"); }
DI s16x4 trread(LAS char* p) { return __builtin_amdgcn_ds_read_tr16_b64_v4i16((LAS s16x4*)p); }
DI bf16x8 cat(s16x4 lo, s16x4 hi) { return __builtin_shufflevector(lo, hi, 0, 1, 2, 3, 4, 5, 6, 7); }
DI unsigned pk2(float lo, float hi) { f32x2 v = {lo, hi}; return __builtin_bit_cast(unsigned, __builtin_convertvector(v, bf16v2)); }
DI bf16x8 pack8(f32x4 a, f32x4 b) { u32x4 r = {pk2(a[0], a[1]), pk2(a[2], a[3]), pk2(b[0], b[1]), pk2(b[2], b[3])}; return __builtin_bit_cast(bf16x8, r); }
DI float bflo(unsigned u) { return __uint_as_float(u << 16); }
DI float bfhi(unsigned u) { return __uint_as_float(u & 0xffff0000u); }
DI float rbf(float x) { return bflo(pk2(x, x)); }
template <int CTRL> DI float dpp(float x) { return __builtin_bit_cast(float, __builtin_amdgcn_mov_dpp(__builtin_bit_cast(int, x), CTRL, 0xf, 0xf, true)); }
constexpr int XOR1 = 0xB1, XOR2 = 0x4E, HMIR = 0x141, MIR = 0x140, ROR8 = 0x128;
DI float row16_max(float x) { x = fmaxf(x, dpp<XOR1>(x)); x = fmaxf(x, dpp<XOR2>(x)); x = fmaxf(x, dpp<HMIR>(x)); return fmaxf(x, dpp<MIR>(x)); }
DI float row16_min(float x) { x = fminf(x, dpp<XOR1>(x)); x = fminf(x, dpp<XOR2>(x)); x = fminf(x, dpp<HMIR>(x)); return fminf(x, dpp<MIR>(x)); }
DI float row16_sum(float x) { x += dpp<XOR1>(x); x += dpp<XOR2>(x); x += dpp<HMIR>(x); return x + dpp<MIR>(x); }
DI float xg_max(float x) {
  auto s = __builtin_amdgcn_permlane16_swap(__float_as_uint(x), __float_as_uint(x), false, false);
  x = fmaxf(__uint_as_float(s[0]), __uint_as_float(s[1]));
  auto t = __builtin_amdgcn_permlane32_swap(__float_as_uint(x), __float_as_uint(x), false, false);
  return fmaxf(__uint_as_float(t[0]), __uint_as_float(t[1]));
}
DI float xg_min(float x) {
  auto s = __builtin_amdgcn_permlane16_swap(__float_as_uint(x), __float_as_uint(x), false, false);
  x = fminf(__uint_as_float(s[0]), __uint_as_float(s[1]));
  auto t = __builtin_amdgcn_permlane32_swap(__float_as_uint(x), __float_as_uint(x), false, false);
  return fminf(__uint_as_float(t[0]), __uint_as_float(t[1]));
}
DI float xg_sum(float x) {
  auto s = __builtin_amdgcn_permlane16_swap(__float_as_uint(x), __float_as_uint(x), false, false);
  x = __uint_as_float(s[0]) + __uint_as_float(s[1]);
  auto t = __builtin_amdgcn_permlane32_swap(__float_as_uint(x), __float_as_uint(x), false, false);
  return __uint_as_float(t[0]) + __uint_as_float(t[1]);
}
constexpr float LOG2E = 1.4426950408889634f;
#define SB() __builtin_amdgcn_sched_barrier(0)
DI u32x4 gelu8(f32x4 a, f32x4 b) {
  const float c1 = -2.0f * 0.7978845608028654f * LOG2E, c3 = c1 * 0.044715f;
  f32x4 ta = a * a, tb = b * b;
  ta = ta * c3 + c1; tb = tb * c3 + c1;
  ta = ta * a; tb = tb * b;
  SB();
#pragma unroll
  for (int r = 0; r < 4; ++r) { ta[r] = __builtin_amdgcn_exp2f(ta[r]); tb[r] = __builtin_amdgcn_exp2f(tb[r]); }
  SB();
  ta = ta + 1.0f; tb = tb + 1.0f;
  SB();
#pragma unroll
  for (int r = 0; r < 4; ++r) { ta[r] = __builtin_amdgcn_rcpf(ta[r]); tb[r] = __builtin_amdgcn_rcpf(tb[r]); }
  SB();
  return __builtin_bit_cast(u32x4, pack8(a * ta, b * tb));
}
DI void dual_softmax(f32x4 p0, f32x4 p1, f32x4& e0, f32x4& e1, f32x4& n0, f32x4& n1) {
  p0 = p0 * LOG2E; p1 = p1 * LOG2E;
  float mx = fmaxf(fmaxf(fmaxf(p0[0], p0[1]), fmaxf(p0[2], p0[3])), fmaxf(fmaxf(p1[0], p1[1]), fmaxf(p1[2], p1[3])));
  float mn = fminf(fminf(fminf(p0[0], p0[1]), fminf(p0[2], p0[3])), fminf(fminf(p1[0], p1[1]), fminf(p1[2], p1[3])));
  mx = xg_max(mx); mn = xg_min(mn);
  e0 = p0 - mx; e1 = p1 - mx; n0 = mn - p0; n1 = mn - p1;
#pragma unroll
  for (int r = 0; r < 4; ++r) {
    e0[r] = __builtin_amdgcn_exp2f(e0[r]); e1[r] = __builtin_amdgcn_exp2f(e1[r]);
    n0[r] = __builtin_amdgcn_exp2f(n0[r]); n1[r] = __builtin_amdgcn_exp2f(n1[r]);
  }
  const f32x4 se = e0 + e1, sn4 = n0 + n1;
  const float sp = xg_sum((se[0] + se[1]) + (se[2] + se[3]));
  const float sn = xg_sum((sn4[0] + sn4[1]) + (sn4[2] + sn4[3]));
  const float rp = __builtin_amdgcn_rcpf(sp), rn = __builtin_amdgcn_rcpf(sn);
  e0 = e0 * rp; e1 = e1 * rp; n0 = n0 * rn; n1 = n1 * rn;
}

#define STAMP(i) do { } while (0)
#define DBG_FIELD
#define DBG_ARG(x)
__global__ __launch_bounds__(512) void k0_ln(const u16* __restrict__ x, u16* __restrict__ xnf, unsigned* __restrict__ flags,
                                             const u16* __restrict__ wqkv, const u16* __restrict__ w1
                                             ) {
  __shared__ float red[2][8][8];
  __shared__ __attribute__((aligned(16))) unsigned dump[256];
  const int tid = threadIdx.x, w = tid >> 6, l = tid & 63, i = l & 7, c = l >> 3;
  const int row = blockIdx.x * 8 + i;
  const u32x4* src = (const u32x4*)(x + (size_t)row * D_) + 16 * w + c;
  const u32x4 v0 = src[0], v1 = src[8];
  if (blockIdx.x == 0) { for (int j = tid; j < FLAG_WORDS; j += 512) flags[j] = 0u; }
  float f[16];
#pragma unroll
  for (int j = 0; j < 4; ++j) { f[2 * j] = bflo(v0[j]); f[2 * j + 1] = bfhi(v0[j]); f[8 + 2 * j] = bflo(v1[j]); f[9 + 2 * j] = bfhi(v1[j]); }
  float s = 0.f;
#pragma unroll
  for (int j = 0; j < 16; ++j) s += f[j];
  s += dpp<ROR8>(s); s = xg_sum(s);
  if (l < 8) red[0][w][i] = s;
  __syncthreads();
  float mean = 0.f;
#pragma unroll
  for (int j = 0; j < 8; ++j) mean += red[0][j][i];
  mean *= (1.0f / D_);
  float q = 0.f;
#pragma unroll
  for (int j = 0; j < 16; ++j) { float d = f[j] - mean; q = fmaf(d, d, q); }
  q += dpp<ROR8>(q); q = xg_sum(q);
  if (l < 8) red[1][w][i] = q;
  __syncthreads();
  float var = 0.f;
#pragma unroll
  for (int j = 0; j < 8; ++j) var += red[1][j][i];
  var *= (1.0f / D_);
  const float mb = rbf(mean);
  const float sd = rbf(sqrtf(rbf(rbf(var) + 1e-5f)));
  const float rs = 1.0f / sd;
  u32x4 o0, o1;
#pragma unroll
  for (int j = 0; j < 4; ++j) {
    o0[j] = pk2(rbf(f[2 * j] - mb) * rs, rbf(f[2 * j + 1] - mb) * rs);
    o1[j] = pk2(rbf(f[8 + 2 * j] - mb) * rs, rbf(f[9 + 2 * j] - mb) * rs);
  }
  const int mfrag = row >> 4, ri = row & 15;
  const int ch0 = 16 * w + c, ch1 = ch0 + 8;
  auto rsx = __builtin_amdgcn_make_buffer_rsrc((void*)xnf, 0, 4 << 20, 0x00020000);
  __builtin_amdgcn_raw_buffer_store_b128(o0, rsx, (((ch0 >> 2) * 128 + mfrag) * 64 + (ch0 & 3) * 16 + ri) * 16, 0, ST_AUX);
  __builtin_amdgcn_raw_buffer_store_b128(o1, rsx, (((ch1 >> 2) * 128 + mfrag) * 64 + (ch1 & 3) * 16 + ri) * 16, 0, ST_AUX);
}

struct G1Args {
  const u16* xnf; const u16* wqkv; const u16* w1; const u16* b1; const u16* wphi;
  u16* af; u16* phiq; float* kvp; const u16* w2; const u16* wo; DBG_FIELD
};

template <bool ISX, int GRP>
DI void g1_body(const G1Args& A, LAS char* lds, int rb, int j, int pos4, int wm, int lane) {
  constexpr int NF = GRP == 0 ? 7 : 6;
  [[maybe_unused]] const int wv_ = GRP * 4 + wm;
  STAMP(0);
  const int g = lane >> 4, q = (lane & 15) >> 2, p = lane & 3;
  const int hp = 2 * j + (pos4 >> 1);
  const int cp = pos4 == 0 ? 0 : pos4 == 3 ? 384 : pos4 == 1 ? 96 : 224;
  const int co = pos4 == 0 ? 64 : pos4 == 3 ? 368 : pos4 == 1 ? 80 : 352;
  const int cr = pos4 == 0 ? 0 : pos4 == 3 ? 320 : pos4 == 1 ? 64 : 192;
  constexpr int RBASE = ISX ? 256 : 128;
  const int colT = cp + 32 * (2 * GRP), colU = cp + 32 * (ISX ? GRP : 2 * GRP + 1);

  const unsigned lbase = (unsigned)(size_t)lds;
  const char* dsrc = nullptr; long dstep = 0; int dj = 0;
  const char* xsrc = (const char*)A.xnf + (size_t)(rb * 16 + 4 * wm) * 1024;
  const unsigned xoff = lane * 16;
  if (GRP == 0) {
    const int u0 = 8 * (wm & 1) + 4 * (wm >> 1), c = lane & 31, line = c >> 3, ch = c & 7, row = u0 + 16 * (lane >> 5);
    const bool fromq = ISX ? (line < 2) : (line < 1);
    const int ld = fromq ? 3 * D_ : F_;
    const u16* base = fromq ? (A.wqkv + (ISX ? (1 + line) * D_ : 0) + 64 * hp) : (A.w1 + 448 * j + cr + 64 * (line - (ISX ? 2 : 1)));
    dsrc = (const char*)(base + (size_t)row * ld + ch * 8); dstep = 64L * ld; dj = 2 * ld;
  }
  const unsigned ddst = lbase + (GRP == 0 ? BOFF + 4 * wm * PSTR : 4 * wm * 1024);
  auto issue = [&](int slot) {
    if (GRP == 0) {
#pragma unroll
      for (int jj = 0; jj < 4; ++jj) dma16(dsrc + jj * dj, ddst + slot * SLOT + jj * PSTR);
      dsrc += dstep;
    } else {
      dma16s_x4k(xsrc, xoff, ddst + slot * SLOT);
      xsrc += 128 * 1024;
    }
  };
  if (GRP == 0) dma16s(A.wphi + (size_t)hp * 2048 + wm * 512, xoff, lbase + G1_WPHI + wm * 1024);
  else if (wm == 0) { const int nch = ISX ? 16 : 24; dma16s(A.b1 + 448 * j + cr, (lane < nch ? lane : 0) * 16, lbase + G1_BIAS); }
  issue(0); issue(1); issue(2); issue(3);
  wait_vm<12>();
  bar();

  STAMP(1);
  f32x4 acc[4][NF];
  {
    LAS char* bp = lds + G1_BIAS + 8 * g;
#pragma unroll
    for (int f = 0; f < NF; ++f) {
      f32x4 b4 = {0.f, 0.f, 0.f, 0.f};
      if (f >= (ISX ? 4 : 2)) {
        const int col = f < 4 ? colT + 16 * (f - 2) : f < 6 ? colU + 16 * (f - 4) : co;
        const u32x2 bb = *(LAS u32x2*)(bp + (col - cr) * 2);
        b4 = (f32x4){bflo(bb[0]), bfhi(bb[0]), bflo(bb[1]), bfhi(bb[1])};
      }
#pragma unroll
      for (int m = 0; m < 4; ++m) acc[m][f] = b4;
    }
  }
  const int lb = BOFF + PSTR * (q + 4 * (g & 1)) + 512 * (g >> 1) + 8 * p;
  LAS char* pA = lds + wm * 4096 + lane * 16;
  LAS char* pS = lds + lb + 64 * GRP;
  LAS char* pT = lds + lb + (ISX ? 128 + 64 * GRP : RBASE + (colT - cr) * 2);
  LAS char* pU = lds + lb + RBASE + (colU - cr) * 2;
  LAS char* pO = lds + lb + RBASE + (co - cr) * 2;
  auto rdB = [&](int so, int f) -> bf16x8 {
    LAS char* pp = (f < 2 ? pS : f < 4 ? pT : f < 6 ? pU : pO) + so + (f < 6 ? (f & 1) * 32 : 0);
    return cat(trread(pp), trread(pp + 8 * PSTR));
  };
  bf16x8 a0[4], b0[NF], a1[4], b1[NF];
#pragma unroll
  for (int f = 0; f < NF; ++f) b0[f] = rdB(0, f);
#pragma unroll
  for (int m = 0; m < 4; ++m) a0[m] = *(LAS bf16x8*)(pA + m * 1024);
  wait_lgkm0();
  wait_vm<8>();
  bar();
  auto step = [&](int s, bf16x8 (&a)[4], bf16x8 (&b)[NF], bf16x8 (&an)[4], bf16x8 (&bn)[NF], auto tag) {
    constexpr int T = decltype(tag)::value;
    const int so = ((s + 1) & 3) * SLOT;
    auto mm = [&](int i) {
      const int f = i >> 2, m = i & 3;
      if (ISX && (f == 2 || f == 3)) acc[m][f] = MFMA16(a[m], b[f], acc[m][f]);
      else acc[m][f] = MFMA16(b[f], a[m], acc[m][f]);
    };
    s16x4 lo[NF];
    auto rd = [&](int i) {
      if (i < 4) an[i] = *(LAS bf16x8*)(pA + so + i * 1024);
      else {
        const int f = (i - 4) >> 1;
        LAS char* pp = (f < 2 ? pS : f < 4 ? pT : f < 6 ? pU : pO) + so + (f < 6 ? (f & 1) * 32 : 0);
        if (((i - 4) & 1) == 0) lo[f] = trread(pp); else bn[f] = cat(lo[f], trread(pp + 8 * PSTR));
      }
    };
    if (GRP == 1) {
      if (T == 4) issue(s & 3);
      if (T > 0) {
#pragma unroll
        for (int i = 0; i < 4 + 2 * NF; ++i) rd(i);
      }
      SB();
#pragma unroll
      for (int i = 0; i < 4 * NF; ++i) mm(i);
      SB();
    } else {
#pragma unroll
      for (int i = 0; i < 4 * NF; ++i) mm(i);
      SB();
      if (T > 0) {
#pragma unroll
        for (int i = 0; i < 4 + 2 * NF; ++i) rd(i);
      }
      if (T == 4) issue(s & 3);
    }
    if (T > 0) {
      wait_lgkm0();
      if (T > 1) wait_vm<(T == 4 ? 8 : T == 3 ? 4 : 0)>();
      bar();
    }
  };
#pragma unroll 1
  for (int s = 0; s < 28; s += 2) { step(s, a0, b0, a1, b1, IC<4>{}); step(s + 1, a1, b1, a0, b0, IC<4>{}); }
  step(28, a0, b0, a1, b1, IC<3>{}); step(29, a1, b1, a0, b0, IC<2>{}); step(30, a0, b0, a1, b1, IC<1>{}); step(31, a1, b1, a0, b0, IC<0>{});

  STAMP(2);
  if (!ISX) {
    const int yw = ((rb * 16 + hp) * 8 + GRP * 4 + wm) * 72;
#pragma unroll
    for (int t = 0; t < 2; ++t) {
      const int ln = yw + t * 36 + (lane < 36 ? lane : 0);
      touch(ln < 57344 ? (const char*)A.w2 + (size_t)ln * 128 : (const char*)A.wo + (size_t)(ln - 57344) * 128, lbase + G1_DUMP);
    }
  }
  const int mf0 = rb * 16 + wm * 4;
  {
    auto rs = __builtin_amdgcn_make_buffer_rsrc((void*)A.af, 0, NKB * 128 * 1024, 0x00020000);
    const int o0 = (mf0 * 64 + lane) * 16;
    if (!ISX) {
      const int kb = (448 * j + colT) >> 5;
#pragma unroll
      for (int m = 0; m < 4; ++m) __builtin_amdgcn_raw_buffer_store_b128(gelu8(acc[m][2], acc[m][3]), rs, o0 + kb * 131072 + m * 1024, 0, ST_AUX);
    }
    {
      const int kb = (448 * j + colU) >> 5;
#pragma unroll
      for (int m = 0; m < 4; ++m) __builtin_amdgcn_raw_buffer_store_b128(gelu8(acc[m][4], acc[m][5]), rs, o0 + kb * 131072 + m * 1024, 0, ST_AUX);
    }
    if (GRP == 0) {
      const int kb = (448 * j + co) >> 5, n = (co >> 4) & 1;
#pragma unroll
      for (int m = 0; m < 4; m += 2) {
        const u32x4 gv = gelu8(acc[m][NF - 1], acc[m + 1][NF - 1]);
        __builtin_amdgcn_raw_buffer_store_b64((u32x2){gv[0], gv[1]}, rs, o0 + kb * 131072 + n * 8 + m * 1024, 0, ST_AUX);
        __builtin_amdgcn_raw_buffer_store_b64((u32x2){gv[2], gv[3]}, rs, o0 + kb * 131072 + n * 8 + (m + 1) * 1024, 0, ST_AUX);
      }
    }
  }
  const int head = 2 * hp + GRP;
  if (!ISX) {
    auto rs = __builtin_amdgcn_make_buffer_rsrc((void*)A.phiq, 0, 4 << 20, 0x00020000);
#pragma unroll
    for (int m = 0; m < 4; ++m)
      __builtin_amdgcn_raw_buffer_store_b128(__builtin_bit_cast(u32x4, pack8(acc[m][0], acc[m][1])), rs, lane * 16 + ((mf0 + m) * 32 + head) * 1024, 0, ST_AUX);
  } else {
    bf16x8 wp[2];
    {
      LAS char* pw = lds + G1_WPHI + GRP * 2048 + (4 * g + q) * 64 + 8 * p;
      wp[0] = cat(trread(pw), trread(pw + 1024));
      wp[1] = cat(trread(pw + 32), trread(pw + 1024 + 32));
    }
    const f32x4 z4 = {0.f, 0.f, 0.f, 0.f};
    f32x4 p0[4], p1[4];
#pragma unroll
    for (int m = 0; m < 4; ++m) {
      const bf16x8 qb = pack8(acc[m][0], acc[m][1]);
      p0[m] = MFMA16(wp[0], qb, z4); p1[m] = MFMA16(wp[1], qb, z4);
    }
    constexpr int TS = 144;
    LAS char* tb = lds + (GRP * 4 + wm) * 12288;
    {
      LAS char* wrp = tb + (lane & 15) * TS + 8 * g;
#pragma unroll
      for (int m = 0; m < 4; ++m) {
        f32x4 e0, e1, n0, n1;
        dual_softmax(p0[m], p1[m], e0, e1, n0, n1);
        const u32x4 pe = __builtin_bit_cast(u32x4, pack8(e0, e1)), pn = __builtin_bit_cast(u32x4, pack8(n0, n1));
        *(LAS u32x2*)(wrp + m * 16 * TS) = (u32x2){pe[0], pe[1]};
        *(LAS u32x2*)(wrp + m * 16 * TS + 32) = (u32x2){pe[2], pe[3]};
        *(LAS u32x2*)(wrp + m * 16 * TS + 64) = (u32x2){pn[0], pn[1]};
        *(LAS u32x2*)(wrp + m * 16 * TS + 96) = (u32x2){pn[2], pn[3]};
      }
    }
    wait_lgkm0();
    f32x4 kv[4][3];
    const bf16x8 ones = {0x3F80, 0x3F80, 0x3F80, 0x3F80, 0x3F80, 0x3F80, 0x3F80, 0x3F80};
    LAS char* rdp = tb + (4 * g + q) * TS + 8 * p;
    bf16x8 pa[2][4];
#pragma unroll
    for (int u = 0; u < 2; ++u)
#pragma unroll
      for (int fq = 0; fq < 4; ++fq) pa[u][fq] = cat(trread(rdp + (32 * u) * TS + 32 * fq), trread(rdp + (32 * u + 16) * TS + 32 * fq));
#pragma unroll
    for (int u = 0; u < 2; ++u) {
      const bf16x8 v0 = pack8(acc[2 * u][2], acc[2 * u + 1][2]), v1 = pack8(acc[2 * u][3], acc[2 * u + 1][3]);
#pragma unroll
      for (int fq = 0; fq < 4; ++fq) {
        kv[fq][0] = MFMA16(pa[u][fq], v0, u == 0 ? z4 : kv[fq][0]);
        kv[fq][1] = MFMA16(pa[u][fq], v1, u == 0 ? z4 : kv[fq][1]);
        kv[fq][2] = MFMA16(pa[u][fq], ones, u == 0 ? z4 : kv[fq][2]);
      }
    }
    wait_lgkm0();
    LAS f32x4* rbuf = (LAS f32x4*)tb + lane;
#pragma unroll
    for (int fq = 0; fq < 4; ++fq)
#pragma unroll
      for (int dc = 0; dc < 3; ++dc) rbuf[(fq * 3 + dc) * 64] = kv[fq][dc];
    wait_lgkm0();
    bar();
    LAS f32x4* sbuf = (LAS f32x4*)(lds + GRP * 4 * 12288) + lane;
    auto rs = __builtin_amdgcn_make_buffer_rsrc((void*)A.kvp, 0, 3 << 20, 0x00020000);
#pragma unroll
    for (int t = 0; t < 3; ++t) {
      const int qd = 3 * wm + t;
      const f32x4 sv = (sbuf[qd * 64] + sbuf[768 + qd * 64]) + (sbuf[1536 + qd * 64] + sbuf[2304 + qd * 64]);
      __builtin_amdgcn_raw_buffer_store_b128(__builtin_bit_cast(u32x4, sv), rs, lane * 16 + ((rb * 32 + head) * 12 + qd) * 1024, 0, ST_AUX);
    }
  }
  STAMP(3);
  STAMP(4);
}

__global__ __launch_bounds__(512, 2) void k1_gemm1(G1Args A) {
  extern __shared__ __attribute__((aligned(16))) char smem[];
  LAS char* lds = (LAS char*)smem;
  const int wid = __builtin_amdgcn_readfirstlane(threadIdx.x >> 6), lane = threadIdx.x & 63;
  const int bid = blockIdx.x, xcd = bid & 7, idx = bid >> 3;
  const int rb = (xcd & 1) * 4 + (idx & 3), ct = (xcd >> 1) * 8 + (idx >> 2);
  const int j = ct >> 2, pos4 = ct & 3, wm = wid & 3;
  const bool isx = (pos4 == 0) || (pos4 == 3);
  if (isx) { if (wid < 4) g1_body<true, 0>(A, lds, rb, j, pos4, wm, lane); else g1_body<true, 1>(A, lds, rb, j, pos4, wm, lane); }
  else { if (wid < 4) g1_body<false, 0>(A, lds, rb, j, pos4, wm, lane); else g1_body<false, 1>(A, lds, rb, j, pos4, wm, lane); }
}

struct G2Args {
  u16* af; const u16* w2; const u16* wo; const u16* phiq; const float* kvp; float* part;
  const u16* x; const u16* b2; u16* out; unsigned* flags; const u16* wphi; DBG_FIELD
};

DI unsigned peek_flag(unsigned* flag) { return __hip_atomic_load((GAS unsigned*)flag, RLX_AGENT); }
DI void await_flag(unsigned seen, unsigned* flag, unsigned want, unsigned* tmo) {
  unsigned spins = 0;
  while ((unsigned)__builtin_amdgcn_readfirstlane(seen) < want) {
    __builtin_amdgcn_s_sleep(1);
    if (++spins > (1u << 22)) { __hip_atomic_store((GAS unsigned*)tmo, 1u, RLX_AGENT); break; }
    seen = peek_flag(flag);
  }
  __builtin_amdgcn_fence(__ATOMIC_ACQUIRE, "workgroup");
}

template <bool FIN, int GRP>
DI void g2_body(const G2Args& A, LAS char* lds, int rb, int ct, int w4, int lane) {
  constexpr int NS = FIN ? N2 : N1;
  constexpr int KB0 = FIN ? 2 * N1 : 0;
  [[maybe_unused]] const int wv_ = GRP * 4 + w4;
  STAMP(0);
  const int g = lane >> 4, q = (lane & 15) >> 2, p = lane & 3;
  const int wm = w4 & 1, wn = w4 >> 1;
  const int tile = rb * 8 + ct, wv = GRP * 4 + w4;
  const int col0 = ct * 128 + wn * 64 + 4 * g;
  u32x2 bb[4];
  if (FIN && GRP == 0) {
#pragma unroll
    for (int n = 0; n < 4; ++n) bb[n] = *(const u32x2*)(A.b2 + col0 + n * 16);
  }
  if (!FIN) {
    const int head = tile & 31, rq = tile >> 5;
    LAS char* kvl = lds + RING;
    u32x4 qf[4];
    {
      const u32x4* q4 = (const u32x4*)A.phiq + ((size_t)(rq * 32 + wv * 4) * 32 + head) * 64 + lane;
#pragma unroll
      for (int m = 0; m < 4; ++m) qf[m] = q4[m * 2048];
    }
    u16 wraw[2][8];
    {
      const u16* wsrc = A.wphi + head * 1024 + 4 * g * 32 + (lane & 15);
#pragma unroll
      for (int fc = 0; fc < 2; ++fc)
#pragma unroll
        for (int e = 0; e < 8; ++e) wraw[fc][e] = wsrc[((e & 3) + 16 * (e >> 2)) * 32 + 16 * fc];
    }
    f32x4 kp[8][2];
    const int kh = wv < 4 ? wv >> 1 : (lane >> 2) & 1, dc = wv < 4 ? wv & 1 : 2, sl = wv < 4 ? lane : (lane & 3) * 16;
    const bool kact = wv < 4 || (wv == 4 && lane < 8);
    if (kact) {
      const f32x4* src = (const f32x4*)A.kvp + ((size_t)head * 12 + (2 * kh) * 3 + dc) * 64 + sl;
#pragma unroll
      for (int r8 = 0; r8 < 8; ++r8) { kp[r8][0] = src[(size_t)r8 * 32 * 768]; kp[r8][1] = src[(size_t)r8 * 32 * 768 + 192]; }
    }
    bar();
    const f32x4 z4 = {0.f, 0.f, 0.f, 0.f};
    bf16x8 pq[4][2];
    {
      bf16x8 wp[2];
#pragma unroll
      for (int fc = 0; fc < 2; ++fc)
#pragma unroll
        for (int e = 0; e < 8; ++e) wp[fc][e] = (short)wraw[fc][e];
#pragma unroll
      for (int m = 0; m < 4; ++m) {
        const bf16x8 qb = __builtin_bit_cast(bf16x8, qf[m]);
        f32x4 e0, e1, n0, n1;
        dual_softmax(MFMA16(wp[0], qb, z4), MFMA16(wp[1], qb, z4), e0, e1, n0, n1);
        pq[m][0] = pack8(e0, e1); pq[m][1] = pack8(n0, n1);
      }
    }
    if (kact) {
      f32x4 s0 = kp[0][0], s1 = kp[0][1];
#pragma unroll
      for (int r8 = 1; r8 < 8; ++r8) { s0 += kp[r8][0]; s1 += kp[r8][1]; }
      *(LAS bf16x8*)(kvl + (kh * 3 + dc) * 1024 + (wv < 4 ? lane : lane & 3) * 16) = pack8(s0, s1);
    }
    STAMP(13);
    wait_lgkm0();
    bar();
    STAMP(14);
    bf16x8 ka[2][3];
#pragma unroll
    for (int k2 = 0; k2 < 2; ++k2)
#pragma unroll
      for (int d2 = 0; d2 < 3; ++d2) ka[k2][d2] = *(LAS bf16x8*)(kvl + (k2 * 3 + d2) * 1024 + (d2 < 2 ? lane : g) * 16);
    auto rsrc = __builtin_amdgcn_make_buffer_rsrc((void*)(A.af + (size_t)(112 + head) * 128 * 512), 0, 128 * 1024, 0x00020000);
#pragma unroll
    for (int m = 0; m < 4; ++m) {
      const bf16x8 b0 = pq[m][0], b1 = pq[m][1];
      f32x4 n0 = MFMA16(ka[0][0], b0, z4); n0 = MFMA16(ka[1][0], b1, n0);
      f32x4 n1 = MFMA16(ka[0][1], b0, z4); n1 = MFMA16(ka[1][1], b1, n1);
      f32x4 dn = MFMA16(ka[0][2], b0, z4); dn = MFMA16(ka[1][2], b1, dn);
      const float rd = __builtin_amdgcn_rcpf(rbf(rbf(dn[0]) + 1e-6f));
      f32x4 o0, o1;
#pragma unroll
      for (int r = 0; r < 4; ++r) { o0[r] = rbf(n0[r]) * rd; o1[r] = rbf(n1[r]) * rd; }
      const int mf = rq * 32 + wv * 4 + m;
      __builtin_amdgcn_raw_buffer_store_b128(__builtin_bit_cast(u32x4, pack8(o0, o1)), rsrc, (mf * 64 + lane) * 16, 0, 16);
    }
    STAMP(15);
  }
  bar();

  STAMP(1);
  f32x4 acc[4][4];
#pragma unroll
  for (int m = 0; m < 4; ++m)
#pragma unroll
    for (int n = 0; n < 4; ++n) {
      if (FIN && GRP == 0) acc[m][n] = (f32x4){bflo(bb[n][0]), bfhi(bb[n][0]), bflo(bb[n][1]), bfhi(bb[n][1])};
      else acc[m][n] = (f32x4){0.f, 0.f, 0.f, 0.f};
    }
  LAS char* pA = lds + GRP * 8192 + wm * 4096 + lane * 16;
  LAS char* pB = lds + BOFF + GRP * 8 * PSTR + PSTR * (4 * (g & 1) + q) + 256 * (g >> 1) + 8 * p + 128 * wn;
  constexpr int SO_FIRST = (112 - KB0) / 2;
  bf16x8 a0[4], b0[4], a1[4], b1[4];
#pragma unroll
  for (int n = 0; n < 4; ++n) b0[n] = cat(trread(pB + n * 32), trread(pB + n * 32 + 512));
#pragma unroll
  for (int m = 0; m < 4; ++m) a0[m] = *(LAS bf16x8*)(pA + m * 1024);
  wait_lgkm0();
  bar();

  auto step = [&](int st, bf16x8 (&a)[4], bf16x8 (&b)[4], bf16x8 (&an)[4], bf16x8 (&bn)[4], auto tag) {
    constexpr int T = decltype(tag)::value;
    const int so = ((st + 1) & 3) * SLOT;
    s16x4 lo[4];
    auto rd = [&](int i) {
      if (i < 4) an[i] = *(LAS bf16x8*)(pA + so + i * 1024);
      else {
        const int n = (i - 4) >> 1;
        LAS char* pp = pB + so + n * 32;
        if (((i - 4) & 1) == 0) lo[n] = trread(pp); else bn[n] = cat(lo[n], trread(pp + 512));
      }
    };
    if (GRP == 1 && T > 0) {
#pragma unroll
      for (int i = 0; i < 12; ++i) rd(i);
    }
    SB();
#pragma unroll
    for (int i = 0; i < 16; ++i) acc[i & 3][i >> 2] = MFMA16(b[i >> 2], a[i & 3], acc[i & 3][i >> 2]);
    SB();
    if (GRP == 0 && T > 0) {
#pragma unroll
      for (int i = 0; i < 12; ++i) rd(i);
    }
    if (T > 0) {
      wait_lgkm0();
      bar();
    }
  };
  unsigned* const ocnt = A.flags + (FLAG_OCNT + (rb >> 2)) * FLAG_STRIDE;
  unsigned* const pflag = A.flags + (FLAG_PART + tile) * FLAG_STRIDE;
  unsigned* const tmo = A.flags + FLAG_TMO * FLAG_STRIDE;
  unsigned seen = 0;
#pragma unroll 1
  for (int st = 0; st < NS - 4; st += 2) {
    if (wv == 0) {
      if (!FIN && st == 2 && lane == 0)
        __hip_atomic_fetch_add((GAS unsigned*)(A.flags + (FLAG_OCNT + (tile >> 5)) * FLAG_STRIDE), 1u, RLX_AGENT);
      if (FIN) {
        constexpr int S0 = (SO_FIRST - 8) & ~1;
        if (st == S0) seen = peek_flag(ocnt);
        if (st == S0 + 2) await_flag(seen, ocnt, 32u, tmo);
      }
    }
    step(st, a0, b0, a1, b1, IC<4>{});
    if (!FIN && st == 0) wait_vm<0>();
    step(st + 1, a1, b1, a0, b0, IC<4>{});
  }
  if (FIN && wv == 0) seen = peek_flag(pflag);
  step(NS - 4, a0, b0, a1, b1, IC<3>{}); step(NS - 3, a1, b1, a0, b0, IC<2>{});
  if (FIN && wv == 0) await_flag(seen, pflag, 1u, tmo);
  step(NS - 2, a0, b0, a1, b1, IC<1>{});
  f32x4 pv[2][4];
  if (FIN) {
    auto rsrc = __builtin_amdgcn_make_buffer_rsrc((void*)(A.part + (size_t)tile * 16384), 0, 65536, 0x00020000);
#pragma unroll
    for (int mm = 0; mm < 2; ++mm)
#pragma unroll
      for (int n = 0; n < 4; ++n)
        pv[mm][n] = __builtin_bit_cast(f32x4, __builtin_amdgcn_raw_buffer_load_b128(rsrc, ((wv * 8 + mm * 4 + n) * 64 + lane) * 16, 0, 16));
    SB();
  }
  step(NS - 1, a1, b1, a0, b0, IC<0>{});

  STAMP(2);
  {
    LAS f32x4* wb = (LAS f32x4*)(lds + CBASE + wv * 8192) + lane;
#pragma unroll
    for (int mm = 0; mm < 2; ++mm)
#pragma unroll
      for (int n = 0; n < 4; ++n) wb[(mm * 4 + n) * 64] = acc[GRP == 0 ? 2 + mm : mm][n];
  }
  wait_lgkm0();
  bar();
  f32x4 r[2][4];
  {
    LAS f32x4* rbuf = (LAS f32x4*)(lds + CBASE + ((1 - GRP) * 4 + w4) * 8192) + lane;
#pragma unroll
    for (int mm = 0; mm < 2; ++mm)
#pragma unroll
      for (int n = 0; n < 4; ++n) r[mm][n] = acc[GRP == 0 ? mm : 2 + mm][n] + rbuf[(mm * 4 + n) * 64];
  }
  if (!FIN) {
    auto rsrc = __builtin_amdgcn_make_buffer_rsrc((void*)(A.part + (size_t)tile * 16384), 0, 65536, 0x00020000);
#pragma unroll
    for (int mm = 0; mm < 2; ++mm)
#pragma unroll
      for (int n = 0; n < 4; ++n)
        __builtin_amdgcn_raw_buffer_store_b128(__builtin_bit_cast(u32x4, r[mm][n]), rsrc, ((wv * 8 + mm * 4 + n) * 64 + lane) * 16, 0, 16);
    wait_vm<0>();
    bar();
    if (wv == 0 && lane == 0) __hip_atomic_store((GAS unsigned*)(A.flags + (FLAG_PART + tile) * FLAG_STRIDE), 1u, RLX_AGENT);
  } else {
    int lz = lane; asm volatile("" : "+v"(lz));
    const int rl = wm * 64 + GRP * 32 + (lz & 15), cl = wn * 64 + 4 * (lz >> 4);
    LAS char* xl = lds + XSLOT * SLOT + (rl >> 2) * XPSTR + (rl & 3) * 256 + cl * 2;
    u16* op = A.out + (size_t)(rb * 128 + rl) * D_ + ct * 128 + cl;
#pragma unroll
    for (int mm = 0; mm < 2; ++mm)
#pragma unroll
      for (int n = 0; n < 4; ++n) {
        const u32x2 xr = *(LAS u32x2*)(xl + mm * 4 * XPSTR + n * 32);
        const f32x4 xv = {bflo(xr[0]), bfhi(xr[0]), bflo(xr[1]), bfhi(xr[1])};
        const f32x4 o = (r[mm][n] + pv[mm][n]) + xv;
        u32x2 ov = {pk2(o[0], o[1]), pk2(o[2], o[3])};
        *(u32x2*)(op + (size_t)mm * 16 * D_ + n * 16) = ov;
      }
  }
  STAMP(3);
  STAMP(4);
}

template <bool FIN>
DI void g2_loader(const G2Args& A, LAS char* lds, int rb, int ct, int w4, int lane) {
  constexpr int NS = FIN ? N2 : N1;
  constexpr int KB0 = FIN ? 2 * N1 : 0;
  const int kbs = w4 >> 1, u0 = 4 * (w4 & 1);
  const unsigned lbase = (unsigned)(size_t)lds;
  const char* asrc = (const char*)A.af + ((size_t)kbs * 128 + rb * 8 + u0) * 1024;
  const unsigned aoff = lane * 16;
  const unsigned boff = (unsigned)(((kbs * 32 + u0 + 8 * (lane >> 4)) * D_ + ct * 128 + (lane & 15) * 8) * 2);
  const unsigned adst = lbase + kbs * 8192 + u0 * 1024, bdst = lbase + BOFF + kbs * 8 * PSTR + u0 * PSTR;
  auto issue = [&](int st) {
    const int kb = KB0 + 2 * st, slot = (st & 3) * SLOT;
    const char* wb = kb < 112 ? (const char*)A.w2 + (size_t)kb * 32 * D_ * 2 : (const char*)A.wo + (size_t)(kb - 112) * 32 * D_ * 2;
    if (FIN && kb >= 112) dma16s_x4k_sc1(asrc + (size_t)kb * 128 * 1024, aoff, adst + slot);
    else dma16s_x4k(asrc + (size_t)kb * 128 * 1024, aoff, adst + slot);
#pragma unroll
    for (int jj = 0; jj < 4; ++jj) dma16s(wb + jj * (D_ * 2), boff, bdst + slot + jj * PSTR);
  };
  if (!FIN) bar();
  issue(0); issue(1); issue(2); issue(3);
  if (!FIN) bar();
  wait_vm<24>();
  bar();
  wait_vm<16>();
  bar();
#pragma unroll 1
  for (int st = 0; st < NS - 4; ++st) { issue(st + 4); wait_vm<16>(); bar(); }
  if (FIN) {
    const char* xs = (const char*)A.x + ((size_t)(rb * 128 + w4 * 32) * D_ + ct * 128) * 2;
    const unsigned xo = (unsigned)((lane >> 4) * (D_ * 2) + (lane & 15) * 16);
#pragma unroll
    for (int jj = 0; jj < 8; ++jj) dma16s(xs + jj * 4 * (D_ * 2), xo, lbase + XSLOT * SLOT + (w4 * 8 + jj) * XPSTR);
    wait_vm<16>(); bar();
    wait_vm<8>(); bar();
    wait_vm<0>(); bar();
  } else {
    wait_vm<8>(); bar();
    wait_vm<0>(); bar();
  }
}

__global__ __launch_bounds__(768) void k2_gemm2(G2Args A) {
  extern __shared__ __attribute__((aligned(16))) char smem[];
  LAS char* lds = (LAS char*)smem;
  const int wid = __builtin_amdgcn_readfirstlane(threadIdx.x >> 6), lane = threadIdx.x & 63;
  const int bid = blockIdx.x, xcd = bid & 7, idx = bid >> 3;
  const int rb = (xcd & 3) * 4 + (idx & 3), ct = (xcd >> 2) * 4 + ((idx >> 2) & 3), fin = idx >> 4;
  const int w4 = wid & 3;
  if (wid >= 8) { if (fin) g2_loader<true>(A, lds, rb, ct, w4, lane); else g2_loader<false>(A, lds, rb, ct, w4, lane); return; }
  if (fin) { if (wid < 4) g2_body<true, 0>(A, lds, rb, ct, w4, lane); else g2_body<true, 1>(A, lds, rb, ct, w4, lane); }
  else { if (wid < 4) g2_body<false, 0>(A, lds, rb, ct, w4, lane); else g2_body<false, 1>(A, lds, rb, ct, w4, lane); }
}

extern "C" void kernel_launch(void* const* d_in, const int* in_sizes, int n_in, void* d_out, int out_size, void* d_ws, size_t ws_size,
                              hipStream_t stream) {
  const u16* x = (const u16*)d_in[0];
  const u16* wqkv = (const u16*)d_in[1];
  const u16* wphi = (const u16*)d_in[2];
  const u16* wo = (const u16*)d_in[3];
  const u16* w1 = (const u16*)d_in[5];
  const u16* b1 = (const u16*)d_in[6];
  const u16* w2 = (const u16*)d_in[7];
  const u16* b2 = (const u16*)d_in[8];
  char* ws = (char*)d_ws;
  unsigned* flags = (unsigned*)(ws + WS_FLAGS);
  u16* xnf = (u16*)(ws + WS_XNF);
  u16* af = (u16*)(ws + WS_AF);
  u16* phiq = (u16*)(ws + WS_PHIQ);
  float* kvp = (float*)(ws + WS_KVP);
  float* part = (float*)(ws + WS_PART);
  (void)hipFuncSetAttribute((const void*)k1_gemm1, hipFuncAttributeMaxDynamicSharedMemorySize, G1_LDS);
  (void)hipFuncSetAttribute((const void*)k2_gemm2, hipFuncAttributeMaxDynamicSharedMemorySize, G2_LDS);
  k0_ln<<<256, 512, 0, stream>>>(x, xnf, flags, wqkv, w1 DBG_ARG(130u));
  G1Args a1 = {xnf, wqkv, w1, b1, wphi, af, phiq, kvp, w2, wo DBG_ARG(128u)};
  k1_gemm1<<<256, 512, G1_LDS, stream>>>(a1);
  G2Args a2 = {af, w2, wo, phiq, kvp, part, x, b2, (u16*)d_out, flags, wphi DBG_ARG(129u)};
  k2_gemm2<<<256, 768, G2_LDS, stream>>>(a2);
}
```

```cpp
#define K0_PF 1
#include <hip/hip_runtime.h>
#include <hip/hip_bf16.h>
#include <stdint.h>

typedef unsigned short u16;
typedef short bf16x8 __attribute__((ext_vector_type(8)));
typedef short s16x4 __attribute__((ext_vector_type(4)));
typedef float f32x4 __attribute__((ext_vector_type(4)));
typedef float f32x2 __attribute__((ext_vector_type(2)));
typedef unsigned u32x4 __attribute__((ext_vector_type(4)));
typedef unsigned u32x2 __attribute__((ext_vector_type(2)));
typedef __bf16 bf16v2 __attribute__((ext_vector_type(2)));
#define LAS __attribute__((address_space(3)))
#define GAS __attribute__((address_space(1)))
#define DI __device__ __forceinline__
#define RLX_AGENT __ATOMIC_RELAXED, __HIP_MEMORY_SCOPE_AGENT

constexpr int S_ = 2048, D_ = 1024, F_ = 3584;
constexpr int SLOT = 33280;
constexpr int BOFF = 16384;
constexpr int PSTR = 1056;
constexpr int RING = 4 * SLOT;
constexpr int RA = 3, RB = 6;
constexpr int BSL = 16 * PSTR;
constexpr int G1_B = RA * 16384;
constexpr int G1_RING = G1_B + RB * BSL;
constexpr int G1_WPHI = G1_RING;
constexpr int G1_BIAS = G1_RING + 4096;
constexpr int G1_DUMP = G1_RING + 5120;
constexpr int G1_LDS = G1_RING + 5120 + 256;
constexpr int G2_LDS = RING + 6144 + 256;
constexpr int NKB = 144;
constexpr int N1 = 30;
constexpr int N2 = 72 - N1;
constexpr int XSLOT = (N2 - 4) & 3;
constexpr int XPSTR = 1040;
constexpr int CBASE = XSLOT >= 2 ? 0 : 2 * SLOT;
static_assert(32 * XPSTR <= SLOT && N1 % 2 == 0, "");

constexpr size_t WS_FLAGS = 0;
constexpr size_t WS_XNF = 1u << 20;
constexpr size_t WS_AF = WS_XNF + (4u << 20);
constexpr size_t WS_PHIQ = WS_AF + (size_t)NKB * 128 * 1024;
constexpr size_t WS_KVP = WS_PHIQ + (8u << 20);
constexpr size_t WS_PART = WS_KVP + (3u << 20);
constexpr int FLAG_STRIDE = 64;
constexpr int FLAG_OCNT = 0;
constexpr int FLAG_PART = 8;
constexpr int FLAG_TMO = 8 + 128;
constexpr int FLAG_WORDS = (8 + 128 + 1) * FLAG_STRIDE;

template <int N> struct IC { static constexpr int value = N; };
#define ST_AUX 16
#define MFMA16(a, b, c) __builtin_amdgcn_mfma_f32_16x16x32_bf16((a), (b), (c), 0, 0, 0)
template <int N> DI void wait_vm() { asm volatile("s_waitcnt vmcnt(%0)" ::"i"(N) : "memory"); }
DI void wait_lgkm0() { asm volatile("" ::: "memory"); __builtin_amdgcn_s_waitcnt(0xC07F); asm volatile("" ::: "memory"); }
DI void bar() { __builtin_amdgcn_sched_barrier(0); __builtin_amdgcn_s_barrier(); __builtin_amdgcn_sched_barrier(0); }
DI void dma16(const void* g, unsigned l) {
  asm volatile("s_mov_b32 m0, %1\n\ts_nop 0\n\tglobal_load_lds_dwordx4 %0, off" ::"v"(g), "s"(l) : "memory");
}
DI void dma16s(const void* sb, unsigned voff, unsigned l) {
  asm volatile("s_mov_b32 m0, %2\n\ts_nop 0\n\tglobal_load_lds_dwordx4 %0, %1" ::"v"(voff), "s"(sb), "s"(l) : "memory");
}
DI void dma16s_x4k(const void* sb, unsigned voff, unsigned l) {
  asm volatile("s_mov_b32 m0, %2\n\ts_nop 0\n\tglobal_load_lds_dwordx4 %0, %1\n\tglobal_load_lds_dwordx4 %0, %1 offset:1024\n\t"
               "global_load_lds_dwordx4 %0, %1 offset:2048\n\tglobal_load_lds_dwordx4 %0, %1 offset:3072" ::"v"(voff), "s"(sb), "s"(l) : "memory");
}
DI void dma16s_x4k_sc1(const void* sb, unsigned voff, unsigned l) {
  asm volatile("s_mov_b32 m0, %2\n\ts_nop 0\n\tglobal_load_lds_dwordx4 %0, %1 sc1\n\tglobal_load_lds_dwordx4 %0, %1 offset:1024 sc1\n\t"
               "global_load_lds_dwordx4 %0, %1 offset:2048 sc1\n\tglobal_load_lds_dwordx4 %0, %1 offset:3072 sc1" ::"v"(voff), "s"(sb), "s"(l) : "memory");
}
DI void touch(const void* g, unsigned l) { asm volatile("s_mov_b32 m0, %1\n\ts_nop 0\n\tglobal_load_lds_dword %0, off" ::"v"(g), "s"(l) : "memory"); }
DI s16x4 trread(LAS char* p) { return __builtin_amdgcn_ds_read_tr16_b64_v4i16((LAS s16x4*)p); }
DI bf16x8 cat(s16x4 lo, s16x4 hi) { return __builtin_shufflevector(lo, hi, 0, 1, 2, 3, 4, 5, 6, 7); }
DI unsigned pk2(float lo, float hi) { f32x2 v = {lo, hi}; return __builtin_bit_cast(unsigned, __builtin_convertvector(v, bf16v2)); }
DI bf16x8 pack8(f32x4 a, f32x4 b) { u32x4 r = {pk2(a[0], a[1]), pk2(a[2], a[3]), pk2(b[0], b[1]), pk2(b[2], b[3])}; return __builtin_bit_cast(bf16x8, r); }
DI float bflo(unsigned u) { return __uint_as_float(u << 16); }
DI float bfhi(unsigned u) { return __uint_as_float(u & 0xffff0000u); }
DI float rbf(float x) { return bflo(pk2(x, x)); }
template <int CTRL> DI float dpp(float x) { return __builtin_bit_cast(float, __builtin_amdgcn_mov_dpp(__builtin_bit_cast(int, x), CTRL, 0xf, 0xf, true)); }
constexpr int XOR1 = 0xB1, XOR2 = 0x4E, HMIR = 0x141, MIR = 0x140, ROR8 = 0x128;
DI float row16_max(float x) { x = fmaxf(x, dpp<XOR1>(x)); x = fmaxf(x, dpp<XOR2>(x)); x = fmaxf(x, dpp<HMIR>(x)); return fmaxf(x, dpp<MIR>(x)); }
DI float row16_min(float x) { x = fminf(x, dpp<XOR1>(x)); x = fminf(x, dpp<XOR2>(x)); x = fminf(x, dpp<HMIR>(x)); return fminf(x, dpp<MIR>(x)); }
DI float row16_sum(float x) { x += dpp<XOR1>(x); x += dpp<XOR2>(x); x += dpp<HMIR>(x); return x + dpp<MIR>(x); }
DI float xg_max(float x) {
  auto s = __builtin_amdgcn_permlane16_swap(__float_as_uint(x), __float_as_uint(x), false, false);
  x = fmaxf(__uint_as_float(s[0]), __uint_as_float(s[1]));
  auto t = __builtin_amdgcn_permlane32_swap(__float_as_uint(x), __float_as_uint(x), false, false);
  return fmaxf(__uint_as_float(t[0]), __uint_as_float(t[1]));
}
DI float xg_min(float x) {
  auto s = __builtin_amdgcn_permlane16_swap(__float_as_uint(x), __float_as_uint(x), false, false);
  x = fminf(__uint_as_float(s[0]), __uint_as_float(s[1]));
  auto t = __builtin_amdgcn_permlane32_swap(__float_as_uint(x), __float_as_uint(x), false, false);
  return fminf(__uint_as_float(t[0]), __uint_as_float(t[1]));
}
DI float xg_sum(float x) {
  auto s = __builtin_amdgcn_permlane16_swap(__float_as_uint(x), __float_as_uint(x), false, false);
  x = __uint_as_float(s[0]) + __uint_as_float(s[1]);
  auto t = __builtin_amdgcn_permlane32_swap(__float_as_uint(x), __float_as_uint(x), false, false);
  return __uint_as_float(t[0]) + __uint_as_float(t[1]);
}
constexpr float LOG2E = 1.4426950408889634f;
#define SB() __builtin_amdgcn_sched_barrier(0)
DI u32x4 gelu8(f32x4 a, f32x4 b) {
  const float c1 = -2.0f * 0.7978845608028654f * LOG2E, c3 = c1 * 0.044715f;
  f32x4 ta = a * a, tb = b * b;
  ta = ta * c3 + c1; tb = tb * c3 + c1;
  ta = ta * a; tb = tb * b;
  SB();
#pragma unroll
  for (int r = 0; r < 4; ++r) { ta[r] = __builtin_amdgcn_exp2f(ta[r]); tb[r] = __builtin_amdgcn_exp2f(tb[r]); }
  SB();
  ta = ta + 1.0f; tb = tb + 1.0f;
  SB();
#pragma unroll
  for (int r = 0; r < 4; ++r) { ta[r] = __builtin_amdgcn_rcpf(ta[r]); tb[r] = __builtin_amdgcn_rcpf(tb[r]); }
  SB();
  return __builtin_bit_cast(u32x4, pack8(a * ta, b * tb));
}
DI void dual_softmax(f32x4 p0, f32x4 p1, f32x4& e0, f32x4& e1, f32x4& n0, f32x4& n1) {
  p0 = p0 * LOG2E; p1 = p1 * LOG2E;
  float mx = fmaxf(fmaxf(fmaxf(p0[0], p0[1]), fmaxf(p0[2], p0[3])), fmaxf(fmaxf(p1[0], p1[1]), fmaxf(p1[2], p1[3])));
  float mn = fminf(fminf(fminf(p0[0], p0[1]), fminf(p0[2], p0[3])), fminf(fminf(p1[0], p1[1]), fminf(p1[2], p1[3])));
  mx = xg_max(mx); mn = xg_min(mn);
  e0 = p0 - mx; e1 = p1 - mx; n0 = mn - p0; n1 = mn - p1;
#pragma unroll
  for (int r = 0; r < 4; ++r) {
    e0[r] = __builtin_amdgcn_exp2f(e0[r]); e1[r] = __builtin_amdgcn_exp2f(e1[r]);
    n0[r] = __builtin_amdgcn_exp2f(n0[r]); n1[r] = __builtin_amdgcn_exp2f(n1[r]);
  }
  const f32x4 se = e0 + e1, sn4 = n0 + n1;
  const float sp = xg_sum((se[0] + se[1]) + (se[2] + se[3]));
  const float sn = xg_sum((sn4[0] + sn4[1]) + (sn4[2] + sn4[3]));
  const float rp = __builtin_amdgcn_rcpf(sp), rn = __builtin_amdgcn_rcpf(sn);
  e0 = e0 * rp; e1 = e1 * rp; n0 = n0 * rn; n1 = n1 * rn;
}

#define STAMP(i) do { } while (0)
#define DBG_FIELD
#define DBG_ARG(x)
__global__ __launch_bounds__(512) void k0_ln(const u16* __restrict__ x, u16* __restrict__ xnf, unsigned* __restrict__ flags,
                                             const u16* __restrict__ wqkv, const u16* __restrict__ w1
                                             ) {
  __shared__ float red[2][8][8];
  __shared__ __attribute__((aligned(16))) unsigned dump[256];
  const int tid = threadIdx.x, w = tid >> 6, l = tid & 63, i = l & 7, c = l >> 3;
  const int row = blockIdx.x * 8 + i;
  const u32x4* src = (const u32x4*)(x + (size_t)row * D_) + 16 * w + c;
  const u32x4 v0 = src[0], v1 = src[8];
  if (blockIdx.x == 0) { for (int j = tid; j < FLAG_WORDS; j += 512) flags[j] = 0u; }
  {
    const unsigned dl = (unsigned)(size_t)(LAS unsigned*)dump;
    constexpr int ROWS = K0_PF * 2048 / 13;
    const int c0 = (blockIdx.x * 8 + __builtin_amdgcn_readfirstlane(w)) * K0_PF;
#pragma unroll
    for (int t = 0; t < K0_PF; ++t) {
      const int ck = c0 + t;
      if (ck < ROWS * 13) dma16s(ck < ROWS * 6 ? (const char*)wqkv + (size_t)ck * 1024 : (const char*)w1 + (size_t)(ck - ROWS * 6) * 1024, l * 16, dl);
    }
  }
  float f[16];
#pragma unroll
  for (int j = 0; j < 4; ++j) { f[2 * j] = bflo(v0[j]); f[2 * j + 1] = bfhi(v0[j]); f[8 + 2 * j] = bflo(v1[j]); f[9 + 2 * j] = bfhi(v1[j]); }
  float s = 0.f;
#pragma unroll
  for (int j = 0; j < 16; ++j) s += f[j];
  s += dpp<ROR8>(s); s = xg_sum(s);
  if (l < 8) red[0][w][i] = s;
  __syncthreads();
  float mean = 0.f;
#pragma unroll
  for (int j = 0; j < 8; ++j) mean += red[0][j][i];
  mean *= (1.0f / D_);
  float q = 0.f;
#pragma unroll
  for (int j = 0; j < 16; ++j) { float d = f[j] - mean; q = fmaf(d, d, q); }
  q += dpp<ROR8>(q); q = xg_sum(q);
  if (l < 8) red[1][w][i] = q;
  __syncthreads();
  float var = 0.f;
#pragma unroll
  for (int j = 0; j < 8; ++j) var += red[1][j][i];
  var *= (1.0f / D_);
  const float mb = rbf(mean);
  const float sd = rbf(sqrtf(rbf(rbf(var) + 1e-5f)));
  const float rs = 1.0f / sd;
  u32x4 o0, o1;
#pragma unroll
  for (int j = 0; j < 4; ++j) {
    o0[j] = pk2(rbf(f[2 * j] - mb) * rs, rbf(f[2 * j + 1] - mb) * rs);
    o1[j] = pk2(rbf(f[8 + 2 * j] - mb) * rs, rbf(f[9 + 2 * j] - mb) * rs);
  }
  const int mfrag = row >> 4, ri = row & 15;
  const int ch0 = 16 * w + c, ch1 = ch0 + 8;
  auto rsx = __builtin_amdgcn_make_buffer_rsrc((void*)xnf, 0, 4 << 20, 0x00020000);
  __builtin_amdgcn_raw_buffer_store_b128(o0, rsx, (((ch0 >> 2) * 128 + mfrag) * 64 + (ch0 & 3) * 16 + ri) * 16, 0, ST_AUX);
  __builtin_amdgcn_raw_buffer_store_b128(o1, rsx, (((ch1 >> 2) * 128 + mfrag) * 64 + (ch1 & 3) * 16 + ri) * 16, 0, ST_AUX);
}

struct G1Args {
  const u16* xnf; const u16* wqkv; const u16* w1; const u16* b1; const u16* wphi;
  u16* af; u16* phiq; float* kvp; const u16* w2; const u16* wo; DBG_FIELD
};

template <bool ISX, int GRP>
DI void g1_body(const G1Args& A, LAS char* lds, int rb, int j, int pos4, int wm, int lane) {
  constexpr int NF = GRP == 0 ? 7 : 6;
  [[maybe_unused]] const int wv_ = GRP * 4 + wm;
  STAMP(0);
  const int g = lane >> 4, q = (lane & 15) >> 2, p = lane & 3;
  const int hp = 2 * j + (pos4 >> 1);
  const int cp = pos4 == 0 ? 0 : pos4 == 3 ? 384 : pos4 == 1 ? 96 : 224;
  const int co = pos4 == 0 ? 64 : pos4 == 3 ? 368 : pos4 == 1 ? 80 : 352;
  const int cr = pos4 == 0 ? 0 : pos4 == 3 ? 320 : pos4 == 1 ? 64 : 192;
  constexpr int RBASE = ISX ? 256 : 128;
  const int colT = cp + 32 * (2 * GRP), colU = cp + 32 * (ISX ? GRP : 2 * GRP + 1);

  const unsigned lbase = (unsigned)(size_t)lds;
  const char* dsrc = nullptr; long dstep = 0; int dj = 0;
  const char* xsrc = (const char*)A.xnf + (size_t)(rb * 16 + 4 * wm) * 1024;
  const unsigned xoff = lane * 16;
  if (GRP == 0) {
    const int u0 = 8 * (wm & 1) + 4 * (wm >> 1), c = lane & 31, line = c >> 3, ch = c & 7, row = u0 + 16 * (lane >> 5);
    const bool fromq = ISX ? (line < 2) : (line < 1);
    const int ld = fromq ? 3 * D_ : F_;
    const u16* base = fromq ? (A.wqkv + (ISX ? (1 + line) * D_ : 0) + 64 * hp) : (A.w1 + 448 * j + cr + 64 * (line - (ISX ? 2 : 1)));
    dsrc = (const char*)(base + (size_t)row * ld + ch * 8); dstep = 64L * ld; dj = 2 * ld;
  }
  const unsigned ddst = lbase + (GRP == 0 ? G1_B + 4 * wm * PSTR : 4 * wm * 1024);
  constexpr int RD = GRP == 0 ? RB : RA;
  auto issue = [&](int slot) {
    if (GRP == 0) {
#pragma unroll
      for (int jj = 0; jj < 4; ++jj) dma16(dsrc + jj * dj, ddst + slot * BSL + jj * PSTR);
      dsrc += dstep;
    } else {
      dma16s_x4k(xsrc, xoff, ddst + slot * 16384);
      xsrc += 128 * 1024;
    }
  };
  if (GRP == 0) dma16s(A.wphi + (size_t)hp * 2048 + wm * 512, xoff, lbase + G1_WPHI + wm * 1024);
  else if (wm == 0) { const int nch = ISX ? 16 : 24; dma16s(A.b1 + 448 * j + cr, (lane < nch ? lane : 0) * 16, lbase + G1_BIAS); }
#pragma unroll
  for (int i = 0; i < RD; ++i) issue(i);
  wait_vm<4 * (RD - 1)>();
  bar();

  STAMP(1);
  f32x4 acc[4][NF];
  {
    LAS char* bp = lds + G1_BIAS + 8 * g;
#pragma unroll
    for (int f = 0; f < NF; ++f) {
      f32x4 b4 = {0.f, 0.f, 0.f, 0.f};
      if (f >= (ISX ? 4 : 2)) {
        const int col = f < 4 ? colT + 16 * (f - 2) : f < 6 ? colU + 16 * (f - 4) : co;
        const u32x2 bb = *(LAS u32x2*)(bp + (col - cr) * 2);
        b4 = (f32x4){bflo(bb[0]), bfhi(bb[0]), bflo(bb[1]), bfhi(bb[1])};
      }
#pragma unroll
      for (int m = 0; m < 4; ++m) acc[m][f] = b4;
    }
  }
  const int lb = G1_B + PSTR * (q + 4 * (g & 1)) + 512 * (g >> 1) + 8 * p;
  LAS char* pA = lds + wm * 4096 + lane * 16;
  LAS char* pS = lds + lb + 64 * GRP;
  LAS char* pT = lds + lb + (ISX ? 128 + 64 * GRP : RBASE + (colT - cr) * 2);
  LAS char* pU = lds + lb + RBASE + (colU - cr) * 2;
  LAS char* pO = lds + lb + RBASE + (co - cr) * 2;
  auto rdB = [&](int so, int f) -> bf16x8 {
    LAS char* pp = (f < 2 ? pS : f < 4 ? pT : f < 6 ? pU : pO) + so + (f < 6 ? (f & 1) * 32 : 0);
    return cat(trread(pp), trread(pp + 8 * PSTR));
  };
  bf16x8 a0[4], b0[NF], a1[4], b1[NF];
#pragma unroll
  for (int f = 0; f < NF; ++f) b0[f] = rdB(0, f);
#pragma unroll
  for (int m = 0; m < 4; ++m) a0[m] = *(LAS bf16x8*)(pA + m * 1024);
  wait_lgkm0();
  wait_vm<4 * (RD - 2)>();
  bar();
  int sa = 0, sb = 0;
  auto step = [&](bf16x8 (&a)[4], bf16x8 (&b)[NF], bf16x8 (&an)[4], bf16x8 (&bn)[NF], auto tag) {
    constexpr int T = decltype(tag)::value;
    constexpr bool ISSUE = T >= RD;
    const int sfree = GRP == 0 ? sb : sa;
    sa = sa + 1 == RA ? 0 : sa + 1; sb = sb + 1 == RB ? 0 : sb + 1;
    const int soa = sa * 16384, so = sb * BSL;
    auto mm = [&](int i) {
      const int f = i >> 2, m = i & 3;
      if (ISX && (f == 2 || f == 3)) acc[m][f] = MFMA16(a[m], b[f], acc[m][f]);
      else acc[m][f] = MFMA16(b[f], a[m], acc[m][f]);
    };
    s16x4 lo[NF];
    auto rd = [&](int i) {
      if (i < 4) an[i] = *(LAS bf16x8*)(pA + soa + i * 1024);
      else {
        const int f = (i - 4) >> 1;
        LAS char* pp = (f < 2 ? pS : f < 4 ? pT : f < 6 ? pU : pO) + so + (f < 6 ? (f & 1) * 32 : 0);
        if (((i - 4) & 1) == 0) lo[f] = trread(pp); else bn[f] = cat(lo[f], trread(pp + 8 * PSTR));
      }
    };
    if (GRP == 1) {
      if (ISSUE) issue(sfree);
      if (T > 0) {
#pragma unroll
        for (int i = 0; i < 4 + 2 * NF; ++i) rd(i);
      }
      SB();
#pragma unroll
      for (int i = 0; i < 4 * NF; ++i) mm(i);
      SB();
    } else {
#pragma unroll
      for (int i = 0; i < 4 * NF; ++i) mm(i);
      SB();
      if (T > 0) {
#pragma unroll
        for (int i = 0; i < 4 + 2 * NF; ++i) rd(i);
      }
      if (ISSUE) issue(sfree);
    }
    if (T > 0) {
      wait_lgkm0();
      if (T > 1) wait_vm<4 * ((T < RD ? T : RD) - 2)>();
      bar();
    }
  };
#pragma unroll 1
  for (int s = 0; s < 24; s += 2) { step(a0, b0, a1, b1, IC<8>{}); step(a1, b1, a0, b0, IC<8>{}); }
  step(a0, b0, a1, b1, IC<7>{}); step(a1, b1, a0, b0, IC<6>{}); step(a0, b0, a1, b1, IC<5>{}); step(a1, b1, a0, b0, IC<4>{});
  step(a0, b0, a1, b1, IC<3>{}); step(a1, b1, a0, b0, IC<2>{}); step(a0, b0, a1, b1, IC<1>{}); step(a1, b1, a0, b0, IC<0>{});

  STAMP(2);
  const int mf0 = rb * 16 + wm * 4;
  {
    auto rs = __builtin_amdgcn_make_buffer_rsrc((void*)A.af, 0, NKB * 128 * 1024, 0x00020000);
    const int o0 = (mf0 * 64 + lane) * 16;
    if (!ISX) {
      const int kb = (448 * j + colT) >> 5;
#pragma unroll
      for (int m = 0; m < 4; ++m) __builtin_amdgcn_raw_buffer_store_b128(gelu8(acc[m][2], acc[m][3]), rs, o0 + kb * 131072 + m * 1024, 0, ST_AUX);
    }
    {
      const int kb = (448 * j + colU) >> 5;
#pragma unroll
      for (int m = 0; m < 4; ++m) __builtin_amdgcn_raw_buffer_store_b128(gelu8(acc[m][4], acc[m][5]), rs, o0 + kb * 131072 + m * 1024, 0, ST_AUX);
    }
    if (GRP == 0) {
      const int kb = (448 * j + co) >> 5, n = (co >> 4) & 1;
#pragma unroll
      for (int m = 0; m < 4; m += 2) {
        const u32x4 gv = gelu8(acc[m][NF - 1], acc[m + 1][NF - 1]);
        __builtin_amdgcn_raw_buffer_store_b64((u32x2){gv[0], gv[1]}, rs, o0 + kb * 131072 + n * 8 + m * 1024, 0, ST_AUX);
        __builtin_amdgcn_raw_buffer_store_b64((u32x2){gv[2], gv[3]}, rs, o0 + kb * 131072 + n * 8 + (m + 1) * 1024, 0, ST_AUX);
      }
    }
  }
  const int head = 2 * hp + GRP;
  if (!ISX) {
    auto rs = __builtin_amdgcn_make_buffer_rsrc((void*)A.phiq, 0, 4 << 20, 0x00020000);
#pragma unroll
    for (int m = 0; m < 4; ++m)
      __builtin_amdgcn_raw_buffer_store_b128(__builtin_bit_cast(u32x4, pack8(acc[m][0], acc[m][1])), rs, lane * 16 + ((mf0 + m) * 32 + head) * 1024, 0, ST_AUX);
    const int yw = ((rb * 16 + hp) * 8 + GRP * 4 + wm) * 72;
#pragma unroll
    for (int t = 0; t < 2; ++t) {
      const int ln = yw + t * 36 + (lane < 36 ? lane : 0);
      touch(ln < 57344 ? (const char*)A.w2 + (size_t)ln * 128 : (const char*)A.wo + (size_t)(ln - 57344) * 128, lbase + G1_DUMP);
    }
  } else {
    bf16x8 wp[2];
    {
      LAS char* pw = lds + G1_WPHI + GRP * 2048 + (4 * g + q) * 64 + 8 * p;
      wp[0] = cat(trread(pw), trread(pw + 1024));
      wp[1] = cat(trread(pw + 32), trread(pw + 1024 + 32));
    }
    const f32x4 z4 = {0.f, 0.f, 0.f, 0.f};
    f32x4 p0[4], p1[4];
#pragma unroll
    for (int m = 0; m < 4; ++m) {
      const bf16x8 qb = pack8(acc[m][0], acc[m][1]);
      p0[m] = MFMA16(wp[0], qb, z4); p1[m] = MFMA16(wp[1], qb, z4);
    }
    constexpr int TS = 144;
    LAS char* tb = lds + (GRP * 4 + wm) * 12288;
    {
      LAS char* wrp = tb + (lane & 15) * TS + 8 * g;
#pragma unroll
      for (int m = 0; m < 4; ++m) {
        f32x4 e0, e1, n0, n1;
        dual_softmax(p0[m], p1[m], e0, e1, n0, n1);
        const u32x4 pe = __builtin_bit_cast(u32x4, pack8(e0, e1)), pn = __builtin_bit_cast(u32x4, pack8(n0, n1));
        *(LAS u32x2*)(wrp + m * 16 * TS) = (u32x2){pe[0], pe[1]};
        *(LAS u32x2*)(wrp + m * 16 * TS + 32) = (u32x2){pe[2], pe[3]};
        *(LAS u32x2*)(wrp + m * 16 * TS + 64) = (u32x2){pn[0], pn[1]};
        *(LAS u32x2*)(wrp + m * 16 * TS + 96) = (u32x2){pn[2], pn[3]};
      }
    }
    wait_lgkm0();
    f32x4 kv[4][3];
    const bf16x8 ones = {0x3F80, 0x3F80, 0x3F80, 0x3F80, 0x3F80, 0x3F80, 0x3F80, 0x3F80};
    LAS char* rdp = tb + (4 * g + q) * TS + 8 * p;
    bf16x8 pa[2][4];
#pragma unroll
    for (int u = 0; u < 2; ++u)
#pragma unroll
      for (int fq = 0; fq < 4; ++fq) pa[u][fq] = cat(trread(rdp + (32 * u) * TS + 32 * fq), trread(rdp + (32 * u + 16) * TS + 32 * fq));
#pragma unroll
    for (int u = 0; u < 2; ++u) {
      const bf16x8 v0 = pack8(acc[2 * u][2], acc[2 * u + 1][2]), v1 = pack8(acc[2 * u][3], acc[2 * u + 1][3]);
#pragma unroll
      for (int fq = 0; fq < 4; ++fq) {
        kv[fq][0] = MFMA16(pa[u][fq], v0, u == 0 ? z4 : kv[fq][0]);
        kv[fq][1] = MFMA16(pa[u][fq], v1, u == 0 ? z4 : kv[fq][1]);
        kv[fq][2] = MFMA16(pa[u][fq], ones, u == 0 ? z4 : kv[fq][2]);
      }
    }
    wait_lgkm0();
    LAS f32x4* rbuf = (LAS f32x4*)tb + lane;
#pragma unroll
    for (int fq = 0; fq < 4; ++fq)
#pragma unroll
      for (int dc = 0; dc < 3; ++dc) rbuf[(fq * 3 + dc) * 64] = kv[fq][dc];
    wait_lgkm0();
    bar();
    LAS f32x4* sbuf = (LAS f32x4*)(lds + GRP * 4 * 12288) + lane;
    auto rs = __builtin_amdgcn_make_buffer_rsrc((void*)A.kvp, 0, 3 << 20, 0x00020000);
#pragma unroll
    for (int t = 0; t < 3; ++t) {
      const int qd = 3 * wm + t;
      const f32x4 sv = (sbuf[qd * 64] + sbuf[768 + qd * 64]) + (sbuf[1536 + qd * 64] + sbuf[2304 + qd * 64]);
      __builtin_amdgcn_raw_buffer_store_b128(__builtin_bit_cast(u32x4, sv), rs, lane * 16 + ((rb * 32 + head) * 12 + qd) * 1024, 0, ST_AUX);
    }
  }
  STAMP(3);
  STAMP(4);
}

__global__ __launch_bounds__(512, 2) void k1_gemm1(G1Args A) {
  extern __shared__ __attribute__((aligned(16))) char smem[];
  LAS char* lds = (LAS char*)smem;
  const int wid = __builtin_amdgcn_readfirstlane(threadIdx.x >> 6), lane = threadIdx.x & 63;
  const int bid = blockIdx.x, xcd = bid & 7, idx = bid >> 3;
  const int rb = (xcd & 1) * 4 + (idx & 3), ct = (xcd >> 1) * 8 + (idx >> 2);
  const int j = ct >> 2, pos4 = ct & 3, wm = wid & 3;
  const bool isx = (pos4 == 0) || (pos4 == 3);
  if (isx) { if (wid < 4) g1_body<true, 0>(A, lds, rb, j, pos4, wm, lane); else g1_body<true, 1>(A, lds, rb, j, pos4, wm, lane); }
  else { if (wid < 4) g1_body<false, 0>(A, lds, rb, j, pos4, wm, lane); else g1_body<false, 1>(A, lds, rb, j, pos4, wm, lane); }
}

struct G2Args {
  u16* af; const u16* w2; const u16* wo; const u16* phiq; const float* kvp; float* part;
  const u16* x; const u16* b2; u16* out; unsigned* flags; const u16* wphi; DBG_FIELD
};

DI unsigned peek_flag(unsigned* flag) { return __hip_atomic_load((GAS unsigned*)flag, RLX_AGENT); }
DI void await_flag(unsigned seen, unsigned* flag, unsigned want, unsigned* tmo) {
  unsigned spins = 0;
  while ((unsigned)__builtin_amdgcn_readfirstlane(seen) < want) {
    __builtin_amdgcn_s_sleep(1);
    if (++spins > (1u << 22)) { __hip_atomic_store((GAS unsigned*)tmo, 1u, RLX_AGENT); break; }
    seen = peek_flag(flag);
  }
  __builtin_amdgcn_fence(__ATOMIC_ACQUIRE, "workgroup");
}

template <bool FIN, int GRP>
DI void g2_body(const G2Args& A, LAS char* lds, int rb, int ct, int w4, int lane) {
  constexpr int NS = FIN ? N2 : N1;
  constexpr int KB0 = FIN ? 2 * N1 : 0;
  [[maybe_unused]] const int wv_ = GRP * 4 + w4;
  STAMP(0);
  const int g = lane >> 4, q = (lane & 15) >> 2, p = lane & 3;
  const int wm = w4 & 1, wn = w4 >> 1;
  const int tile = rb * 8 + ct, wv = GRP * 4 + w4;
  const int col0 = ct * 128 + wn * 64 + 4 * g;
  u32x2 bb[4];
  if (FIN && GRP == 0) {
#pragma unroll
    for (int n = 0; n < 4; ++n) bb[n] = *(const u32x2*)(A.b2 + col0 + n * 16);
  }
  if (!FIN) {
    const int head = tile & 31, rq = tile >> 5;
    LAS char* kvl = lds + RING;
    u32x4 qf[4];
    {
      const u32x4* q4 = (const u32x4*)A.phiq + ((size_t)(rq * 32 + wv * 4) * 32 + head) * 64 + lane;
#pragma unroll
      for (int m = 0; m < 4; ++m) qf[m] = q4[m * 2048];
    }
    u16 wraw[2][8];
    {
      const u16* wsrc = A.wphi + head * 1024 + 4 * g * 32 + (lane & 15);
#pragma unroll
      for (int fc = 0; fc < 2; ++fc)
#pragma unroll
        for (int e = 0; e < 8; ++e) wraw[fc][e] = wsrc[((e & 3) + 16 * (e >> 2)) * 32 + 16 * fc];
    }
    f32x4 kp[8][2];
    const int kh = wv < 4 ? wv >> 1 : (lane >> 2) & 1, dc = wv < 4 ? wv & 1 : 2, sl = wv < 4 ? lane : (lane & 3) * 16;
    const bool kact = wv < 4 || (wv == 4 && lane < 8);
    if (kact) {
      const f32x4* src = (const f32x4*)A.kvp + ((size_t)head * 12 + (2 * kh) * 3 + dc) * 64 + sl;
#pragma unroll
      for (int r8 = 0; r8 < 8; ++r8) { kp[r8][0] = src[(size_t)r8 * 32 * 768]; kp[r8][1] = src[(size_t)r8 * 32 * 768 + 192]; }
    }
    bar();
    const f32x4 z4 = {0.f, 0.f, 0.f, 0.f};
    bf16x8 pq[4][2];
    {
      bf16x8 wp[2];
#pragma unroll
      for (int fc = 0; fc < 2; ++fc)
#pragma unroll
        for (int e = 0; e < 8; ++e) wp[fc][e] = (short)wraw[fc][e];
#pragma unroll
      for (int m = 0; m < 4; ++m) {
        const bf16x8 qb = __builtin_bit_cast(bf16x8, qf[m]);
        f32x4 e0, e1, n0, n1;
        dual_softmax(MFMA16(wp[0], qb, z4), MFMA16(wp[1], qb, z4), e0, e1, n0, n1);
        pq[m][0] = pack8(e0, e1); pq[m][1] = pack8(n0, n1);
      }
    }
    if (kact) {
      f32x4 s0 = kp[0][0], s1 = kp[0][1];
#pragma unroll
      for (int r8 = 1; r8 < 8; ++r8) { s0 += kp[r8][0]; s1 += kp[r8][1]; }
      *(LAS bf16x8*)(kvl + (kh * 3 + dc) * 1024 + (wv < 4 ? lane : lane & 3) * 16) = pack8(s0, s1);
    }
    STAMP(13);
    wait_lgkm0();
    bar();
    STAMP(14);
    bf16x8 ka[2][3];
#pragma unroll
    for (int k2 = 0; k2 < 2; ++k2)
#pragma unroll
      for (int d2 = 0; d2 < 3; ++d2) ka[k2][d2] = *(LAS bf16x8*)(kvl + (k2 * 3 + d2) * 1024 + (d2 < 2 ? lane : g) * 16);
    auto rsrc = __builtin_amdgcn_make_buffer_rsrc((void*)(A.af + (size_t)(112 + head) * 128 * 512), 0, 128 * 1024, 0x00020000);
#pragma unroll
    for (int m = 0; m < 4; ++m) {
      const bf16x8 b0 = pq[m][0], b1 = pq[m][1];
      f32x4 n0 = MFMA16(ka[0][0], b0, z4); n0 = MFMA16(ka[1][0], b1, n0);
      f32x4 n1 = MFMA16(ka[0][1], b0, z4); n1 = MFMA16(ka[1][1], b1, n1);
      f32x4 dn = MFMA16(ka[0][2], b0, z4); dn = MFMA16(ka[1][2], b1, dn);
      const float rd = __builtin_amdgcn_rcpf(rbf(rbf(dn[0]) + 1e-6f));
      f32x4 o0, o1;
#pragma unroll
      for (int r = 0; r < 4; ++r) { o0[r] = rbf(n0[r]) * rd; o1[r] = rbf(n1[r]) * rd; }
      const int mf = rq * 32 + wv * 4 + m;
      __builtin_amdgcn_raw_buffer_store_b128(__builtin_bit_cast(u32x4, pack8(o0, o1)), rsrc, (mf * 64 + lane) * 16, 0, 16);
    }
    STAMP(15);
  }
  bar();

  STAMP(1);
  f32x4 acc[4][4];
#pragma unroll
  for (int m = 0; m < 4; ++m)
#pragma unroll
    for (int n = 0; n < 4; ++n) {
      if (FIN && GRP == 0) acc[m][n] = (f32x4){bflo(bb[n][0]), bfhi(bb[n][0]), bflo(bb[n][1]), bfhi(bb[n][1])};
      else acc[m][n] = (f32x4){0.f, 0.f, 0.f, 0.f};
    }
  LAS char* pA = lds + GRP * 8192 + wm * 4096 + lane * 16;
  LAS char* pB = lds + BOFF + GRP * 8 * PSTR + PSTR * (4 * (g & 1) + q) + 256 * (g >> 1) + 8 * p + 128 * wn;
  constexpr int SO_FIRST = (112 - KB0) / 2;
  bf16x8 a0[4], b0[4], a1[4], b1[4];
#pragma unroll
  for (int n = 0; n < 4; ++n) b0[n] = cat(trread(pB + n * 32), trread(pB + n * 32 + 512));
#pragma unroll
  for (int m = 0; m < 4; ++m) a0[m] = *(LAS bf16x8*)(pA + m * 1024);
  wait_lgkm0();
  bar();

  auto step = [&](int st, bf16x8 (&a)[4], bf16x8 (&b)[4], bf16x8 (&an)[4], bf16x8 (&bn)[4], auto tag) {
    constexpr int T = decltype(tag)::value;
    const int so = ((st + 1) & 3) * SLOT;
    s16x4 lo[4];
    auto rd = [&](int i) {
      if (i < 4) an[i] = *(LAS bf16x8*)(pA + so + i * 1024);
      else {
        const int n = (i - 4) >> 1;
        LAS char* pp = pB + so + n * 32;
        if (((i - 4) & 1) == 0) lo[n] = trread(pp); else bn[n] = cat(lo[n], trread(pp + 512));
      }
    };
    if (GRP == 1 && T > 0) {
#pragma unroll
      for (int i = 0; i < 12; ++i) rd(i);
    }
    SB();
#pragma unroll
    for (int i = 0; i < 16; ++i) acc[i & 3][i >> 2] = MFMA16(b[i >> 2], a[i & 3], acc[i & 3][i >> 2]);
    SB();
    if (GRP == 0 && T > 0) {
#pragma unroll
      for (int i = 0; i < 12; ++i) rd(i);
    }
    if (T > 0) {
      wait_lgkm0();
      bar();
    }
  };
  unsigned* const ocnt = A.flags + (FLAG_OCNT + (rb >> 2)) * FLAG_STRIDE;
  unsigned* const pflag = A.flags + (FLAG_PART + tile) * FLAG_STRIDE;
  unsigned* const tmo = A.flags + FLAG_TMO * FLAG_STRIDE;
  unsigned seen = 0;
#pragma unroll 1
  for (int st = 0; st < NS - 4; st += 2) {
    if (wv == 0) {
      if (!FIN && st == 2 && lane == 0)
        __hip_atomic_fetch_add((GAS unsigned*)(A.flags + (FLAG_OCNT + (tile >> 5)) * FLAG_STRIDE), 1u, RLX_AGENT);
      if (FIN) {
        constexpr int S0 = (SO_FIRST - 8) & ~1;
        if (st == S0) seen = peek_flag(ocnt);
        if (st == S0 + 2) await_flag(seen, ocnt, 32u, tmo);
      }
    }
    step(st, a0, b0, a1, b1, IC<4>{});
    if (!FIN && st == 0) wait_vm<0>();
    step(st + 1, a1, b1, a0, b0, IC<4>{});
  }
  if (FIN && wv == 0) seen = peek_flag(pflag);
  step(NS - 4, a0, b0, a1, b1, IC<3>{}); step(NS - 3, a1, b1, a0, b0, IC<2>{});
  if (FIN && wv == 0) await_flag(seen, pflag, 1u, tmo);
  step(NS - 2, a0, b0, a1, b1, IC<1>{});
  f32x4 pv[2][4];
  if (FIN) {
    auto rsrc = __builtin_amdgcn_make_buffer_rsrc((void*)(A.part + (size_t)tile * 16384), 0, 65536, 0x00020000);
#pragma unroll
    for (int mm = 0; mm < 2; ++mm)
#pragma unroll
      for (int n = 0; n < 4; ++n)
        pv[mm][n] = __builtin_bit_cast(f32x4, __builtin_amdgcn_raw_buffer_load_b128(rsrc, ((wv * 8 + mm * 4 + n) * 64 + lane) * 16, 0, 16));
    SB();
  }
  step(NS - 1, a1, b1, a0, b0, IC<0>{});

  STAMP(2);
  {
    LAS f32x4* wb = (LAS f32x4*)(lds + CBASE + wv * 8192) + lane;
#pragma unroll
    for (int mm = 0; mm < 2; ++mm)
#pragma unroll
      for (int n = 0; n < 4; ++n) wb[(mm * 4 + n) * 64] = acc[GRP == 0 ? 2 + mm : mm][n];
  }
  wait_lgkm0();
  bar();
  f32x4 r[2][4];
  {
    LAS f32x4* rbuf = (LAS f32x4*)(lds + CBASE + ((1 - GRP) * 4 + w4) * 8192) + lane;
#pragma unroll
    for (int mm = 0; mm < 2; ++mm)
#pragma unroll
      for (int n = 0; n < 4; ++n) r[mm][n] = acc[GRP == 0 ? mm : 2 + mm][n] + rbuf[(mm * 4 + n) * 64];
  }
  if (!FIN) {
    auto rsrc = __builtin_amdgcn_make_buffer_rsrc((void*)(A.part + (size_t)tile * 16384), 0, 65536, 0x00020000);
#pragma unroll
    for (int mm = 0; mm < 2; ++mm)
#pragma unroll
      for (int n = 0; n < 4; ++n)
        __builtin_amdgcn_raw_buffer_store_b128(__builtin_bit_cast(u32x4, r[mm][n]), rsrc, ((wv * 8 + mm * 4 + n) * 64 + lane) * 16, 0, 16);
    wait_vm<0>();
    bar();
    if (wv == 0 && lane == 0) __hip_atomic_store((GAS unsigned*)(A.flags + (FLAG_PART + tile) * FLAG_STRIDE), 1u, RLX_AGENT);
  } else {
    int lz = lane; asm volatile("" : "+v"(lz));
    const int rl = wm * 64 + GRP * 32 + (lz & 15), cl = wn * 64 + 4 * (lz >> 4);
    LAS char* xl = lds + XSLOT * SLOT + (rl >> 2) * XPSTR + (rl & 3) * 256 + cl * 2;
    u16* op = A.out + (size_t)(rb * 128 + rl) * D_ + ct * 128 + cl;
#pragma unroll
    for (int mm = 0; mm < 2; ++mm)
#pragma unroll
      for (int n = 0; n < 4; ++n) {
        const u32x2 xr = *(LAS u32x2*)(xl + mm * 4 * XPSTR + n * 32);
        const f32x4 xv = {bflo(xr[0]), bfhi(xr[0]), bflo(xr[1]), bfhi(xr[1])};
        const f32x4 o = (r[mm][n] + pv[mm][n]) + xv;
        u32x2 ov = {pk2(o[0], o[1]), pk2(o[2], o[3])};
        *(u32x2*)(op + (size_t)mm * 16 * D_ + n * 16) = ov;
      }
  }
  STAMP(3);
  STAMP(4);
}

template <bool FIN>
DI void g2_loader(const G2Args& A, LAS char* lds, int rb, int ct, int w4, int lane) {
  constexpr int NS = FIN ? N2 : N1;
  constexpr int KB0 = FIN ? 2 * N1 : 0;
  const int kbs = w4 >> 1, u0 = 4 * (w4 & 1);
  const unsigned lbase = (unsigned)(size_t)lds;
  const char* asrc = (const char*)A.af + ((size_t)kbs * 128 + rb * 8 + u0) * 1024;
  const unsigned aoff = lane * 16;
  const unsigned boff = (unsigned)(((kbs * 32 + u0 + 8 * (lane >> 4)) * D_ + ct * 128 + (lane & 15) * 8) * 2);
  const unsigned adst = lbase + kbs * 8192 + u0 * 1024, bdst = lbase + BOFF + kbs * 8 * PSTR + u0 * PSTR;
  auto issue = [&](int st) {
    const int kb = KB0 + 2 * st, slot = (st & 3) * SLOT;
    const char* wb = kb < 112 ? (const char*)A.w2 + (size_t)kb * 32 * D_ * 2 : (const char*)A.wo + (size_t)(kb - 112) * 32 * D_ * 2;
    if (FIN && kb >= 112) dma16s_x4k_sc1(asrc + (size_t)kb * 128 * 1024, aoff, adst + slot);
    else dma16s_x4k(asrc + (size_t)kb * 128 * 1024, aoff, adst + slot);
#pragma unroll
    for (int jj = 0; jj < 4; ++jj) dma16s(wb + jj * (D_ * 2), boff, bdst + slot + jj * PSTR);
  };
  if (!FIN) bar();
  issue(0); issue(1); issue(2); issue(3);
  if (!FIN) bar();
  wait_vm<24>();
  bar();
  wait_vm<16>();
  bar();
#pragma unroll 1
  for (int st = 0; st < NS - 4; ++st) { issue(st + 4); wait_vm<16>(); bar(); }
  if (FIN) {
    const char* xs = (const char*)A.x + ((size_t)(rb * 128 + w4 * 32) * D_ + ct * 128) * 2;
    const unsigned xo = (unsigned)((lane >> 4) * (D_ * 2) + (lane & 15) * 16);
#pragma unroll
    for (int jj = 0; jj < 8; ++jj) dma16s(xs + jj * 4 * (D_ * 2), xo, lbase + XSLOT * SLOT + (w4 * 8 + jj) * XPSTR);
    wait_vm<16>(); bar();
    wait_vm<8>(); bar();
    wait_vm<0>(); bar();
  } else {
    wait_vm<8>(); bar();
    wait_vm<0>(); bar();
  }
}

__global__ __launch_bounds__(768) void k2_gemm2(G2Args A) {
  extern __shared__ __attribute__((aligned(16))) char smem[];
  LAS char* lds = (LAS char*)smem;
  const int wid = __builtin_amdgcn_readfirstlane(threadIdx.x >> 6), lane = threadIdx.x & 63;
  const int bid = blockIdx.x, xcd = bid & 7, idx = bid >> 3;
  const int rb = (xcd & 3) * 4 + (idx & 3), ct = (xcd >> 2) * 4 + ((idx >> 2) & 3), fin = idx >> 4;
  const int w4 = wid & 3;
  if (wid >= 8) { if (fin) g2_loader<true>(A, lds, rb, ct, w4, lane); else g2_loader<false>(A, lds, rb, ct, w4, lane); return; }
  if (fin) { if (wid < 4) g2_body<true, 0>(A, lds, rb, ct, w4, lane); else g2_body<true, 1>(A, lds, rb, ct, w4, lane); }
  else { if (wid < 4) g2_body<false, 0>(A, lds, rb, ct, w4, lane); else g2_body<false, 1>(A, lds, rb, ct, w4, lane); }
}

extern "C" void kernel_launch(void* const* d_in, const int* in_sizes, int n_in, void* d_out, int out_size, void* d_ws, size_t ws_size,
                              hipStream_t stream) {
  const u16* x = (const u16*)d_in[0];
  const u16* wqkv = (const u16*)d_in[1];
  const u16* wphi = (const u16*)d_in[2];
  const u16* wo = (const u16*)d_in[3];
  const u16* w1 = (const u16*)d_in[5];
  const u16* b1 = (const u16*)d_in[6];
  const u16* w2 = (const u16*)d_in[7];
  const u16* b2 = (const u16*)d_in[8];
  char* ws = (char*)d_ws;
  unsigned* flags = (unsigned*)(ws + WS_FLAGS);
  u16* xnf = (u16*)(ws + WS_XNF);
  u16* af = (u16*)(ws + WS_AF);
  u16* phiq = (u16*)(ws + WS_PHIQ);
  float* kvp = (float*)(ws + WS_KVP);
  float* part = (float*)(ws + WS_PART);
  (void)hipFuncSetAttribute((const void*)k1_gemm1, hipFuncAttributeMaxDynamicSharedMemorySize, G1_LDS);
  (void)hipFuncSetAttribute((const void*)k2_gemm2, hipFuncAttributeMaxDynamicSharedMemorySize, G2_LDS);
  k0_ln<<<256, 512, 0, stream>>>(x, xnf, flags, wqkv, w1 DBG_ARG(130u));
  G1Args a1 = {xnf, wqkv, w1, b1, wphi, af, phiq, kvp, w2, wo DBG_ARG(128u)};
  k1_gemm1<<<256, 512, G1_LDS, stream>>>(a1);
  G2Args a2 = {af, w2, wo, phiq, kvp, part, x, b2, (u16*)d_out, flags, wphi DBG_ARG(129u)};
  k2_gemm2<<<256, 768, G2_LDS, stream>>>(a2);
}
```

```cpp
#include <hip/hip_runtime.h>
#include <hip/hip_bf16.h>
#include <stdint.h>

typedef unsigned short u16;
typedef short bf16x8 __attribute__((ext_vector_type(8)));
typedef short s16x4 __attribute__((ext_vector_type(4)));
typedef float f32x4 __attribute__((ext_vector_type(4)));
typedef float f32x2 __attribute__((ext_vector_type(2)));
typedef unsigned u32x4 __attribute__((ext_vector_type(4)));
typedef unsigned u32x2 __attribute__((ext_vector_type(2)));
typedef __bf16 bf16v2 __attribute__((ext_vector_type(2)));
#define LAS __attribute__((address_space(3)))
#define GAS __attribute__((address_space(1)))
#define DI __device__ __forceinline__
#define RLX_AGENT __ATOMIC_RELAXED, __HIP_MEMORY_SCOPE_AGENT

constexpr int S_ = 2048, D_ = 1024, F_ = 3584;
constexpr int SLOT = 33280;
constexpr int BOFF = 16384;
constexpr int PSTR = 1056;
constexpr int RING = 4 * SLOT;
constexpr int RA = 3, RB = 6;
constexpr int BSL = 16 * PSTR;
constexpr int G1_B = RA * 16384;
constexpr int G1_RING = G1_B + RB * BSL;
constexpr int G1_WPHI = G1_RING;
constexpr int G1_BIAS = G1_RING + 4096;
constexpr int G1_DUMP = G1_RING + 5120;
constexpr int G1_LDS = G1_RING + 5120 + 256;
constexpr int G2_LDS = RING + 6144 + 256;
constexpr int NKB = 144;
constexpr int N1 = 30;
constexpr int N2 = 72 - N1;
constexpr int XSLOT = (N2 - 4) & 3;
constexpr int XPSTR = 1040;
constexpr int CBASE = XSLOT >= 2 ? 0 : 2 * SLOT;
static_assert(32 * XPSTR <= SLOT && N1 % 2 == 0, "");

constexpr size_t WS_FLAGS = 0;
constexpr size_t WS_XNF = 1u << 20;
constexpr size_t WS_AF = WS_XNF + (4u << 20);
constexpr size_t WS_PHIQ = WS_AF + (size_t)NKB * 128 * 1024;
constexpr size_t WS_KVP = WS_PHIQ + (8u << 20);
constexpr size_t WS_PART = WS_KVP + (3u << 20);
constexpr int FLAG_STRIDE = 64;
constexpr int FLAG_OCNT = 0;
constexpr int FLAG_PART = 8;
constexpr int FLAG_TMO = 8 + 128;
constexpr int FLAG_WORDS = (8 + 128 + 1) * FLAG_STRIDE;

template <int N> struct IC { static constexpr int value = N; };
#define ST_AUX 16
#define K0_PF 0
#define MFMA16(a, b, c) __builtin_amdgcn_mfma_f32_16x16x32_bf16((a), (b), (c), 0, 0, 0)
template <int N> DI void wait_vm() { asm volatile("s_waitcnt vmcnt(%0)" ::"i"(N) : "memory"); }
DI void wait_lgkm0() { asm volatile("" ::: "memory"); __builtin_amdgcn_s_waitcnt(0xC07F); asm volatile("" ::: "memory"); }
DI void bar() { __builtin_amdgcn_sched_barrier(0); __builtin_amdgcn_s_barrier(); __builtin_amdgcn_sched_barrier(0); }
DI void dma16(const void* g, unsigned l) {
  asm volatile("s_mov_b32 m0, %1\n\ts_nop 0\n\tglobal_load_lds_dwordx4 %0, off" ::"v"(g), "s"(l) : "memory");
}
DI void dma16s(const void* sb, unsigned voff, unsigned l) {
  asm volatile("s_mov_b32 m0, %2\n\ts_nop 0\n\tglobal_load_lds_dwordx4 %0, %1" ::"v"(voff), "s"(sb), "s"(l) : "memory");
}
DI void dma16s_x4k(const void* sb, unsigned voff, unsigned l) {
  asm volatile("s_mov_b32 m0, %2\n\ts_nop 0\n\tglobal_load_lds_dwordx4 %0, %1\n\tglobal_load_lds_dwordx4 %0, %1 offset:1024\n\t"
               "global_load_lds_dwordx4 %0, %1 offset:2048\n\tglobal_load_lds_dwordx4 %0, %1 offset:3072" ::"v"(voff), "s"(sb), "s"(l) : "memory");
}
DI void dma16s_x4k_sc1(const void* sb, unsigned voff, unsigned l) {
  asm volatile("s_mov_b32 m0, %2\n\ts_nop 0\n\tglobal_load_lds_dwordx4 %0, %1 sc1\n\tglobal_load_lds_dwordx4 %0, %1 offset:1024 sc1\n\t"
               "global_load_lds_dwordx4 %0, %1 offset:2048 sc1\n\tglobal_load_lds_dwordx4 %0, %1 offset:3072 sc1" ::"v"(voff), "s"(sb), "s"(l) : "memory");
}
DI void touch(const void* g, unsigned l) { asm volatile("s_mov_b32 m0, %1\n\ts_nop 0\n\tglobal_load_lds_dword %0, off" ::"v"(g), "s"(l) : "memory"); }
DI s16x4 trread(LAS char* p) { return __builtin_amdgcn_ds_read_tr16_b64_v4i16((LAS s16x4*)p); }
DI bf16x8 cat(s16x4 lo, s16x4 hi) { return __builtin_shufflevector(lo, hi, 0, 1, 2, 3, 4, 5, 6, 7); }
DI unsigned pk2(float lo, float hi) { f32x2 v = {lo, hi}; return __builtin_bit_cast(unsigned, __builtin_convertvector(v, bf16v2)); }
DI bf16x8 pack8(f32x4 a, f32x4 b) { u32x4 r = {pk2(a[0], a[1]), pk2(a[2], a[3]), pk2(b[0], b[1]), pk2(b[2], b[3])}; return __builtin_bit_cast(bf16x8, r); }
DI float bflo(unsigned u) { return __uint_as_float(u << 16); }
DI float bfhi(unsigned u) { return __uint_as_float(u & 0xffff0000u); }
DI float rbf(float x) { return bflo(pk2(x, x)); }
template <int CTRL> DI float dpp(float x) { return __builtin_bit_cast(float, __builtin_amdgcn_mov_dpp(__builtin_bit_cast(int, x), CTRL, 0xf, 0xf, true)); }
constexpr int XOR1 = 0xB1, XOR2 = 0x4E, HMIR = 0x141, MIR = 0x140, ROR8 = 0x128;
DI float row16_max(float x) { x = fmaxf(x, dpp<XOR1>(x)); x = fmaxf(x, dpp<XOR2>(x)); x = fmaxf(x, dpp<HMIR>(x)); return fmaxf(x, dpp<MIR>(x)); }
DI float row16_min(float x) { x = fminf(x, dpp<XOR1>(x)); x = fminf(x, dpp<XOR2>(x)); x = fminf(x, dpp<HMIR>(x)); return fminf(x, dpp<MIR>(x)); }
DI float row16_sum(float x) { x += dpp<XOR1>(x); x += dpp<XOR2>(x); x += dpp<HMIR>(x); return x + dpp<MIR>(x); }
DI float xg_max(float x) {
  auto s = __builtin_amdgcn_permlane16_swap(__float_as_uint(x), __float_as_uint(x), false, false);
  x = fmaxf(__uint_as_float(s[0]), __uint_as_float(s[1]));
  auto t = __builtin_amdgcn_permlane32_swap(__float_as_uint(x), __float_as_uint(x), false, false);
  return fmaxf(__uint_as_float(t[0]), __uint_as_float(t[1]));
}
DI float xg_min(float x) {
  auto s = __builtin_amdgcn_permlane16_swap(__float_as_uint(x), __float_as_uint(x), false, false);
  x = fminf(__uint_as_float(s[0]), __uint_as_float(s[1]));
  auto t = __builtin_amdgcn_permlane32_swap(__float_as_uint(x), __float_as_uint(x), false, false);
  return fminf(__uint_as_float(t[0]), __uint_as_float(t[1]));
}
DI float xg_sum(float x) {
  auto s = __builtin_amdgcn_permlane16_swap(__float_as_uint(x), __float_as_uint(x), false, false);
  x = __uint_as_float(s[0]) + __uint_as_float(s[1]);
  auto t = __builtin_amdgcn_permlane32_swap(__float_as_uint(x), __float_as_uint(x), false, false);
  return __uint_as_float(t[0]) + __uint_as_float(t[1]);
}
constexpr float LOG2E = 1.4426950408889634f;
#define SB() __builtin_amdgcn_sched_barrier(0)
DI u32x4 gelu8(f32x4 a, f32x4 b) {
  const float c1 = -2.0f * 0.7978845608028654f * LOG2E, c3 = c1 * 0.044715f;
  f32x4 ta = a * a, tb = b * b;
  ta = ta * c3 + c1; tb = tb * c3 + c1;
  ta = ta * a; tb = tb * b;
  SB();
#pragma unroll
  for (int r = 0; r < 4; ++r) { ta[r] = __builtin_amdgcn_exp2f(ta[r]); tb[r] = __builtin_amdgcn_exp2f(tb[r]); }
  SB();
  ta = ta + 1.0f; tb = tb + 1.0f;
  SB();
#pragma unroll
  for (int r = 0; r < 4; ++r) { ta[r] = __builtin_amdgcn_rcpf(ta[r]); tb[r] = __builtin_amdgcn_rcpf(tb[r]); }
  SB();
  return __builtin_bit_cast(u32x4, pack8(a * ta, b * tb));
}
DI void dual_softmax(f32x4 p0, f32x4 p1, f32x4& e0, f32x4& e1, f32x4& n0, f32x4& n1) {
  p0 = p0 * LOG2E; p1 = p1 * LOG2E;
  float mx = fmaxf(fmaxf(fmaxf(p0[0], p0[1]), fmaxf(p0[2], p0[3])), fmaxf(fmaxf(p1[0], p1[1]), fmaxf(p1[2], p1[3])));
  float mn = fminf(fminf(fminf(p0[0], p0[1]), fminf(p0[2], p0[3])), fminf(fminf(p1[0], p1[1]), fminf(p1[2], p1[3])));
  mx = xg_max(mx); mn = xg_min(mn);
  e0 = p0 - mx; e1 = p1 - mx; n0 = mn - p0; n1 = mn - p1;
#pragma unroll
  for (int r = 0; r < 4; ++r) {
    e0[r] = __builtin_amdgcn_exp2f(e0[r]); e1[r] = __builtin_amdgcn_exp2f(e1[r]);
    n0[r] = __builtin_amdgcn_exp2f(n0[r]); n1[r] = __builtin_amdgcn_exp2f(n1[r]);
  }
  const f32x4 se = e0 + e1, sn4 = n0 + n1;
  const float sp = xg_sum((se[0] + se[1]) + (se[2] + se[3]));
  const float sn = xg_sum((sn4[0] + sn4[1]) + (sn4[2] + sn4[3]));
  const float rp = __builtin_amdgcn_rcpf(sp), rn = __builtin_amdgcn_rcpf(sn);
  e0 = e0 * rp; e1 = e1 * rp; n0 = n0 * rn; n1 = n1 * rn;
}

#define STAMP(i) do { } while (0)
#define DBG_FIELD
#define DBG_ARG(x)
__global__ __launch_bounds__(512) void k0_ln(const u16* __restrict__ x, u16* __restrict__ xnf, unsigned* __restrict__ flags,
                                             const u16* __restrict__ wqkv, const u16* __restrict__ w1
                                             ) {
  __shared__ float red[2][8][8];
  __shared__ __attribute__((aligned(16))) unsigned dump[256];
  const int tid = threadIdx.x, w = tid >> 6, l = tid & 63, i = l & 7, c = l >> 3;
  const int row = blockIdx.x * 8 + i;
  const u32x4* src = (const u32x4*)(x + (size_t)row * D_) + 16 * w + c;
  const u32x4 v0 = src[0], v1 = src[8];
  if (blockIdx.x == 0) { for (int j = tid; j < FLAG_WORDS; j += 512) flags[j] = 0u; }
  float f[16];
#pragma unroll
  for (int j = 0; j < 4; ++j) { f[2 * j] = bflo(v0[j]); f[2 * j + 1] = bfhi(v0[j]); f[8 + 2 * j] = bflo(v1[j]); f[9 + 2 * j] = bfhi(v1[j]); }
  float s = 0.f;
#pragma unroll
  for (int j = 0; j < 16; ++j) s += f[j];
  s += dpp<ROR8>(s); s = xg_sum(s);
  if (l < 8) red[0][w][i] = s;
  __syncthreads();
  float mean = 0.f;
#pragma unroll
  for (int j = 0; j < 8; ++j) mean += red[0][j][i];
  mean *= (1.0f / D_);
  float q = 0.f;
#pragma unroll
  for (int j = 0; j < 16; ++j) { float d = f[j] - mean; q = fmaf(d, d, q); }
  q += dpp<ROR8>(q); q = xg_sum(q);
  if (l < 8) red[1][w][i] = q;
  __syncthreads();
  float var = 0.f;
#pragma unroll
  for (int j = 0; j < 8; ++j) var += red[1][j][i];
  var *= (1.0f / D_);
  const float mb = rbf(mean);
  const float sd = rbf(sqrtf(rbf(rbf(var) + 1e-5f)));
  const float rs = 1.0f / sd;
  u32x4 o0, o1;
#pragma unroll
  for (int j = 0; j < 4; ++j) {
    o0[j] = pk2(rbf(f[2 * j] - mb) * rs, rbf(f[2 * j + 1] - mb) * rs);
    o1[j] = pk2(rbf(f[8 + 2 * j] - mb) * rs, rbf(f[9 + 2 * j] - mb) * rs);
  }
  const int mfrag = row >> 4, ri = row & 15;
  const int ch0 = 16 * w + c, ch1 = ch0 + 8;
  auto rsx = __builtin_amdgcn_make_buffer_rsrc((void*)xnf, 0, 4 << 20, 0x00020000);
  __builtin_amdgcn_raw_buffer_store_b128(o0, rsx, (((ch0 >> 2) * 128 + mfrag) * 64 + (ch0 & 3) * 16 + ri) * 16, 0, ST_AUX);
  __builtin_amdgcn_raw_buffer_store_b128(o1, rsx, (((ch1 >> 2) * 128 + mfrag) * 64 + (ch1 & 3) * 16 + ri) * 16, 0, ST_AUX);
}

struct G1Args {
  const u16* xnf; const u16* wqkv; const u16* w1; const u16* b1; const u16* wphi;
  u16* af; u16* phiq; float* kvp; const u16* w2; const u16* wo; DBG_FIELD
};

template <bool ISX, int GRP>
DI void g1_body(const G1Args& A, LAS char* lds, int rb, int j, int pos4, int wm, int lane) {
  constexpr int NF = GRP == 0 ? 7 : 6;
  [[maybe_unused]] const int wv_ = GRP * 4 + wm;
  STAMP(0);
  const int g = lane >> 4, q = (lane & 15) >> 2, p = lane & 3;
  const int hp = 2 * j + (pos4 >> 1);
  const int cp = pos4 == 0 ? 0 : pos4 == 3 ? 384 : pos4 == 1 ? 96 : 224;
  const int co = pos4 == 0 ? 64 : pos4 == 3 ? 368 : pos4 == 1 ? 80 : 352;
  const int cr = pos4 == 0 ? 0 : pos4 == 3 ? 320 : pos4 == 1 ? 64 : 192;
  constexpr int RBASE = ISX ? 256 : 128;
  const int colT = cp + 32 * (2 * GRP), colU = cp + 32 * (ISX ? GRP : 2 * GRP + 1);

  const unsigned lbase = (unsigned)(size_t)lds;
  const char* dsrc = nullptr; long dstep = 0; int dj = 0;
  const char* xsrc = (const char*)A.xnf + (size_t)(rb * 16 + 4 * wm) * 1024;
  const unsigned xoff = lane * 16;
  if (GRP == 0) {
    const int u0 = 8 * (wm & 1) + 4 * (wm >> 1), c = lane & 31, line = c >> 3, ch = c & 7, row = u0 + 16 * (lane >> 5);
    const bool fromq = ISX ? (line < 2) : (line < 1);
    const int ld = fromq ? 3 * D_ : F_;
    const u16* base = fromq ? (A.wqkv + (ISX ? (1 + line) * D_ : 0) + 64 * hp) : (A.w1 + 448 * j + cr + 64 * (line - (ISX ? 2 : 1)));
    dsrc = (const char*)(base + (size_t)row * ld + ch * 8); dstep = 64L * ld; dj = 2 * ld;
  }
  const unsigned ddst = lbase + (GRP == 0 ? G1_B + 4 * wm * PSTR : 4 * wm * 1024);
  constexpr int RD = GRP == 0 ? RB : RA;
  auto issue = [&](int slot) {
    if (GRP == 0) {
#pragma unroll
      for (int jj = 0; jj < 4; ++jj) dma16(dsrc + jj * dj, ddst + slot * BSL + jj * PSTR);
      dsrc += dstep;
    } else {
      dma16s_x4k(xsrc, xoff, ddst + slot * 16384);
      xsrc += 128 * 1024;
    }
  };
  if (GRP == 0) dma16s(A.wphi + (size_t)hp * 2048 + wm * 512, xoff, lbase + G1_WPHI + wm * 1024);
  else if (wm == 0) { const int nch = ISX ? 16 : 24; dma16s(A.b1 + 448 * j + cr, (lane < nch ? lane : 0) * 16, lbase + G1_BIAS); }
  issue(0);
#pragma unroll
  for (int i = 1; i < RD; ++i) issue(i);
  wait_vm<4 * (RD - 1)>();
  bar();

  STAMP(1);
  f32x4 acc[4][NF];
  {
    LAS char* bp = lds + G1_BIAS + 8 * g;
#pragma unroll
    for (int f = 0; f < NF; ++f) {
      f32x4 b4 = {0.f, 0.f, 0.f, 0.f};
      if (f >= (ISX ? 4 : 2)) {
        const int col = f < 4 ? colT + 16 * (f - 2) : f < 6 ? colU + 16 * (f - 4) : co;
        const u32x2 bb = *(LAS u32x2*)(bp + (col - cr) * 2);
        b4 = (f32x4){bflo(bb[0]), bfhi(bb[0]), bflo(bb[1]), bfhi(bb[1])};
      }
#pragma unroll
      for (int m = 0; m < 4; ++m) acc[m][f] = b4;
    }
  }
  const int lb = G1_B + PSTR * (q + 4 * (g & 1)) + 512 * (g >> 1) + 8 * p;
  LAS char* pA = lds + wm * 4096 + lane * 16;
  LAS char* pS = lds + lb + 64 * GRP;
  LAS char* pT = lds + lb + (ISX ? 128 + 64 * GRP : RBASE + (colT - cr) * 2);
  LAS char* pU = lds + lb + RBASE + (colU - cr) * 2;
  LAS char* pO = lds + lb + RBASE + (co - cr) * 2;
  auto rdB = [&](int so, int f) -> bf16x8 {
    LAS char* pp = (f < 2 ? pS : f < 4 ? pT : f < 6 ? pU : pO) + so + (f < 6 ? (f & 1) * 32 : 0);
    return cat(trread(pp), trread(pp + 8 * PSTR));
  };
  bf16x8 a0[4], b0[NF], a1[4], b1[NF];
#pragma unroll
  for (int f = 0; f < NF; ++f) b0[f] = rdB(0, f);
#pragma unroll
  for (int m = 0; m < 4; ++m) a0[m] = *(LAS bf16x8*)(pA + m * 1024);
  wait_lgkm0();
  wait_vm<4 * (RD - 2)>();
  bar();
  int sa = 0, sb = 0;
  auto step = [&](bf16x8 (&a)[4], bf16x8 (&b)[NF], bf16x8 (&an)[4], bf16x8 (&bn)[NF], auto tag) {
    constexpr int T = decltype(tag)::value;
    constexpr bool ISSUE = T >= RD;
    const int sfree = GRP == 0 ? sb : sa;
    sa = sa + 1 == RA ? 0 : sa + 1; sb = sb + 1 == RB ? 0 : sb + 1;
    const int soa = sa * 16384, so = sb * BSL;
    auto mm = [&](int i) {
      const int f = i >> 2, m = i & 3;
      if (ISX && (f == 2 || f == 3)) acc[m][f] = MFMA16(a[m], b[f], acc[m][f]);
      else acc[m][f] = MFMA16(b[f], a[m], acc[m][f]);
    };
    s16x4 lo[NF];
    auto rd = [&](int i) {
      if (i < 4) an[i] = *(LAS bf16x8*)(pA + soa + i * 1024);
      else {
        const int f = (i - 4) >> 1;
        LAS char* pp = (f < 2 ? pS : f < 4 ? pT : f < 6 ? pU : pO) + so + (f < 6 ? (f & 1) * 32 : 0);
        if (((i - 4) & 1) == 0) lo[f] = trread(pp); else bn[f] = cat(lo[f], trread(pp + 8 * PSTR));
      }
    };
    if (GRP == 1) {
      if (ISSUE) issue(sfree);
      if (T > 0) {
#pragma unroll
        for (int i = 0; i < 4 + 2 * NF; ++i) rd(i);
      }
      SB();
#pragma unroll
      for (int i = 0; i < 4 * NF; ++i) mm(i);
      SB();
    } else {
#pragma unroll
      for (int i = 0; i < 4 * NF; ++i) mm(i);
      SB();
      if (T > 0) {
#pragma unroll
        for (int i = 0; i < 4 + 2 * NF; ++i) rd(i);
      }
      if (ISSUE) issue(sfree);
    }
    if (T > 0) {
      wait_lgkm0();
      if (T > 1) wait_vm<4 * ((T < RD ? T : RD) - 2)>();
      bar();
    }
  };
#pragma unroll 1
  for (int s = 0; s < 24; s += 2) { step(a0, b0, a1, b1, IC<8>{}); step(a1, b1, a0, b0, IC<8>{}); }
  step(a0, b0, a1, b1, IC<7>{}); step(a1, b1, a0, b0, IC<6>{}); step(a0, b0, a1, b1, IC<5>{}); step(a1, b1, a0, b0, IC<4>{});
  step(a0, b0, a1, b1, IC<3>{}); step(a1, b1, a0, b0, IC<2>{}); step(a0, b0, a1, b1, IC<1>{}); step(a1, b1, a0, b0, IC<0>{});

  STAMP(2);
  const int mf0 = rb * 16 + wm * 4;
  {
    auto rs = __builtin_amdgcn_make_buffer_rsrc((void*)A.af, 0, NKB * 128 * 1024, 0x00020000);
    const int o0 = (mf0 * 64 + lane) * 16;
    if (!ISX) {
      const int kb = (448 * j + colT) >> 5;
#pragma unroll
      for (int m = 0; m < 4; ++m) __builtin_amdgcn_raw_buffer_store_b128(gelu8(acc[m][2], acc[m][3]), rs, o0 + kb * 131072 + m * 1024, 0, ST_AUX);
    }
    {
      const int kb = (448 * j + colU) >> 5;
#pragma unroll
      for (int m = 0; m < 4; ++m) __builtin_amdgcn_raw_buffer_store_b128(gelu8(acc[m][4], acc[m][5]), rs, o0 + kb * 131072 + m * 1024, 0, ST_AUX);
    }
    if (GRP == 0) {
      const int kb = (448 * j + co) >> 5, n = (co >> 4) & 1;
#pragma unroll
      for (int m = 0; m < 4; m += 2) {
        const u32x4 gv = gelu8(acc[m][NF - 1], acc[m + 1][NF - 1]);
        __builtin_amdgcn_raw_buffer_store_b64((u32x2){gv[0], gv[1]}, rs, o0 + kb * 131072 + n * 8 + m * 1024, 0, ST_AUX);
        __builtin_amdgcn_raw_buffer_store_b64((u32x2){gv[2], gv[3]}, rs, o0 + kb * 131072 + n * 8 + (m + 1) * 1024, 0, ST_AUX);
      }
    }
  }
  const int head = 2 * hp + GRP;
  if (!ISX) {
    auto rs = __builtin_amdgcn_make_buffer_rsrc((void*)A.phiq, 0, 4 << 20, 0x00020000);
#pragma unroll
    for (int m = 0; m < 4; ++m)
      __builtin_amdgcn_raw_buffer_store_b128(__builtin_bit_cast(u32x4, pack8(acc[m][0], acc[m][1])), rs, lane * 16 + ((mf0 + m) * 32 + head) * 1024, 0, ST_AUX);
    const int yw = ((rb * 16 + hp) * 8 + GRP * 4 + wm) * 72;
#pragma unroll
    for (int t = 0; t < 2; ++t) {
      const int ln = yw + t * 36 + (lane < 36 ? lane : 0);
      touch(ln < 57344 ? (const char*)A.w2 + (size_t)ln * 128 : (const char*)A.wo + (size_t)(ln - 57344) * 128, lbase + G1_DUMP);
    }
  } else {
    bf16x8 wp[2];
    {
      LAS char* pw = lds + G1_WPHI + GRP * 2048 + (4 * g + q) * 64 + 8 * p;
      wp[0] = cat(trread(pw), trread(pw + 1024));
      wp[1] = cat(trread(pw + 32), trread(pw + 1024 + 32));
    }
    const f32x4 z4 = {0.f, 0.f, 0.f, 0.f};
    f32x4 p0[4], p1[4];
#pragma unroll
    for (int m = 0; m < 4; ++m) {
      const bf16x8 qb = pack8(acc[m][0], acc[m][1]);
      p0[m] = MFMA16(wp[0], qb, z4); p1[m] = MFMA16(wp[1], qb, z4);
    }
    constexpr int TS = 144;
    LAS char* tb = lds + (GRP * 4 + wm) * 12288;
    {
      LAS char* wrp = tb + (lane & 15) * TS + 8 * g;
#pragma unroll
      for (int m = 0; m < 4; ++m) {
        f32x4 e0, e1, n0, n1;
        dual_softmax(p0[m], p1[m], e0, e1, n0, n1);
        const u32x4 pe = __builtin_bit_cast(u32x4, pack8(e0, e1)), pn = __builtin_bit_cast(u32x4, pack8(n0, n1));
        *(LAS u32x2*)(wrp + m * 16 * TS) = (u32x2){pe[0], pe[1]};
        *(LAS u32x2*)(wrp + m * 16 * TS + 32) = (u32x2){pe[2], pe[3]};
        *(LAS u32x2*)(wrp + m * 16 * TS + 64) = (u32x2){pn[0], pn[1]};
        *(LAS u32x2*)(wrp + m * 16 * TS + 96) = (u32x2){pn[2], pn[3]};
      }
    }
    wait_lgkm0();
    f32x4 kv[4][3];
    const bf16x8 ones = {0x3F80, 0x3F80, 0x3F80, 0x3F80, 0x3F80, 0x3F80, 0x3F80, 0x3F80};
    LAS char* rdp = tb + (4 * g + q) * TS + 8 * p;
    bf16x8 pa[2][4];
#pragma unroll
    for (int u = 0; u < 2; ++u)
#pragma unroll
      for (int fq = 0; fq < 4; ++fq) pa[u][fq] = cat(trread(rdp + (32 * u) * TS + 32 * fq), trread(rdp + (32 * u + 16) * TS + 32 * fq));
#pragma unroll
    for (int u = 0; u < 2; ++u) {
      const bf16x8 v0 = pack8(acc[2 * u][2], acc[2 * u + 1][2]), v1 = pack8(acc[2 * u][3], acc[2 * u + 1][3]);
#pragma unroll
      for (int fq = 0; fq < 4; ++fq) {
        kv[fq][0] = MFMA16(pa[u][fq], v0, u == 0 ? z4 : kv[fq][0]);
        kv[fq][1] = MFMA16(pa[u][fq], v1, u == 0 ? z4 : kv[fq][1]);
        kv[fq][2] = MFMA16(pa[u][fq], ones, u == 0 ? z4 : kv[fq][2]);
      }
    }
    wait_lgkm0();
    LAS f32x4* rbuf = (LAS f32x4*)tb + lane;
#pragma unroll
    for (int fq = 0; fq < 4; ++fq)
#pragma unroll
      for (int dc = 0; dc < 3; ++dc) rbuf[(fq * 3 + dc) * 64] = kv[fq][dc];
    wait_lgkm0();
    bar();
    LAS f32x4* sbuf = (LAS f32x4*)(lds + GRP * 4 * 12288) + lane;
    auto rs = __builtin_amdgcn_make_buffer_rsrc((void*)A.kvp, 0, 3 << 20, 0x00020000);
#pragma unroll
    for (int t = 0; t < 3; ++t) {
      const int qd = 3 * wm + t;
      const f32x4 sv = (sbuf[qd * 64] + sbuf[768 + qd * 64]) + (sbuf[1536 + qd * 64] + sbuf[2304 + qd * 64]);
      __builtin_amdgcn_raw_buffer_store_b128(__builtin_bit_cast(u32x4, sv), rs, lane * 16 + ((rb * 32 + head) * 12 + qd) * 1024, 0, ST_AUX);
    }
  }
  STAMP(3);
  STAMP(4);
}

__global__ __launch_bounds__(512, 2) void k1_gemm1(G1Args A) {
  extern __shared__ __attribute__((aligned(16))) char smem[];
  LAS char* lds = (LAS char*)smem;
  const int wid = __builtin_amdgcn_readfirstlane(threadIdx.x >> 6), lane = threadIdx.x & 63;
  const int bid = blockIdx.x, xcd = bid & 7, idx = bid >> 3;
  const int rb = (xcd & 1) * 4 + (idx & 3), ct = (xcd >> 1) * 8 + (idx >> 2);
  const int j = ct >> 2, pos4 = ct & 3, wm = wid & 3;
  const bool isx = (pos4 == 0) || (pos4 == 3);
  if (isx) { if (wid < 4) g1_body<true, 0>(A, lds, rb, j, pos4, wm, lane); else g1_body<true, 1>(A, lds, rb, j, pos4, wm, lane); }
  else { if (wid < 4) g1_body<false, 0>(A, lds, rb, j, pos4, wm, lane); else g1_body<false, 1>(A, lds, rb, j, pos4, wm, lane); }
}

struct G2Args {
  u16* af; const u16* w2; const u16* wo; const u16* phiq; const float* kvp; float* part;
  const u16* x; const u16* b2; u16* out; unsigned* flags; const u16* wphi; DBG_FIELD
};

DI unsigned peek_flag(unsigned* flag) { return __hip_atomic_load((GAS unsigned*)flag, RLX_AGENT); }
DI void await_flag(unsigned seen, unsigned* flag, unsigned want, unsigned* tmo) {
  unsigned spins = 0;
  while ((unsigned)__builtin_amdgcn_readfirstlane(seen) < want) {
    __builtin_amdgcn_s_sleep(1);
    if (++spins > (1u << 22)) { __hip_atomic_store((GAS unsigned*)tmo, 1u, RLX_AGENT); break; }
    seen = peek_flag(flag);
  }
  __builtin_amdgcn_fence(__ATOMIC_ACQUIRE, "workgroup");
}

template <bool FIN, int GRP>
DI void g2_body(const G2Args& A, LAS char* lds, int rb, int ct, int w4, int lane) {
  constexpr int NS = FIN ? N2 : N1;
  constexpr int KB0 = FIN ? 2 * N1 : 0;
  [[maybe_unused]] const int wv_ = GRP * 4 + w4;
  STAMP(0);
  const int g = lane >> 4, q = (lane & 15) >> 2, p = lane & 3;
  const int wm = w4 & 1, wn = w4 >> 1;
  const int tile = rb * 8 + ct, wv = GRP * 4 + w4;
  const int col0 = ct * 128 + wn * 64 + 4 * g;
  u32x2 bb[4];
  if (FIN && GRP == 0) {
#pragma unroll
    for (int n = 0; n < 4; ++n) bb[n] = *(const u32x2*)(A.b2 + col0 + n * 16);
  }
  if (!FIN) {
    const int head = tile & 31, rq = tile >> 5;
    LAS char* kvl = lds + RING;
    u32x4 qf[4];
    {
      const u32x4* q4 = (const u32x4*)A.phiq + ((size_t)(rq * 32 + wv * 4) * 32 + head) * 64 + lane;
#pragma unroll
      for (int m = 0; m < 4; ++m) qf[m] = q4[m * 2048];
    }
    u16 wraw[2][8];
    {
      const u16* wsrc = A.wphi + head * 1024 + 4 * g * 32 + (lane & 15);
#pragma unroll
      for (int fc = 0; fc < 2; ++fc)
#pragma unroll
        for (int e = 0; e < 8; ++e) wraw[fc][e] = wsrc[((e & 3) + 16 * (e >> 2)) * 32 + 16 * fc];
    }
    f32x4 kp[8][2];
    const int kh = wv < 4 ? wv >> 1 : (lane >> 2) & 1, dc = wv < 4 ? wv & 1 : 2, sl = wv < 4 ? lane : (lane & 3) * 16;
    const bool kact = wv < 4 || (wv == 4 && lane < 8);
    if (kact) {
      const f32x4* src = (const f32x4*)A.kvp + ((size_t)head * 12 + (2 * kh) * 3 + dc) * 64 + sl;
#pragma unroll
      for (int r8 = 0; r8 < 8; ++r8) { kp[r8][0] = src[(size_t)r8 * 32 * 768]; kp[r8][1] = src[(size_t)r8 * 32 * 768 + 192]; }
    }
    bar();
    const f32x4 z4 = {0.f, 0.f, 0.f, 0.f};
    bf16x8 pq[4][2];
    {
      bf16x8 wp[2];
#pragma unroll
      for (int fc = 0; fc < 2; ++fc)
#pragma unroll
        for (int e = 0; e < 8; ++e) wp[fc][e] = (short)wraw[fc][e];
#pragma unroll
      for (int m = 0; m < 4; ++m) {
        const bf16x8 qb = __builtin_bit_cast(bf16x8, qf[m]);
        f32x4 e0, e1, n0, n1;
        dual_softmax(MFMA16(wp[0], qb, z4), MFMA16(wp[1], qb, z4), e0, e1, n0, n1);
        pq[m][0] = pack8(e0, e1); pq[m][1] = pack8(n0, n1);
      }
    }
    if (kact) {
      f32x4 s0 = kp[0][0], s1 = kp[0][1];
#pragma unroll
      for (int r8 = 1; r8 < 8; ++r8) { s0 += kp[r8][0]; s1 += kp[r8][1]; }
      *(LAS bf16x8*)(kvl + (kh * 3 + dc) * 1024 + (wv < 4 ? lane : lane & 3) * 16) = pack8(s0, s1);
    }
    STAMP(13);
    wait_lgkm0();
    bar();
    STAMP(14);
    bf16x8 ka[2][3];
#pragma unroll
    for (int k2 = 0; k2 < 2; ++k2)
#pragma unroll
      for (int d2 = 0; d2 < 3; ++d2) ka[k2][d2] = *(LAS bf16x8*)(kvl + (k2 * 3 + d2) * 1024 + (d2 < 2 ? lane : g) * 16);
    auto rsrc = __builtin_amdgcn_make_buffer_rsrc((void*)(A.af + (size_t)(112 + head) * 128 * 512), 0, 128 * 1024, 0x00020000);
#pragma unroll
    for (int m = 0; m < 4; ++m) {
      const bf16x8 b0 = pq[m][0], b1 = pq[m][1];
      f32x4 n0 = MFMA16(ka[0][0], b0, z4); n0 = MFMA16(ka[1][0], b1, n0);
      f32x4 n1 = MFMA16(ka[0][1], b0, z4); n1 = MFMA16(ka[1][1], b1, n1);
      f32x4 dn = MFMA16(ka[0][2], b0, z4); dn = MFMA16(ka[1][2], b1, dn);
      const float rd = __builtin_amdgcn_rcpf(rbf(rbf(dn[0]) + 1e-6f));
      f32x4 o0, o1;
#pragma unroll
      for (int r = 0; r < 4; ++r) { o0[r] = rbf(n0[r]) * rd; o1[r] = rbf(n1[r]) * rd; }
      const int mf = rq * 32 + wv * 4 + m;
      __builtin_amdgcn_raw_buffer_store_b128(__builtin_bit_cast(u32x4, pack8(o0, o1)), rsrc, (mf * 64 + lane) * 16, 0, 16);
    }
    STAMP(15);
  }
  bar();

  STAMP(1);
  f32x4 acc[4][4];
#pragma unroll
  for (int m = 0; m < 4; ++m)
#pragma unroll
    for (int n = 0; n < 4; ++n) {
      if (FIN && GRP == 0) acc[m][n] = (f32x4){bflo(bb[n][0]), bfhi(bb[n][0]), bflo(bb[n][1]), bfhi(bb[n][1])};
      else acc[m][n] = (f32x4){0.f, 0.f, 0.f, 0.f};
    }
  LAS char* pA = lds + GRP * 8192 + wm * 4096 + lane * 16;
  LAS char* pB = lds + BOFF + GRP * 8 * PSTR + PSTR * (4 * (g & 1) + q) + 256 * (g >> 1) + 8 * p + 128 * wn;
  constexpr int SO_FIRST = (112 - KB0) / 2;
  bf16x8 a0[4], b0[4], a1[4], b1[4];
#pragma unroll
  for (int n = 0; n < 4; ++n) b0[n] = cat(trread(pB + n * 32), trread(pB + n * 32 + 512));
#pragma unroll
  for (int m = 0; m < 4; ++m) a0[m] = *(LAS bf16x8*)(pA + m * 1024);
  wait_lgkm0();
  bar();

  auto step = [&](int st, bf16x8 (&a)[4], bf16x8 (&b)[4], bf16x8 (&an)[4], bf16x8 (&bn)[4], auto tag) {
    constexpr int T = decltype(tag)::value;
    const int so = ((st + 1) & 3) * SLOT;
    s16x4 lo[4];
    auto rd = [&](int i) {
      if (i < 4) an[i] = *(LAS bf16x8*)(pA + so + i * 1024);
      else {
        const int n = (i - 4) >> 1;
        LAS char* pp = pB + so + n * 32;
        if (((i - 4) & 1) == 0) lo[n] = trread(pp); else bn[n] = cat(lo[n], trread(pp + 512));
      }
    };
    if (GRP == 1 && T > 0) {
#pragma unroll
      for (int i = 0; i < 12; ++i) rd(i);
    }
    SB();
#pragma unroll
    for (int i = 0; i < 16; ++i) acc[i & 3][i >> 2] = MFMA16(b[i >> 2], a[i & 3], acc[i & 3][i >> 2]);
    SB();
    if (GRP == 0 && T > 0) {
#pragma unroll
      for (int i = 0; i < 12; ++i) rd(i);
    }
    if (T > 0) {
      wait_lgkm0();
      bar();
    }
  };
  unsigned* const ocnt = A.flags + (FLAG_OCNT + (rb >> 2)) * FLAG_STRIDE;
  unsigned* const pflag = A.flags + (FLAG_PART + tile) * FLAG_STRIDE;
  unsigned* const tmo = A.flags + FLAG_TMO * FLAG_STRIDE;
  unsigned seen = 0;
#pragma unroll 1
  for (int st = 0; st < NS - 4; st += 2) {
    if (wv == 0) {
      if (!FIN && st == 2 && lane == 0)
        __hip_atomic_fetch_add((GAS unsigned*)(A.flags + (FLAG_OCNT + (tile >> 5)) * FLAG_STRIDE), 1u, RLX_AGENT);
      if (FIN) {
        constexpr int S0 = (SO_FIRST - 8) & ~1;
        if (st == S0) seen = peek_flag(ocnt);
        if (st == S0 + 2) await_flag(seen, ocnt, 32u, tmo);
      }
    }
    step(st, a0, b0, a1, b1, IC<4>{});
    if (!FIN && st == 0) wait_vm<0>();
    step(st + 1, a1, b1, a0, b0, IC<4>{});
  }
  if (FIN && wv == 0) seen = peek_flag(pflag);
  step(NS - 4, a0, b0, a1, b1, IC<3>{}); step(NS - 3, a1, b1, a0, b0, IC<2>{});
  if (FIN && wv == 0) await_flag(seen, pflag, 1u, tmo);
  step(NS - 2, a0, b0, a1, b1, IC<1>{});
  f32x4 pv[2][4];
  if (FIN) {
    auto rsrc = __builtin_amdgcn_make_buffer_rsrc((void*)(A.part + (size_t)tile * 16384), 0, 65536, 0x00020000);
#pragma unroll
    for (int mm = 0; mm < 2; ++mm)
#pragma unroll
      for (int n = 0; n < 4; ++n)
        pv[mm][n] = __builtin_bit_cast(f32x4, __builtin_amdgcn_raw_buffer_load_b128(rsrc, ((wv * 8 + mm * 4 + n) * 64 + lane) * 16, 0, 16));
    SB();
  }
  step(NS - 1, a1, b1, a0, b0, IC<0>{});

  STAMP(2);
  {
    LAS f32x4* wb = (LAS f32x4*)(lds + CBASE + wv * 8192) + lane;
#pragma unroll
    for (int mm = 0; mm < 2; ++mm)
#pragma unroll
      for (int n = 0; n < 4; ++n) wb[(mm * 4 + n) * 64] = acc[GRP == 0 ? 2 + mm : mm][n];
  }
  wait_lgkm0();
  bar();
  f32x4 r[2][4];
  {
    LAS f32x4* rbuf = (LAS f32x4*)(lds + CBASE + ((1 - GRP) * 4 + w4) * 8192) + lane;
#pragma unroll
    for (int mm = 0; mm < 2; ++mm)
#pragma unroll
      for (int n = 0; n < 4; ++n) r[mm][n] = acc[GRP == 0 ? mm : 2 + mm][n] + rbuf[(mm * 4 + n) * 64];
  }
  if (!FIN) {
    auto rsrc = __builtin_amdgcn_make_buffer_rsrc((void*)(A.part + (size_t)tile * 16384), 0, 65536, 0x00020000);
#pragma unroll
    for (int mm = 0; mm < 2; ++mm)
#pragma unroll
      for (int n = 0; n < 4; ++n)
        __builtin_amdgcn_raw_buffer_store_b128(__builtin_bit_cast(u32x4, r[mm][n]), rsrc, ((wv * 8 + mm * 4 + n) * 64 + lane) * 16, 0, 16);
    wait_vm<0>();
    bar();
    if (wv == 0 && lane == 0) __hip_atomic_store((GAS unsigned*)(A.flags + (FLAG_PART + tile) * FLAG_STRIDE), 1u, RLX_AGENT);
  } else {
    int lz = lane; asm volatile("" : "+v"(lz));
    const int rl = wm * 64 + GRP * 32 + (lz & 15), cl = wn * 64 + 4 * (lz >> 4);
    LAS char* xl = lds + XSLOT * SLOT + (rl >> 2) * XPSTR + (rl & 3) * 256 + cl * 2;
    u16* op = A.out + (size_t)(rb * 128 + rl) * D_ + ct * 128 + cl;
#pragma unroll
    for (int mm = 0; mm < 2; ++mm)
#pragma unroll
      for (int n = 0; n < 4; ++n) {
        const u32x2 xr = *(LAS u32x2*)(xl + mm * 4 * XPSTR + n * 32);
        const f32x4 xv = {bflo(xr[0]), bfhi(xr[0]), bflo(xr[1]), bfhi(xr[1])};
        const f32x4 o = (r[mm][n] + pv[mm][n]) + xv;
        u32x2 ov = {pk2(o[0], o[1]), pk2(o[2], o[3])};
        *(u32x2*)(op + (size_t)mm * 16 * D_ + n * 16) = ov;
      }
  }
  STAMP(3);
  STAMP(4);
}

template <bool FIN>
DI void g2_loader(const G2Args& A, LAS char* lds, int rb, int ct, int w4, int lane) {
  constexpr int NS = FIN ? N2 : N1;
  constexpr int KB0 = FIN ? 2 * N1 : 0;
  const int kbs = w4 >> 1, u0 = 4 * (w4 & 1);
  const unsigned lbase = (unsigned)(size_t)lds;
  const char* asrc = (const char*)A.af + ((size_t)kbs * 128 + rb * 8 + u0) * 1024;
  const unsigned aoff = lane * 16;
  const unsigned boff = (unsigned)(((kbs * 32 + u0 + 8 * (lane >> 4)) * D_ + ct * 128 + (lane & 15) * 8) * 2);
  const unsigned adst = lbase + kbs * 8192 + u0 * 1024, bdst = lbase + BOFF + kbs * 8 * PSTR + u0 * PSTR;
  auto issue = [&](int st) {
    const int kb = KB0 + 2 * st, slot = (st & 3) * SLOT;
    const char* wb = kb < 112 ? (const char*)A.w2 + (size_t)kb * 32 * D_ * 2 : (const char*)A.wo + (size_t)(kb - 112) * 32 * D_ * 2;
    if (FIN && kb >= 112) dma16s_x4k_sc1(asrc + (size_t)kb * 128 * 1024, aoff, adst + slot);
    else dma16s_x4k(asrc + (size_t)kb * 128 * 1024, aoff, adst + slot);
#pragma unroll
    for (int jj = 0; jj < 4; ++jj) dma16s(wb + jj * (D_ * 2), boff, bdst + slot + jj * PSTR);
  };
  if (!FIN) bar();
  issue(0); issue(1); issue(2); issue(3);
  if (!FIN) bar();
  wait_vm<24>();
  bar();
  wait_vm<16>();
  bar();
#pragma unroll 1
  for (int st = 0; st < NS - 4; ++st) { issue(st + 4); wait_vm<16>(); bar(); }
  if (FIN) {
    const char* xs = (const char*)A.x + ((size_t)(rb * 128 + w4 * 32) * D_ + ct * 128) * 2;
    const unsigned xo = (unsigned)((lane >> 4) * (D_ * 2) + (lane & 15) * 16);
#pragma unroll
    for (int jj = 0; jj < 8; ++jj) dma16s(xs + jj * 4 * (D_ * 2), xo, lbase + XSLOT * SLOT + (w4 * 8 + jj) * XPSTR);
    wait_vm<16>(); bar();
    wait_vm<8>(); bar();
    wait_vm<0>(); bar();
  } else {
    wait_vm<8>(); bar();
    wait_vm<0>(); bar();
  }
}

__global__ __launch_bounds__(768) void k2_gemm2(G2Args A) {
  extern __shared__ __attribute__((aligned(16))) char smem[];
  LAS char* lds = (LAS char*)smem;
  const int wid = __builtin_amdgcn_readfirstlane(threadIdx.x >> 6), lane = threadIdx.x & 63;
  const int bid = blockIdx.x, xcd = bid & 7, idx = bid >> 3;
  const int rb = (xcd & 3) * 4 + (idx & 3), ct = (xcd >> 2) * 4 + ((idx >> 2) & 3), fin = idx >> 4;
  const int w4 = wid & 3;
  if (wid >= 8) { if (fin) g2_loader<true>(A, lds, rb, ct, w4, lane); else g2_loader<false>(A, lds, rb, ct, w4, lane); return; }
  if (fin) { if (wid < 4) g2_body<true, 0>(A, lds, rb, ct, w4, lane); else g2_body<true, 1>(A, lds, rb, ct, w4, lane); }
  else { if (wid < 4) g2_body<false, 0>(A, lds, rb, ct, w4, lane); else g2_body<false, 1>(A, lds, rb, ct, w4, lane); }
}

extern "C" void kernel_launch(void* const* d_in, const int* in_sizes, int n_in, void* d_out, int out_size, void* d_ws, size_t ws_size,
                              hipStream_t stream) {
  const u16* x = (const u16*)d_in[0];
  const u16* wqkv = (const u16*)d_in[1];
  const u16* wphi = (const u16*)d_in[2];
  const u16* wo = (const u16*)d_in[3];
  const u16* w1 = (const u16*)d_in[5];
  const u16* b1 = (const u16*)d_in[6];
  const u16* w2 = (const u16*)d_in[7];
  const u16* b2 = (const u16*)d_in[8];
  char* ws = (char*)d_ws + ((ws_size - (48u << 20)) & ~(size_t)4095);
  unsigned* flags = (unsigned*)(ws + WS_FLAGS);
  u16* xnf = (u16*)(ws + WS_XNF);
  u16* af = (u16*)(ws + WS_AF);
  u16* phiq = (u16*)(ws + WS_PHIQ);
  float* kvp = (float*)(ws + WS_KVP);
  float* part = (float*)(ws + WS_PART);
  (void)hipFuncSetAttribute((const void*)k1_gemm1, hipFuncAttributeMaxDynamicSharedMemorySize, G1_LDS);
  (void)hipFuncSetAttribute((const void*)k2_gemm2, hipFuncAttributeMaxDynamicSharedMemorySize, G2_LDS);
  k0_ln<<<256, 512, 0, stream>>>(x, xnf, flags, wqkv, w1 DBG_ARG(130u));
  G1Args a1 = {xnf, wqkv, w1, b1, wphi, af, phiq, kvp, w2, wo DBG_ARG(128u)};
  k1_gemm1<<<256, 512, G1_LDS, stream>>>(a1);
  G2Args a2 = {af, w2, wo, phiq, kvp, part, x, b2, (u16*)d_out, flags, wphi DBG_ARG(129u)};
  k2_gemm2<<<256, 768, G2_LDS, stream>>>(a2);
}
```

```cpp
#include <hip/hip_runtime.h>
#include <hip/hip_bf16.h>
#include <stdint.h>

typedef unsigned short u16;
typedef short bf16x8 __attribute__((ext_vector_type(8)));
typedef short s16x4 __attribute__((ext_vector_type(4)));
typedef float f32x4 __attribute__((ext_vector_type(4)));
typedef float f32x2 __attribute__((ext_vector_type(2)));
typedef unsigned u32x4 __attribute__((ext_vector_type(4)));
typedef unsigned u32x2 __attribute__((ext_vector_type(2)));
typedef __bf16 bf16v2 __attribute__((ext_vector_type(2)));
#define LAS __attribute__((address_space(3)))
#define GAS __attribute__((address_space(1)))
#define DI __device__ __forceinline__
#define RLX_AGENT __ATOMIC_RELAXED, __HIP_MEMORY_SCOPE_AGENT

constexpr int S_ = 2048, D_ = 1024, F_ = 3584;
constexpr int SLOT = 33280;
constexpr int BOFF = 16384;
constexpr int PSTR = 1056;
constexpr int RING = 4 * SLOT;
constexpr int RA = 3, RB = 6;
constexpr int BSL = 16 * PSTR;
constexpr int G1_B = RA * 16384;
constexpr int G1_RING = G1_B + RB * BSL;
constexpr int G1_WPHI = G1_RING;
constexpr int G1_BIAS = G1_RING + 4096;
constexpr int G1_DUMP = G1_RING + 5120;
constexpr int G1_LDS = G1_RING + 5120 + 256;
constexpr int G2_LDS = RING + 6144 + 256;
constexpr int NKB = 144;
constexpr int N1 = 30;
constexpr int N2 = 72 - N1;
constexpr int XSLOT = (N2 - 4) & 3;
constexpr int XPSTR = 1040;
constexpr int CBASE = XSLOT >= 2 ? 0 : 2 * SLOT;
static_assert(32 * XPSTR <= SLOT && N1 % 2 == 0, "");

constexpr size_t WS_FLAGS = 0;
constexpr size_t WS_XNF = 1u << 20;
constexpr size_t WS_AF = WS_XNF + (4u << 20);
constexpr size_t WS_PHIQ = WS_AF + (size_t)NKB * 128 * 1024;
constexpr size_t WS_KVP = WS_PHIQ + (8u << 20);
constexpr size_t WS_PART = WS_KVP + (3u << 20);
constexpr int FLAG_STRIDE = 64;
constexpr int FLAG_OCNT = 0;
constexpr int FLAG_PART = 8;
constexpr int FLAG_TMO = 8 + 128;
constexpr int FLAG_WORDS = (8 + 128 + 1) * FLAG_STRIDE;

template <int N> struct IC { static constexpr int value = N; };
constexpr int ST_AUX = 16;
#define MFMA16(a, b, c) __builtin_amdgcn_mfma_f32_16x16x32_bf16((a), (b), (c), 0, 0, 0)
template <int N> DI void wait_vm() { asm volatile("s_waitcnt vmcnt(%0)" ::"i"(N) : "memory"); }
DI void wait_lgkm0() { asm volatile("" ::: "memory"); __builtin_amdgcn_s_waitcnt(0xC07F); asm volatile("" ::: "memory"); }
DI void bar() { __builtin_amdgcn_sched_barrier(0); __builtin_amdgcn_s_barrier(); __builtin_amdgcn_sched_barrier(0); }
DI void dma16(const void* g, unsigned l) {
  asm volatile("s_mov_b32 m0, %1\n\ts_nop 0\n\tglobal_load_lds_dwordx4 %0, off" ::"v"(g), "s"(l) : "memory");
}
DI void dma16s(const void* sb, unsigned voff, unsigned l) {
  asm volatile("s_mov_b32 m0, %2\n\ts_nop 0\n\tglobal_load_lds_dwordx4 %0, %1" ::"v"(voff), "s"(sb), "s"(l) : "memory");
}
DI void dma16s_x4k(const void* sb, unsigned voff, unsigned l) {
  asm volatile("s_mov_b32 m0, %2\n\ts_nop 0\n\tglobal_load_lds_dwordx4 %0, %1\n\tglobal_load_lds_dwordx4 %0, %1 offset:1024\n\t"
               "global_load_lds_dwordx4 %0, %1 offset:2048\n\tglobal_load_lds_dwordx4 %0, %1 offset:3072" ::"v"(voff), "s"(sb), "s"(l) : "memory");
}
DI void dma16s_x4k_sc1(const void* sb, unsigned voff, unsigned l) {
  asm volatile("s_mov_b32 m0, %2\n\ts_nop 0\n\tglobal_load_lds_dwordx4 %0, %1 sc1\n\tglobal_load_lds_dwordx4 %0, %1 offset:1024 sc1\n\t"
               "global_load_lds_dwordx4 %0, %1 offset:2048 sc1\n\tglobal_load_lds_dwordx4 %0, %1 offset:3072 sc1" ::"v"(voff), "s"(sb), "s"(l) : "memory");
}
DI void touch(const void* g, unsigned l) { asm volatile("s_mov_b32 m0, %1\n\ts_nop 0\n\tglobal_load_lds_dword %0, off" ::"v"(g), "s"(l) : "memory"); }
DI s16x4 trread(LAS char* p) { return __builtin_amdgcn_ds_read_tr16_b64_v4i16((LAS s16x4*)p); }
DI bf16x8 cat(s16x4 lo, s16x4 hi) { return __builtin_shufflevector(lo, hi, 0, 1, 2, 3, 4, 5, 6, 7); }
DI unsigned pk2(float lo, float hi) { f32x2 v = {lo, hi}; return __builtin_bit_cast(unsigned, __builtin_convertvector(v, bf16v2)); }
DI bf16x8 pack8(f32x4 a, f32x4 b) { u32x4 r = {pk2(a[0], a[1]), pk2(a[2], a[3]), pk2(b[0], b[1]), pk2(b[2], b[3])}; return __builtin_bit_cast(bf16x8, r); }
DI float bflo(unsigned u) { return __uint_as_float(u << 16); }
DI float bfhi(unsigned u) { return __uint_as_float(u & 0xffff0000u); }
DI float rbf(float x) { return bflo(pk2(x, x)); }
template <int CTRL> DI float dpp(float x) { return __builtin_bit_cast(float, __builtin_amdgcn_mov_dpp(__builtin_bit_cast(int, x), CTRL, 0xf, 0xf, true)); }
constexpr int ROR8 = 0x128;
DI float xg_sum(float x) {
  auto s = __builtin_amdgcn_permlane16_swap(__float_as_uint(x), __float_as_uint(x), false, false);
  x = __uint_as_float(s[0]) + __uint_as_float(s[1]);
  auto t = __builtin_amdgcn_permlane32_swap(__float_as_uint(x), __float_as_uint(x), false, false);
  return __uint_as_float(t[0]) + __uint_as_float(t[1]);
}
constexpr float LOG2E = 1.4426950408889634f;
#define SB() __builtin_amdgcn_sched_barrier(0)
DI u32x4 gelu8(f32x4 a, f32x4 b) {
  const float c1 = -2.0f * 0.7978845608028654f * LOG2E, c3 = c1 * 0.044715f;
  f32x4 ta = a * a, tb = b * b;
  ta = ta * c3 + c1; tb = tb * c3 + c1;
  ta = ta * a; tb = tb * b;
  SB();
#pragma unroll
  for (int r = 0; r < 4; ++r) { ta[r] = __builtin_amdgcn_exp2f(ta[r]); tb[r] = __builtin_amdgcn_exp2f(tb[r]); }
  SB();
  ta = ta + 1.0f; tb = tb + 1.0f;
  SB();
#pragma unroll
  for (int r = 0; r < 4; ++r) { ta[r] = __builtin_amdgcn_rcpf(ta[r]); tb[r] = __builtin_amdgcn_rcpf(tb[r]); }
  SB();
  return __builtin_bit_cast(u32x4, pack8(a * ta, b * tb));
}
template <int OP> DI float rop(float a, float b) { return OP == 0 ? fmaxf(a, b) : OP == 1 ? fminf(a, b) : a + b; }
template <int OP> DI void xg_red4(float (&v)[4]) {
  auto s0 = __builtin_amdgcn_permlane16_swap(__float_as_uint(v[0]), __float_as_uint(v[1]), false, false);
  auto s1 = __builtin_amdgcn_permlane16_swap(__float_as_uint(v[2]), __float_as_uint(v[3]), false, false);
  const float x = rop<OP>(__uint_as_float(s0[0]), __uint_as_float(s0[1])), y = rop<OP>(__uint_as_float(s1[0]), __uint_as_float(s1[1]));
  auto t = __builtin_amdgcn_permlane32_swap(__float_as_uint(x), __float_as_uint(y), false, false);
  const unsigned e = __float_as_uint(rop<OP>(__uint_as_float(t[0]), __uint_as_float(t[1])));
  auto u = __builtin_amdgcn_permlane32_swap(e, e, false, false);
  auto b0 = __builtin_amdgcn_permlane16_swap(u[0], u[0], false, false);
  auto b1 = __builtin_amdgcn_permlane16_swap(u[1], u[1], false, false);
  v[0] = __uint_as_float(b0[0]); v[1] = __uint_as_float(b0[1]); v[2] = __uint_as_float(b1[0]); v[3] = __uint_as_float(b1[1]);
}
DI void dual_softmax4(f32x4 (&p0)[4], f32x4 (&p1)[4], bf16x8 (&pe)[4], bf16x8 (&pn)[4]) {
  float mx[4], mn[4], sp[4], sn[4];
#pragma unroll
  for (int m = 0; m < 4; ++m) {
    p0[m] = p0[m] * LOG2E; p1[m] = p1[m] * LOG2E;
    mx[m] = fmaxf(fmaxf(fmaxf(p0[m][0], p0[m][1]), fmaxf(p0[m][2], p0[m][3])), fmaxf(fmaxf(p1[m][0], p1[m][1]), fmaxf(p1[m][2], p1[m][3])));
    mn[m] = fminf(fminf(fminf(p0[m][0], p0[m][1]), fminf(p0[m][2], p0[m][3])), fminf(fminf(p1[m][0], p1[m][1]), fminf(p1[m][2], p1[m][3])));
  }
  xg_red4<0>(mx); xg_red4<1>(mn);
  f32x4 n0[4], n1[4];
#pragma unroll
  for (int m = 0; m < 4; ++m) {
    n0[m] = mn[m] - p0[m]; n1[m] = mn[m] - p1[m]; p0[m] = p0[m] - mx[m]; p1[m] = p1[m] - mx[m];
#pragma unroll
    for (int r = 0; r < 4; ++r) {
      p0[m][r] = __builtin_amdgcn_exp2f(p0[m][r]); p1[m][r] = __builtin_amdgcn_exp2f(p1[m][r]);
      n0[m][r] = __builtin_amdgcn_exp2f(n0[m][r]); n1[m][r] = __builtin_amdgcn_exp2f(n1[m][r]);
    }
    const f32x4 se = p0[m] + p1[m], sq = n0[m] + n1[m];
    sp[m] = (se[0] + se[1]) + (se[2] + se[3]); sn[m] = (sq[0] + sq[1]) + (sq[2] + sq[3]);
  }
  xg_red4<2>(sp); xg_red4<2>(sn);
#pragma unroll
  for (int m = 0; m < 4; ++m) {
    const float rp = __builtin_amdgcn_rcpf(sp[m]), rn = __builtin_amdgcn_rcpf(sn[m]);
    pe[m] = pack8(p0[m] * rp, p1[m] * rp); pn[m] = pack8(n0[m] * rn, n1[m] * rn);
  }
}

__global__ __launch_bounds__(512) void k0_ln(const u16* __restrict__ x, u16* __restrict__ xnf, unsigned* __restrict__ flags) {
  __shared__ float red[2][8][8];
  const int tid = threadIdx.x, w = tid >> 6, l = tid & 63, i = l & 7, c = l >> 3;
  const int row = blockIdx.x * 8 + i;
  const u32x4* src = (const u32x4*)(x + (size_t)row * D_) + 16 * w + c;
  const u32x4 v0 = src[0], v1 = src[8];
  if (blockIdx.x == 0) { for (int j = tid; j < FLAG_WORDS; j += 512) flags[j] = 0u; }
  float f[16];
#pragma unroll
  for (int j = 0; j < 4; ++j) { f[2 * j] = bflo(v0[j]); f[2 * j + 1] = bfhi(v0[j]); f[8 + 2 * j] = bflo(v1[j]); f[9 + 2 * j] = bfhi(v1[j]); }
  float s = 0.f;
#pragma unroll
  for (int j = 0; j < 16; ++j) s += f[j];
  s += dpp<ROR8>(s); s = xg_sum(s);
  if (l < 8) red[0][w][i] = s;
  __syncthreads();
  float mean = 0.f;
#pragma unroll
  for (int j = 0; j < 8; ++j) mean += red[0][j][i];
  mean *= (1.0f / D_);
  float q = 0.f;
#pragma unroll
  for (int j = 0; j < 16; ++j) { float d = f[j] - mean; q = fmaf(d, d, q); }
  q += dpp<ROR8>(q); q = xg_sum(q);
  if (l < 8) red[1][w][i] = q;
  __syncthreads();
  float var = 0.f;
#pragma unroll
  for (int j = 0; j < 8; ++j) var += red[1][j][i];
  var *= (1.0f / D_);
  const float mb = rbf(mean);
  const float sd = rbf(sqrtf(rbf(rbf(var) + 1e-5f)));
  const float rs = 1.0f / sd;
  u32x4 o0, o1;
#pragma unroll
  for (int j = 0; j < 4; ++j) {
    o0[j] = pk2(rbf(f[2 * j] - mb) * rs, rbf(f[2 * j + 1] - mb) * rs);
    o1[j] = pk2(rbf(f[8 + 2 * j] - mb) * rs, rbf(f[9 + 2 * j] - mb) * rs);
  }
  const int mfrag = row >> 4, ri = row & 15;
  const int ch0 = 16 * w + c, ch1 = ch0 + 8;
  auto rsx = __builtin_amdgcn_make_buffer_rsrc((void*)xnf, 0, 4 << 20, 0x00020000);
  __builtin_amdgcn_raw_buffer_store_b128(o0, rsx, (((ch0 >> 2) * 128 + mfrag) * 64 + (ch0 & 3) * 16 + ri) * 16, 0, ST_AUX);
  __builtin_amdgcn_raw_buffer_store_b128(o1, rsx, (((ch1 >> 2) * 128 + mfrag) * 64 + (ch1 & 3) * 16 + ri) * 16, 0, ST_AUX);
}

struct G1Args {
  const u16* xnf; const u16* wqkv; const u16* w1; const u16* b1; const u16* wphi;
  u16* af; u16* phiq; float* kvp; const u16* w2; const u16* wo;
};

template <bool ISX, int GRP>
DI void g1_body(const G1Args& A, LAS char* lds, int rb, int j, int pos4, int wm, int lane) {
  constexpr int NF = GRP == 0 ? 7 : 6;
  const int g = lane >> 4, q = (lane & 15) >> 2, p = lane & 3;
  const int hp = 2 * j + (pos4 >> 1);
  const int cp = pos4 == 0 ? 0 : pos4 == 3 ? 384 : pos4 == 1 ? 96 : 224;
  const int co = pos4 == 0 ? 64 : pos4 == 3 ? 368 : pos4 == 1 ? 80 : 352;
  const int cr = pos4 == 0 ? 0 : pos4 == 3 ? 320 : pos4 == 1 ? 64 : 192;
  constexpr int RBASE = ISX ? 256 : 128;
  const int colT = cp + 32 * (2 * GRP), colU = cp + 32 * (ISX ? GRP : 2 * GRP + 1);

  const unsigned lbase = (unsigned)(size_t)lds;
  const char* dsrc = nullptr; long dstep = 0; int dj = 0;
  const char* xsrc = (const char*)A.xnf + (size_t)(rb * 16 + 4 * wm) * 1024;
  const unsigned xoff = lane * 16;
  if (GRP == 0) {
    const int u0 = 8 * (wm & 1) + 4 * (wm >> 1), c = lane & 31, line = c >> 3, ch = c & 7, row = u0 + 16 * (lane >> 5);
    const bool fromq = ISX ? (line < 2) : (line < 1);
    const int ld = fromq ? 3 * D_ : F_;
    const u16* base = fromq ? (A.wqkv + (ISX ? (1 + line) * D_ : 0) + 64 * hp) : (A.w1 + 448 * j + cr + 64 * (line - (ISX ? 2 : 1)));
    dsrc = (const char*)(base + (size_t)row * ld + ch * 8); dstep = 64L * ld; dj = 2 * ld;
  }
  const unsigned ddst = lbase + (GRP == 0 ? G1_B + 4 * wm * PSTR : 4 * wm * 1024);
  constexpr int RD = GRP == 0 ? RB : RA;
  auto issue = [&](int slot) {
    if (GRP == 0) {
#pragma unroll
      for (int jj = 0; jj < 4; ++jj) dma16(dsrc + jj * dj, ddst + slot * BSL + jj * PSTR);
      dsrc += dstep;
    } else {
      dma16s_x4k(xsrc, xoff, ddst + slot * 16384);
      xsrc += 128 * 1024;
    }
  };
  if (GRP == 0) dma16s(A.wphi + (size_t)hp * 2048 + wm * 512, xoff, lbase + G1_WPHI + wm * 1024);
  else if (wm == 0) { const int nch = ISX ? 16 : 24; dma16s(A.b1 + 448 * j + cr, (lane < nch ? lane : 0) * 16, lbase + G1_BIAS); }
  issue(0);
#pragma unroll
  for (int i = 1; i < RD; ++i) issue(i);
  wait_vm<4 * (RD - 1)>();
  bar();

  f32x4 acc[4][NF];
  {
    LAS char* bp = lds + G1_BIAS + 8 * g;
#pragma unroll
    for (int f = 0; f < NF; ++f) {
      f32x4 b4 = {0.f, 0.f, 0.f, 0.f};
      if (f >= (ISX ? 4 : 2)) {
        const int col = f < 4 ? colT + 16 * (f - 2) : f < 6 ? colU + 16 * (f - 4) : co;
        const u32x2 bb = *(LAS u32x2*)(bp + (col - cr) * 2);
        b4 = (f32x4){bflo(bb[0]), bfhi(bb[0]), bflo(bb[1]), bfhi(bb[1])};
      }
#pragma unroll
      for (int m = 0; m < 4; ++m) acc[m][f] = b4;
    }
  }
  const int lb = G1_B + PSTR * (q + 4 * (g & 1)) + 512 * (g >> 1) + 8 * p;
  LAS char* pA = lds + wm * 4096 + lane * 16;
  LAS char* pS = lds + lb + 64 * GRP;
  LAS char* pT = lds + lb + (ISX ? 128 + 64 * GRP : RBASE + (colT - cr) * 2);
  LAS char* pU = lds + lb + RBASE + (colU - cr) * 2;
  LAS char* pO = lds + lb + RBASE + (co - cr) * 2;
  auto rdB = [&](int so, int f) -> bf16x8 {
    LAS char* pp = (f < 2 ? pS : f < 4 ? pT : f < 6 ? pU : pO) + so + (f < 6 ? (f & 1) * 32 : 0);
    return cat(trread(pp), trread(pp + 8 * PSTR));
  };
  bf16x8 a0[4], b0[NF], a1[4], b1[NF];
#pragma unroll
  for (int f = 0; f < NF; ++f) b0[f] = rdB(0, f);
#pragma unroll
  for (int m = 0; m < 4; ++m) a0[m] = *(LAS bf16x8*)(pA + m * 1024);
  wait_lgkm0();
  wait_vm<4 * (RD - 2)>();
  bar();
  int sa = 0, sb = 0;
  auto step = [&](bf16x8 (&a)[4], bf16x8 (&b)[NF], bf16x8 (&an)[4], bf16x8 (&bn)[NF], auto tag) {
    constexpr int T = decltype(tag)::value;
    constexpr bool ISSUE = T >= RD;
    const int sfree = GRP == 0 ? sb : sa;
    sa = sa + 1 == RA ? 0 : sa + 1; sb = sb + 1 == RB ? 0 : sb + 1;
    const int soa = sa * 16384, so = sb * BSL;
    auto mm = [&](int i) {
      const int f = i >> 2, m = i & 3;
      if (ISX && (f == 2 || f == 3)) acc[m][f] = MFMA16(a[m], b[f], acc[m][f]);
      else acc[m][f] = MFMA16(b[f], a[m], acc[m][f]);
    };
    s16x4 lo[NF];
    auto rd = [&](int i) {
      if (i < 4) an[i] = *(LAS bf16x8*)(pA + soa + i * 1024);
      else {
        const int f = (i - 4) >> 1;
        LAS char* pp = (f < 2 ? pS : f < 4 ? pT : f < 6 ? pU : pO) + so + (f < 6 ? (f & 1) * 32 : 0);
        if (((i - 4) & 1) == 0) lo[f] = trread(pp); else bn[f] = cat(lo[f], trread(pp + 8 * PSTR));
      }
    };
    if (GRP == 1) {
      if (ISSUE) issue(sfree);
      if (T > 0) {
#pragma unroll
        for (int i = 0; i < 4 + 2 * NF; ++i) rd(i);
      }
      SB();
#pragma unroll
      for (int i = 0; i < 4 * NF; ++i) mm(i);
      SB();
    } else {
#pragma unroll
      for (int i = 0; i < 4 * NF; ++i) mm(i);
      SB();
      if (T > 0) {
#pragma unroll
        for (int i = 0; i < 4 + 2 * NF; ++i) rd(i);
      }
      if (ISSUE) issue(sfree);
    }
    if (T > 0) {
      wait_lgkm0();
      if (T > 1) wait_vm<4 * ((T < RD ? T : RD) - 2)>();
      bar();
    }
  };
#pragma unroll 1
  for (int s = 0; s < 24; s += 2) { step(a0, b0, a1, b1, IC<8>{}); step(a1, b1, a0, b0, IC<8>{}); }
  step(a0, b0, a1, b1, IC<7>{}); step(a1, b1, a0, b0, IC<6>{}); step(a0, b0, a1, b1, IC<5>{}); step(a1, b1, a0, b0, IC<4>{});
  step(a0, b0, a1, b1, IC<3>{}); step(a1, b1, a0, b0, IC<2>{}); step(a0, b0, a1, b1, IC<1>{}); step(a1, b1, a0, b0, IC<0>{});

  const int mf0 = rb * 16 + wm * 4;
  {
    auto rs = __builtin_amdgcn_make_buffer_rsrc((void*)A.af, 0, NKB * 128 * 1024, 0x00020000);
    const int o0 = (mf0 * 64 + lane) * 16;
    if (!ISX) {
      const int kb = (448 * j + colT) >> 5;
#pragma unroll
      for (int m = 0; m < 4; ++m) __builtin_amdgcn_raw_buffer_store_b128(gelu8(acc[m][2], acc[m][3]), rs, o0 + kb * 131072 + m * 1024, 0, ST_AUX);
    }
    {
      const int kb = (448 * j + colU) >> 5;
#pragma unroll
      for (int m = 0; m < 4; ++m) __builtin_amdgcn_raw_buffer_store_b128(gelu8(acc[m][4], acc[m][5]), rs, o0 + kb * 131072 + m * 1024, 0, ST_AUX);
    }
    if (GRP == 0) {
      const int kb = (448 * j + co) >> 5, n = (co >> 4) & 1;
#pragma unroll
      for (int m = 0; m < 4; m += 2) {
        const u32x4 gv = gelu8(acc[m][NF - 1], acc[m + 1][NF - 1]);
        __builtin_amdgcn_raw_buffer_store_b64((u32x2){gv[0], gv[1]}, rs, o0 + kb * 131072 + n * 8 + m * 1024, 0, ST_AUX);
        __builtin_amdgcn_raw_buffer_store_b64((u32x2){gv[2], gv[3]}, rs, o0 + kb * 131072 + n * 8 + (m + 1) * 1024, 0, ST_AUX);
      }
    }
  }
  const int head = 2 * hp + GRP;
  if (!ISX) {
    auto rs = __builtin_amdgcn_make_buffer_rsrc((void*)A.phiq, 0, 4 << 20, 0x00020000);
#pragma unroll
    for (int m = 0; m < 4; ++m)
      __builtin_amdgcn_raw_buffer_store_b128(__builtin_bit_cast(u32x4, pack8(acc[m][0], acc[m][1])), rs, lane * 16 + ((mf0 + m) * 32 + head) * 1024, 0, ST_AUX);
    const int yw = ((rb * 16 + hp) * 8 + GRP * 4 + wm) * 72;
#pragma unroll
    for (int t = 0; t < 2; ++t) {
      const int ln = yw + t * 36 + (lane < 36 ? lane : 0);
      touch(ln < 57344 ? (const char*)A.w2 + (size_t)ln * 128 : (const char*)A.wo + (size_t)(ln - 57344) * 128, lbase + G1_DUMP);
    }
  } else {
    bf16x8 wp[2];
    {
      LAS char* pw = lds + G1_WPHI + GRP * 2048 + (4 * g + q) * 64 + 8 * p;
      wp[0] = cat(trread(pw), trread(pw + 1024));
      wp[1] = cat(trread(pw + 32), trread(pw + 1024 + 32));
    }
    const f32x4 z4 = {0.f, 0.f, 0.f, 0.f};
    f32x4 p0[4], p1[4];
#pragma unroll
    for (int m = 0; m < 4; ++m) {
      const bf16x8 qb = pack8(acc[m][0], acc[m][1]);
      p0[m] = MFMA16(wp[0], qb, z4); p1[m] = MFMA16(wp[1], qb, z4);
    }
    constexpr int TS = 144;
    LAS char* tb = lds + (GRP * 4 + wm) * 12288;
    {
      LAS char* wrp = tb + (lane & 15) * TS + 8 * g;
      bf16x8 fe[4], fn[4];
      dual_softmax4(p0, p1, fe, fn);
#pragma unroll
      for (int m = 0; m < 4; ++m) {
        const u32x4 pe = __builtin_bit_cast(u32x4, fe[m]), pn = __builtin_bit_cast(u32x4, fn[m]);
        *(LAS u32x2*)(wrp + m * 16 * TS) = (u32x2){pe[0], pe[1]};
        *(LAS u32x2*)(wrp + m * 16 * TS + 32) = (u32x2){pe[2], pe[3]};
        *(LAS u32x2*)(wrp + m * 16 * TS + 64) = (u32x2){pn[0], pn[1]};
        *(LAS u32x2*)(wrp + m * 16 * TS + 96) = (u32x2){pn[2], pn[3]};
      }
    }
    wait_lgkm0();
    f32x4 kv[4][3];
    const bf16x8 ones = {0x3F80, 0x3F80, 0x3F80, 0x3F80, 0x3F80, 0x3F80, 0x3F80, 0x3F80};
    LAS char* rdp = tb + (4 * g + q) * TS + 8 * p;
    bf16x8 pa[2][4];
#pragma unroll
    for (int u = 0; u < 2; ++u)
#pragma unroll
      for (int fq = 0; fq < 4; ++fq) pa[u][fq] = cat(trread(rdp + (32 * u) * TS + 32 * fq), trread(rdp + (32 * u + 16) * TS + 32 * fq));
#pragma unroll
    for (int u = 0; u < 2; ++u) {
      const bf16x8 v0 = pack8(acc[2 * u][2], acc[2 * u + 1][2]), v1 = pack8(acc[2 * u][3], acc[2 * u + 1][3]);
#pragma unroll
      for (int fq = 0; fq < 4; ++fq) {
        kv[fq][0] = MFMA16(pa[u][fq], v0, u == 0 ? z4 : kv[fq][0]);
        kv[fq][1] = MFMA16(pa[u][fq], v1, u == 0 ? z4 : kv[fq][1]);
        kv[fq][2] = MFMA16(pa[u][fq], ones, u == 0 ? z4 : kv[fq][2]);
      }
    }
    wait_lgkm0();
    LAS f32x4* rbuf = (LAS f32x4*)tb + lane;
#pragma unroll
    for (int fq = 0; fq < 4; ++fq)
#pragma unroll
      for (int dc = 0; dc < 3; ++dc) rbuf[(fq * 3 + dc) * 64] = kv[fq][dc];
    wait_lgkm0();
    bar();
    LAS f32x4* sbuf = (LAS f32x4*)(lds + GRP * 4 * 12288) + lane;
    auto rs = __builtin_amdgcn_make_buffer_rsrc((void*)A.kvp, 0, 3 << 20, 0x00020000);
#pragma unroll
    for (int t = 0; t < 3; ++t) {
      const int qd = 3 * wm + t;
      const f32x4 sv = (sbuf[qd * 64] + sbuf[768 + qd * 64]) + (sbuf[1536 + qd * 64] + sbuf[2304 + qd * 64]);
      __builtin_amdgcn_raw_buffer_store_b128(__builtin_bit_cast(u32x4, sv), rs, lane * 16 + ((rb * 32 + head) * 12 + qd) * 1024, 0, ST_AUX);
    }
  }
}

__global__ __launch_bounds__(512, 2) void k1_gemm1(G1Args A) {
  extern __shared__ __attribute__((aligned(16))) char smem[];
  LAS char* lds = (LAS char*)smem;
  const int wid = __builtin_amdgcn_readfirstlane(threadIdx.x >> 6), lane = threadIdx.x & 63;
  const int bid = blockIdx.x, xcd = bid & 7, idx = bid >> 3;
  const int rb = (xcd & 1) * 4 + (idx & 3), ct = (xcd >> 1) * 8 + (idx >> 2);
  const int j = ct >> 2, pos4 = ct & 3, wm = wid & 3;
  const bool isx = (pos4 == 0) || (pos4 == 3);
  if (isx) { if (wid < 4) g1_body<true, 0>(A, lds, rb, j, pos4, wm, lane); else g1_body<true, 1>(A, lds, rb, j, pos4, wm, lane); }
  else { if (wid < 4) g1_body<false, 0>(A, lds, rb, j, pos4, wm, lane); else g1_body<false, 1>(A, lds, rb, j, pos4, wm, lane); }
}

struct G2Args {
  u16* af; const u16* w2; const u16* wo; const u16* phiq; const float* kvp; float* part;
  const u16* x; const u16* b2; u16* out; unsigned* flags; const u16* wphi;
};

DI unsigned peek_flag(unsigned* flag) { return __hip_atomic_load((GAS unsigned*)flag, RLX_AGENT); }
DI void await_flag(unsigned seen, unsigned* flag, unsigned want, unsigned* tmo) {
  unsigned spins = 0;
  while ((unsigned)__builtin_amdgcn_readfirstlane(seen) < want) {
    __builtin_amdgcn_s_sleep(1);
    if (++spins > (1u << 18)) { __hip_atomic_store((GAS unsigned*)tmo, 1u, RLX_AGENT); break; }
    seen = peek_flag(flag);
  }
  __builtin_amdgcn_fence(__ATOMIC_ACQUIRE, "workgroup");
}

template <bool FIN, int GRP>
DI void g2_body(const G2Args& A, LAS char* lds, int rb, int ct, int w4, int lane) {
  constexpr int NS = FIN ? N2 : N1;
  constexpr int KB0 = FIN ? 2 * N1 : 0;
  const int g = lane >> 4, q = (lane & 15) >> 2, p = lane & 3;
  const int wm = w4 & 1, wn = w4 >> 1;
  const int tile = rb * 8 + ct, wv = GRP * 4 + w4;
  const int col0 = ct * 128 + wn * 64 + 4 * g;
  u32x2 bb[4];
  if (FIN && GRP == 0) {
#pragma unroll
    for (int n = 0; n < 4; ++n) bb[n] = *(const u32x2*)(A.b2 + col0 + n * 16);
  }
  if (!FIN) {
    const int head = tile & 31, rq = tile >> 5;
    LAS char* kvl = lds + RING;
    u32x4 qf[4];
    {
      const u32x4* q4 = (const u32x4*)A.phiq + ((size_t)(rq * 32 + wv * 4) * 32 + head) * 64 + lane;
#pragma unroll
      for (int m = 0; m < 4; ++m) qf[m] = q4[m * 2048];
    }
    u16 wraw[2][8];
    {
      const u16* wsrc = A.wphi + head * 1024 + 4 * g * 32 + (lane & 15);
#pragma unroll
      for (int fc = 0; fc < 2; ++fc)
#pragma unroll
        for (int e = 0; e < 8; ++e) wraw[fc][e] = wsrc[((e & 3) + 16 * (e >> 2)) * 32 + 16 * fc];
    }
    f32x4 kp[8][2];
    const int kh = wv < 4 ? wv >> 1 : (lane >> 2) & 1, dc = wv < 4 ? wv & 1 : 2, sl = wv < 4 ? lane : (lane & 3) * 16;
    const bool kact = wv < 4 || (wv == 4 && lane < 8);
    if (kact) {
      const f32x4* src = (const f32x4*)A.kvp + ((size_t)head * 12 + (2 * kh) * 3 + dc) * 64 + sl;
#pragma unroll
      for (int r8 = 0; r8 < 8; ++r8) { kp[r8][0] = src[(size_t)r8 * 32 * 768]; kp[r8][1] = src[(size_t)r8 * 32 * 768 + 192]; }
    }
    bar();
    const f32x4 z4 = {0.f, 0.f, 0.f, 0.f};
    bf16x8 pqe[4], pqn[4];
    {
      bf16x8 wp[2];
#pragma unroll
      for (int fc = 0; fc < 2; ++fc)
#pragma unroll
        for (int e = 0; e < 8; ++e) wp[fc][e] = (short)wraw[fc][e];
      f32x4 p0[4], p1[4];
#pragma unroll
      for (int m = 0; m < 4; ++m) {
        const bf16x8 qb = __builtin_bit_cast(bf16x8, qf[m]);
        p0[m] = MFMA16(wp[0], qb, z4); p1[m] = MFMA16(wp[1], qb, z4);
      }
      dual_softmax4(p0, p1, pqe, pqn);
    }
    if (kact) {
      f32x4 s0 = kp[0][0], s1 = kp[0][1];
#pragma unroll
      for (int r8 = 1; r8 < 8; ++r8) { s0 += kp[r8][0]; s1 += kp[r8][1]; }
      *(LAS bf16x8*)(kvl + (kh * 3 + dc) * 1024 + (wv < 4 ? lane : lane & 3) * 16) = pack8(s0, s1);
    }
    wait_lgkm0();
    bar();
    bf16x8 ka[2][3];
#pragma unroll
    for (int k2 = 0; k2 < 2; ++k2)
#pragma unroll
      for (int d2 = 0; d2 < 3; ++d2) ka[k2][d2] = *(LAS bf16x8*)(kvl + (k2 * 3 + d2) * 1024 + (d2 < 2 ? lane : g) * 16);
    auto rsrc = __builtin_amdgcn_make_buffer_rsrc((void*)(A.af + (size_t)(112 + head) * 128 * 512), 0, 128 * 1024, 0x00020000);
#pragma unroll
    for (int m = 0; m < 4; ++m) {
      const bf16x8 b0 = pqe[m], b1 = pqn[m];
      f32x4 n0 = MFMA16(ka[0][0], b0, z4); n0 = MFMA16(ka[1][0], b1, n0);
      f32x4 n1 = MFMA16(ka[0][1], b0, z4); n1 = MFMA16(ka[1][1], b1, n1);
      f32x4 dn = MFMA16(ka[0][2], b0, z4); dn = MFMA16(ka[1][2], b1, dn);
      const float rd = __builtin_amdgcn_rcpf(rbf(rbf(dn[0]) + 1e-6f));
      const f32x4 o0 = n0 * rd, o1 = n1 * rd;
      const int mf = rq * 32 + wv * 4 + m;
      __builtin_amdgcn_raw_buffer_store_b128(__builtin_bit_cast(u32x4, pack8(o0, o1)), rsrc, (mf * 64 + lane) * 16, 0, 16);
    }
  }
  bar();

  f32x4 acc[4][4];
#pragma unroll
  for (int m = 0; m < 4; ++m)
#pragma unroll
    for (int n = 0; n < 4; ++n) {
      if (FIN && GRP == 0) acc[m][n] = (f32x4){bflo(bb[n][0]), bfhi(bb[n][0]), bflo(bb[n][1]), bfhi(bb[n][1])};
      else acc[m][n] = (f32x4){0.f, 0.f, 0.f, 0.f};
    }
  LAS char* pA = lds + GRP * 8192 + wm * 4096 + lane * 16;
  LAS char* pB = lds + BOFF + GRP * 8 * PSTR + PSTR * (4 * (g & 1) + q) + 256 * (g >> 1) + 8 * p + 128 * wn;
  constexpr int SO_FIRST = (112 - KB0) / 2;
  bf16x8 a0[4], b0[4], a1[4], b1[4];
#pragma unroll
  for (int n = 0; n < 4; ++n) b0[n] = cat(trread(pB + n * 32), trread(pB + n * 32 + 512));
#pragma unroll
  for (int m = 0; m < 4; ++m) a0[m] = *(LAS bf16x8*)(pA + m * 1024);
  wait_lgkm0();
  bar();

  auto step = [&](int st, bf16x8 (&a)[4], bf16x8 (&b)[4], bf16x8 (&an)[4], bf16x8 (&bn)[4], auto tag) {
    constexpr int T = decltype(tag)::value;
    const int so = ((st + 1) & 3) * SLOT;
    s16x4 lo[4];
    auto rd = [&](int i) {
      if (i < 4) an[i] = *(LAS bf16x8*)(pA + so + i * 1024);
      else {
        const int n = (i - 4) >> 1;
        LAS char* pp = pB + so + n * 32;
        if (((i - 4) & 1) == 0) lo[n] = trread(pp); else bn[n] = cat(lo[n], trread(pp + 512));
      }
    };
    if (GRP == 1 && T > 0) {
#pragma unroll
      for (int i = 0; i < 12; ++i) rd(i);
    }
    SB();
#pragma unroll
    for (int i = 0; i < 16; ++i) acc[i & 3][i >> 2] = MFMA16(b[i >> 2], a[i & 3], acc[i & 3][i >> 2]);
    SB();
    if (GRP == 0 && T > 0) {
#pragma unroll
      for (int i = 0; i < 12; ++i) rd(i);
    }
    if (T > 0) {
      wait_lgkm0();
      bar();
    }
  };
  unsigned* const ocnt = A.flags + (FLAG_OCNT + (rb >> 2)) * FLAG_STRIDE;
  unsigned* const pflag = A.flags + (FLAG_PART + tile) * FLAG_STRIDE;
  unsigned* const tmo = A.flags + FLAG_TMO * FLAG_STRIDE;
  unsigned seen = 0;
#pragma unroll 1
  for (int st = 0; st < NS - 4; st += 2) {
    if (wv == 0) {
      if (!FIN && st == 2 && lane == 0)
        __hip_atomic_fetch_add((GAS unsigned*)(A.flags + (FLAG_OCNT + (tile >> 5)) * FLAG_STRIDE), 1u, RLX_AGENT);
      if (FIN) {
        constexpr int S0 = (SO_FIRST - 8) & ~1;
        if (st == S0) seen = peek_flag(ocnt);
        if (st == S0 + 2) await_flag(seen, ocnt, 32u, tmo);
      }
    }
    step(st, a0, b0, a1, b1, IC<4>{});
    if (!FIN && st == 0) wait_vm<0>();
    step(st + 1, a1, b1, a0, b0, IC<4>{});
  }
  if (FIN && wv == 0) seen = peek_flag(pflag);
  step(NS - 4, a0, b0, a1, b1, IC<3>{}); step(NS - 3, a1, b1, a0, b0, IC<2>{});
  if (FIN && wv == 0) await_flag(seen, pflag, 1u, tmo);
  step(NS - 2, a0, b0, a1, b1, IC<1>{});
  f32x4 pv[2][4];
  if (FIN) {
    auto rsrc = __builtin_amdgcn_make_buffer_rsrc((void*)(A.part + (size_t)tile * 16384), 0, 65536, 0x00020000);
#pragma unroll
    for (int mm = 0; mm < 2; ++mm)
#pragma unroll
      for (int n = 0; n < 4; ++n)
        pv[mm][n] = __builtin_bit_cast(f32x4, __builtin_amdgcn_raw_buffer_load_b128(rsrc, ((wv * 8 + mm * 4 + n) * 64 + lane) * 16, 0, 16));
    SB();
  }
  step(NS - 1, a1, b1, a0, b0, IC<0>{});

  {
    LAS f32x4* wb = (LAS f32x4*)(lds + CBASE + wv * 8192) + lane;
#pragma unroll
    for (int mm = 0; mm < 2; ++mm)
#pragma unroll
      for (int n = 0; n < 4; ++n) wb[(mm * 4 + n) * 64] = acc[GRP == 0 ? 2 + mm : mm][n];
  }
  wait_lgkm0();
  bar();
  f32x4 r[2][4];
  {
    LAS f32x4* rbuf = (LAS f32x4*)(lds + CBASE + ((1 - GRP) * 4 + w4) * 8192) + lane;
#pragma unroll
    for (int mm = 0; mm < 2; ++mm)
#pragma unroll
      for (int n = 0; n < 4; ++n) r[mm][n] = acc[GRP == 0 ? mm : 2 + mm][n] + rbuf[(mm * 4 + n) * 64];
  }
  if (!FIN) {
    auto rsrc = __builtin_amdgcn_make_buffer_rsrc((void*)(A.part + (size_t)tile * 16384), 0, 65536, 0x00020000);
#pragma unroll
    for (int mm = 0; mm < 2; ++mm)
#pragma unroll
      for (int n = 0; n < 4; ++n)
        __builtin_amdgcn_raw_buffer_store_b128(__builtin_bit_cast(u32x4, r[mm][n]), rsrc, ((wv * 8 + mm * 4 + n) * 64 + lane) * 16, 0, 16);
    wait_vm<0>();
    bar();
    if (wv == 0 && lane == 0) __hip_atomic_store((GAS unsigned*)(A.flags + (FLAG_PART + tile) * FLAG_STRIDE), 1u, RLX_AGENT);
  } else {
    int lz = lane; asm volatile("" : "+v"(lz));
    const int rl = wm * 64 + GRP * 32 + (lz & 15), cl = wn * 64 + 4 * (lz >> 4);
    LAS char* xl = lds + XSLOT * SLOT + (rl >> 2) * XPSTR + (rl & 3) * 256 + cl * 2;
    u16* op = A.out + (size_t)(rb * 128 + rl) * D_ + ct * 128 + cl;
#pragma unroll
    for (int mm = 0; mm < 2; ++mm)
#pragma unroll
      for (int n = 0; n < 4; ++n) {
        const u32x2 xr = *(LAS u32x2*)(xl + mm * 4 * XPSTR + n * 32);
        const f32x4 xv = {bflo(xr[0]), bfhi(xr[0]), bflo(xr[1]), bfhi(xr[1])};
        const f32x4 o = (r[mm][n] + pv[mm][n]) + xv;
        u32x2 ov = {pk2(o[0], o[1]), pk2(o[2], o[3])};
        *(u32x2*)(op + (size_t)mm * 16 * D_ + n * 16) = ov;
      }
  }
}

template <bool FIN>
DI void g2_loader(const G2Args& A, LAS char* lds, int rb, int ct, int w4, int lane) {
  constexpr int NS = FIN ? N2 : N1;
  constexpr int KB0 = FIN ? 2 * N1 : 0;
  const int kbs = w4 >> 1, u0 = 4 * (w4 & 1);
  const unsigned lbase = (unsigned)(size_t)lds;
  const char* asrc = (const char*)A.af + ((size_t)kbs * 128 + rb * 8 + u0) * 1024;
  const unsigned aoff = lane * 16;
  const unsigned boff = (unsigned)(((kbs * 32 + u0 + 8 * (lane >> 4)) * D_ + ct * 128 + (lane & 15) * 8) * 2);
  const unsigned adst = lbase + kbs * 8192 + u0 * 1024, bdst = lbase + BOFF + kbs * 8 * PSTR + u0 * PSTR;
  auto issue = [&](int st) {
    const int kb = KB0 + 2 * st, slot = (st & 3) * SLOT;
    const char* wb = kb < 112 ? (const char*)A.w2 + (size_t)kb * 32 * D_ * 2 : (const char*)A.wo + (size_t)(kb - 112) * 32 * D_ * 2;
    if (FIN && kb >= 112) dma16s_x4k_sc1(asrc + (size_t)kb * 128 * 1024, aoff, adst + slot);
    else dma16s_x4k(asrc + (size_t)kb * 128 * 1024, aoff, adst + slot);
#pragma unroll
    for (int jj = 0; jj < 4; ++jj) dma16s(wb + jj * (D_ * 2), boff, bdst + slot + jj * PSTR);
  };
  if (!FIN) bar();
  issue(0); issue(1); issue(2); issue(3);
  if (!FIN) bar();
  wait_vm<24>();
  bar();
  wait_vm<16>();
  bar();
#pragma unroll 1
  for (int st = 0; st < NS - 4; ++st) { issue(st + 4); wait_vm<16>(); bar(); }
  if (FIN) {
    const char* xs = (const char*)A.x + ((size_t)(rb * 128 + w4 * 32) * D_ + ct * 128) * 2;
    const unsigned xo = (unsigned)((lane >> 4) * (D_ * 2) + (lane & 15) * 16);
#pragma unroll
    for (int jj = 0; jj < 8; ++jj) dma16s(xs + jj * 4 * (D_ * 2), xo, lbase + XSLOT * SLOT + (w4 * 8 + jj) * XPSTR);
    wait_vm<16>(); bar();
    wait_vm<8>(); bar();
    wait_vm<0>(); bar();
  } else {
    wait_vm<8>(); bar();
    wait_vm<0>(); bar();
  }
}

__global__ __launch_bounds__(768) void k2_gemm2(G2Args A) {
  extern __shared__ __attribute__((aligned(16))) char smem[];
  LAS char* lds = (LAS char*)smem;
  const int wid = __builtin_amdgcn_readfirstlane(threadIdx.x >> 6), lane = threadIdx.x & 63;
  const int bid = blockIdx.x, xcd = bid & 7, idx = bid >> 3;
  const int rb = (xcd & 3) * 4 + (idx & 3), ct = (xcd >> 2) * 4 + ((idx >> 2) & 3), fin = idx >> 4;
  const int w4 = wid & 3;
  if (wid >= 8) { if (fin) g2_loader<true>(A, lds, rb, ct, w4, lane); else g2_loader<false>(A, lds, rb, ct, w4, lane); return; }
  if (fin) { if (wid < 4) g2_body<true, 0>(A, lds, rb, ct, w4, lane); else g2_body<true, 1>(A, lds, rb, ct, w4, lane); }
  else { if (wid < 4) g2_body<false, 0>(A, lds, rb, ct, w4, lane); else g2_body<false, 1>(A, lds, rb, ct, w4, lane); }
}

extern "C" void kernel_launch(void* const* d_in, const int* in_sizes, int n_in, void* d_out, int out_size, void* d_ws, size_t ws_size,
                              hipStream_t stream) {
  const u16* x = (const u16*)d_in[0];
  const u16* wqkv = (const u16*)d_in[1];
  const u16* wphi = (const u16*)d_in[2];
  const u16* wo = (const u16*)d_in[3];
  const u16* w1 = (const u16*)d_in[5];
  const u16* b1 = (const u16*)d_in[6];
  const u16* w2 = (const u16*)d_in[7];
  const u16* b2 = (const u16*)d_in[8];
  char* ws = (char*)d_ws;
  unsigned* flags = (unsigned*)(ws + WS_FLAGS);
  u16* xnf = (u16*)(ws + WS_XNF);
  u16* af = (u16*)(ws + WS_AF);
  u16* phiq = (u16*)(ws + WS_PHIQ);
  float* kvp = (float*)(ws + WS_KVP);
  float* part = (float*)(ws + WS_PART);
  (void)hipFuncSetAttribute((const void*)k1_gemm1, hipFuncAttributeMaxDynamicSharedMemorySize, G1_LDS);
  (void)hipFuncSetAttribute((const void*)k2_gemm2, hipFuncAttributeMaxDynamicSharedMemorySize, G2_LDS);
  k0_ln<<<256, 512, 0, stream>>>(x, xnf, flags);
  G1Args a1 = {xnf, wqkv, w1, b1, wphi, af, phiq, kvp, w2, wo};
  k1_gemm1<<<256, 512, G1_LDS, stream>>>(a1);
  G2Args a2 = {af, w2, wo, phiq, kvp, part, x, b2, (u16*)d_out, flags, wphi};
  k2_gemm2<<<256, 768, G2_LDS, stream>>>(a2);
}
```

```cpp
#include <hip/hip_runtime.h>
#include <hip/hip_bf16.h>
#include <stdint.h>

typedef unsigned short u16;
typedef short bf16x8 __attribute__((ext_vector_type(8)));
typedef short s16x4 __attribute__((ext_vector_type(4)));
typedef float f32x4 __attribute__((ext_vector_type(4)));
typedef float f32x2 __attribute__((ext_vector_type(2)));
typedef unsigned u32x4 __attribute__((ext_vector_type(4)));
typedef unsigned u32x2 __attribute__((ext_vector_type(2)));
typedef __bf16 bf16v2 __attribute__((ext_vector_type(2)));
#define LAS __attribute__((address_space(3)))
#define GAS __attribute__((address_space(1)))
#define DI __device__ __forceinline__
#define RLX_AGENT __ATOMIC_RELAXED, __HIP_MEMORY_SCOPE_AGENT

constexpr int D_ = 1024, F_ = 3584;
constexpr int SLOT = 33280;
constexpr int BOFF = 16384;
constexpr int PSTR = 1056;
constexpr int RING = 4 * SLOT;
constexpr int RA = 3, RB = 6;
constexpr int BSL = 16 * PSTR;
constexpr int G1_B = RA * 16384;
constexpr int G1_RING = G1_B + RB * BSL;
constexpr int G1_WPHI = G1_RING;
constexpr int G1_BIAS = G1_RING + 4096;
constexpr int G1_DUMP = G1_RING + 5120;
constexpr int G1_LDS = G1_RING + 5120 + 256;
constexpr int G2_LDS = RING + 6144 + 256;
constexpr int NKB = 144;
constexpr int N1 = 30;
constexpr int N2 = 72 - N1;
constexpr int XSLOT = (N2 - 4) & 3;
constexpr int XPSTR = 1040;
constexpr int CBASE = XSLOT >= 2 ? 0 : 2 * SLOT;
static_assert(32 * XPSTR <= SLOT && N1 % 2 == 0, "");

constexpr size_t WS_FLAGS = 0;
constexpr size_t WS_XNF = 1u << 20;
constexpr size_t WS_AF = WS_XNF + (4u << 20);
constexpr size_t WS_PHIQ = WS_AF + (size_t)NKB * 128 * 1024;
constexpr size_t WS_KVP = WS_PHIQ + (8u << 20);
constexpr size_t WS_PART = WS_KVP + (3u << 20);
constexpr int FLAG_STRIDE = 64;
constexpr int FLAG_OCNT = 0;
constexpr int FLAG_PART = 8;
constexpr int FLAG_TMO = 8 + 128;
constexpr int FLAG_WORDS = (8 + 128 + 1) * FLAG_STRIDE;

template <int N> struct IC { static constexpr int value = N; };
constexpr int ST_AUX = 16;
#define MFMA16(a, b, c) __builtin_amdgcn_mfma_f32_16x16x32_bf16((a), (b), (c), 0, 0, 0)
template <int N> DI void wait_vm() { asm volatile("s_waitcnt vmcnt(%0)" ::"i"(N) : "memory"); }
DI void wait_lgkm0() { asm volatile("" ::: "memory"); __builtin_amdgcn_s_waitcnt(0xC07F); asm volatile("" ::: "memory"); }
DI void bar() { __builtin_amdgcn_sched_barrier(0); __builtin_amdgcn_s_barrier(); __builtin_amdgcn_sched_barrier(0); }
DI void dma16(const void* g, unsigned l) {
  asm volatile("s_mov_b32 m0, %1\n\ts_nop 0\n\tglobal_load_lds_dwordx4 %0, off" ::"v"(g), "s"(l) : "memory");
}
DI void dma16s(const void* sb, unsigned voff, unsigned l) {
  asm volatile("s_mov_b32 m0, %2\n\ts_nop 0\n\tglobal_load_lds_dwordx4 %0, %1" ::"v"(voff), "s"(sb), "s"(l) : "memory");
}
DI void dma16s_x4k(const void* sb, unsigned voff, unsigned l) {
  asm volatile("s_mov_b32 m0, %2\n\ts_nop 0\n\tglobal_load_lds_dwordx4 %0, %1\n\tglobal_load_lds_dwordx4 %0, %1 offset:1024\n\t"
               "global_load_lds_dwordx4 %0, %1 offset:2048\n\tglobal_load_lds_dwordx4 %0, %1 offset:3072" ::"v"(voff), "s"(sb), "s"(l) : "memory");
}
DI void dma16s_x4k_sc1(const void* sb, unsigned voff, unsigned l) {
  asm volatile("s_mov_b32 m0, %2\n\ts_nop 0\n\tglobal_load_lds_dwordx4 %0, %1 sc1\n\tglobal_load_lds_dwordx4 %0, %1 offset:1024 sc1\n\t"
               "global_load_lds_dwordx4 %0, %1 offset:2048 sc1\n\tglobal_load_lds_dwordx4 %0, %1 offset:3072 sc1" ::"v"(voff), "s"(sb), "s"(l) : "memory");
}
DI void touch(const void* g, unsigned l) { asm volatile("s_mov_b32 m0, %1\n\ts_nop 0\n\tglobal_load_lds_dword %0, off" ::"v"(g), "s"(l) : "memory"); }
DI s16x4 trread(LAS char* p) { return __builtin_amdgcn_ds_read_tr16_b64_v4i16((LAS s16x4*)p); }
DI bf16x8 cat(s16x4 lo, s16x4 hi) { return __builtin_shufflevector(lo, hi, 0, 1, 2, 3, 4, 5, 6, 7); }
DI unsigned pk2(float lo, float hi) { f32x2 v = {lo, hi}; return __builtin_bit_cast(unsigned, __builtin_convertvector(v, bf16v2)); }
DI bf16x8 pack8(f32x4 a, f32x4 b) { u32x4 r = {pk2(a[0], a[1]), pk2(a[2], a[3]), pk2(b[0], b[1]), pk2(b[2], b[3])}; return __builtin_bit_cast(bf16x8, r); }
DI float bflo(unsigned u) { return __uint_as_float(u << 16); }
DI float bfhi(unsigned u) { return __uint_as_float(u & 0xffff0000u); }
DI float rbf(float x) { return bflo(pk2(x, x)); }
template <int CTRL> DI float dpp(float x) { return __builtin_bit_cast(float, __builtin_amdgcn_mov_dpp(__builtin_bit_cast(int, x), CTRL, 0xf, 0xf, true)); }
constexpr int ROR8 = 0x128;
DI float xg_sum(float x) {
  auto s = __builtin_amdgcn_permlane16_swap(__float_as_uint(x), __float_as_uint(x), false, false);
  x = __uint_as_float(s[0]) + __uint_as_float(s[1]);
  auto t = __builtin_amdgcn_permlane32_swap(__float_as_uint(x), __float_as_uint(x), false, false);
  return __uint_as_float(t[0]) + __uint_as_float(t[1]);
}
constexpr float LOG2E = 1.4426950408889634f;
#define SB() __builtin_amdgcn_sched_barrier(0)
DI u32x4 gelu8(f32x4 a, f32x4 b) {
  const float c1 = -2.0f * 0.7978845608028654f * LOG2E, c3 = c1 * 0.044715f;
  f32x4 ta = a * a, tb = b * b;
  ta = ta * c3 + c1; tb = tb * c3 + c1;
  ta = ta * a; tb = tb * b;
  SB();
#pragma unroll
  for (int r = 0; r < 4; ++r) { ta[r] = __builtin_amdgcn_exp2f(ta[r]); tb[r] = __builtin_amdgcn_exp2f(tb[r]); }
  SB();
  ta = ta + 1.0f; tb = tb + 1.0f;
  SB();
#pragma unroll
  for (int r = 0; r < 4; ++r) { ta[r] = __builtin_amdgcn_rcpf(ta[r]); tb[r] = __builtin_amdgcn_rcpf(tb[r]); }
  SB();
  return __builtin_bit_cast(u32x4, pack8(a * ta, b * tb));
}
template <int OP> DI float rop(float a, float b) { return OP == 0 ? fmaxf(a, b) : OP == 1 ? fminf(a, b) : a + b; }
template <int OP> DI void xg_red4(float (&v)[4]) {
  auto s0 = __builtin_amdgcn_permlane16_swap(__float_as_uint(v[0]), __float_as_uint(v[1]), false, false);
  auto s1 = __builtin_amdgcn_permlane16_swap(__float_as_uint(v[2]), __float_as_uint(v[3]), false, false);
  const float x = rop<OP>(__uint_as_float(s0[0]), __uint_as_float(s0[1])), y = rop<OP>(__uint_as_float(s1[0]), __uint_as_float(s1[1]));
  auto t = __builtin_amdgcn_permlane32_swap(__float_as_uint(x), __float_as_uint(y), false, false);
  const unsigned e = __float_as_uint(rop<OP>(__uint_as_float(t[0]), __uint_as_float(t[1])));
  auto u = __builtin_amdgcn_permlane32_swap(e, e, false, false);
  auto b0 = __builtin_amdgcn_permlane16_swap(u[0], u[0], false, false);
  auto b1 = __builtin_amdgcn_permlane16_swap(u[1], u[1], false, false);
  v[0] = __uint_as_float(b0[0]); v[1] = __uint_as_float(b0[1]); v[2] = __uint_as_float(b1[0]); v[3] = __uint_as_float(b1[1]);
}
DI void dual_softmax4(f32x4 (&p0)[4], f32x4 (&p1)[4], bf16x8 (&pe)[4], bf16x8 (&pn)[4]) {
  float mx[4], mn[4], sp[4], sn[4];
#pragma unroll
  for (int m = 0; m < 4; ++m) {
    const u32x4 pr = __builtin_bit_cast(u32x4, pack8(p0[m], p1[m]));
    p0[m] = (f32x4){bflo(pr[0]), bfhi(pr[0]), bflo(pr[1]), bfhi(pr[1])} * LOG2E;
    p1[m] = (f32x4){bflo(pr[2]), bfhi(pr[2]), bflo(pr[3]), bfhi(pr[3])} * LOG2E;
    mx[m] = fmaxf(fmaxf(fmaxf(p0[m][0], p0[m][1]), fmaxf(p0[m][2], p0[m][3])), fmaxf(fmaxf(p1[m][0], p1[m][1]), fmaxf(p1[m][2], p1[m][3])));
    mn[m] = fminf(fminf(fminf(p0[m][0], p0[m][1]), fminf(p0[m][2], p0[m][3])), fminf(fminf(p1[m][0], p1[m][1]), fminf(p1[m][2], p1[m][3])));
  }
  xg_red4<0>(mx); xg_red4<1>(mn);
  f32x4 n0[4], n1[4];
#pragma unroll
  for (int m = 0; m < 4; ++m) {
    n0[m] = mn[m] - p0[m]; n1[m] = mn[m] - p1[m]; p0[m] = p0[m] - mx[m]; p1[m] = p1[m] - mx[m];
#pragma unroll
    for (int r = 0; r < 4; ++r) {
      p0[m][r] = __builtin_amdgcn_exp2f(p0[m][r]); p1[m][r] = __builtin_amdgcn_exp2f(p1[m][r]);
      n0[m][r] = __builtin_amdgcn_exp2f(n0[m][r]); n1[m][r] = __builtin_amdgcn_exp2f(n1[m][r]);
    }
    const f32x4 se = p0[m] + p1[m], sq = n0[m] + n1[m];
    sp[m] = (se[0] + se[1]) + (se[2] + se[3]); sn[m] = (sq[0] + sq[1]) + (sq[2] + sq[3]);
  }
  xg_red4<2>(sp); xg_red4<2>(sn);
#pragma unroll
  for (int m = 0; m < 4; ++m) {
    const float rp = __builtin_amdgcn_rcpf(sp[m]), rn = __builtin_amdgcn_rcpf(sn[m]);
    pe[m] = pack8(p0[m] * rp, p1[m] * rp); pn[m] = pack8(n0[m] * rn, n1[m] * rn);
  }
}

__global__ __launch_bounds__(512) void k0_ln(const u16* __restrict__ x, u16* __restrict__ xnf, unsigned* __restrict__ flags) {
  __shared__ float red[2][8][8];
  const int tid = threadIdx.x, w = tid >> 6, l = tid & 63, i = l & 7, c = l >> 3;
  const int row = blockIdx.x * 8 + i;
  const u32x4* src = (const u32x4*)(x + (size_t)row * D_) + 16 * w + c;
  const u32x4 v0 = src[0], v1 = src[8];
  if (blockIdx.x == 0) { for (int j = tid; j < FLAG_WORDS; j += 512) flags[j] = 0u; }
  float f[16];
#pragma unroll
  for (int j = 0; j < 4; ++j) { f[2 * j] = bflo(v0[j]); f[2 * j + 1] = bfhi(v0[j]); f[8 + 2 * j] = bflo(v1[j]); f[9 + 2 * j] = bfhi(v1[j]); }
  float s = 0.f;
#pragma unroll
  for (int j = 0; j < 16; ++j) s += f[j];
  s += dpp<ROR8>(s); s = xg_sum(s);
  if (l < 8) red[0][w][i] = s;
  __syncthreads();
  float mean = 0.f;
#pragma unroll
  for (int j = 0; j < 8; ++j) mean += red[0][j][i];
  mean *= (1.0f / D_);
  float q = 0.f;
#pragma unroll
  for (int j = 0; j < 16; ++j) { float d = f[j] - mean; q = fmaf(d, d, q); }
  q += dpp<ROR8>(q); q = xg_sum(q);
  if (l < 8) red[1][w][i] = q;
  __syncthreads();
  float var = 0.f;
#pragma unroll
  for (int j = 0; j < 8; ++j) var += red[1][j][i];
  var *= (1.0f / D_);
  const float mb = rbf(mean);
  const float sd = rbf(sqrtf(rbf(rbf(var) + 1e-5f)));
  const float rs = 1.0f / sd;
  u32x4 o0, o1;
#pragma unroll
  for (int j = 0; j < 4; ++j) {
    o0[j] = pk2(rbf(f[2 * j] - mb) * rs, rbf(f[2 * j + 1] - mb) * rs);
    o1[j] = pk2(rbf(f[8 + 2 * j] - mb) * rs, rbf(f[9 + 2 * j] - mb) * rs);
  }
  const int mfrag = row >> 4, ri = row & 15;
  const int ch0 = 16 * w + c, ch1 = ch0 + 8;
  auto rsx = __builtin_amdgcn_make_buffer_rsrc((void*)xnf, 0, 4 << 20, 0x00020000);
  __builtin_amdgcn_raw_buffer_store_b128(o0, rsx, (((ch0 >> 2) * 128 + mfrag) * 64 + (ch0 & 3) * 16 + ri) * 16, 0, ST_AUX);
  __builtin_amdgcn_raw_buffer_store_b128(o1, rsx, (((ch1 >> 2) * 128 + mfrag) * 64 + (ch1 & 3) * 16 + ri) * 16, 0, ST_AUX);
}

struct G1Args {
  const u16* xnf; const u16* wqkv; const u16* w1; const u16* b1; const u16* wphi;
  u16* af; u16* phiq; float* kvp; const u16* w2; const u16* wo;
};

template <bool ISX, int GRP>
DI void g1_body(const G1Args& A, LAS char* lds, int rb, int j, int pos4, int wm, int lane) {
  constexpr int NF = GRP == 0 ? 7 : 6;
  const int g = lane >> 4, q = (lane & 15) >> 2, p = lane & 3;
  const int hp = 2 * j + (pos4 >> 1);
  const int cp = pos4 == 0 ? 0 : pos4 == 3 ? 384 : pos4 == 1 ? 96 : 224;
  const int co = pos4 == 0 ? 64 : pos4 == 3 ? 368 : pos4 == 1 ? 80 : 352;
  const int cr = pos4 == 0 ? 0 : pos4 == 3 ? 320 : pos4 == 1 ? 64 : 192;
  constexpr int RBASE = ISX ? 256 : 128;
  const int colT = cp + 32 * (2 * GRP), colU = cp + 32 * (ISX ? GRP : 2 * GRP + 1);

  const unsigned lbase = (unsigned)(size_t)lds;
  const char* dsrc = nullptr; long dstep = 0; int dj = 0;
  const char* xsrc = (const char*)A.xnf + (size_t)(rb * 16 + 4 * wm) * 1024;
  const unsigned xoff = lane * 16;
  if (GRP == 0) {
    const int u0 = 8 * (wm & 1) + 4 * (wm >> 1), c = lane & 31, line = c >> 3, ch = c & 7, row = u0 + 16 * (lane >> 5);
    const bool fromq = ISX ? (line < 2) : (line < 1);
    const int ld = fromq ? 3 * D_ : F_;
    const u16* base = fromq ? (A.wqkv + (ISX ? (1 + line) * D_ : 0) + 64 * hp) : (A.w1 + 448 * j + cr + 64 * (line - (ISX ? 2 : 1)));
    dsrc = (const char*)(base + (size_t)row * ld + ch * 8); dstep = 64L * ld; dj = 2 * ld;
  }
  const unsigned ddst = lbase + (GRP == 0 ? G1_B + 4 * wm * PSTR : 4 * wm * 1024);
  constexpr int RD = GRP == 0 ? RB : RA;
  auto issue = [&](int slot) {
    if (GRP == 0) {
#pragma unroll
      for (int jj = 0; jj < 4; ++jj) dma16(dsrc + jj * dj, ddst + slot * BSL + jj * PSTR);
      dsrc += dstep;
    } else {
      dma16s_x4k(xsrc, xoff, ddst + slot * 16384);
      xsrc += 128 * 1024;
    }
  };
  if (GRP == 0) dma16s(A.wphi + (size_t)hp * 2048 + wm * 512, xoff, lbase + G1_WPHI + wm * 1024);
  else if (wm == 0) { const int nch = ISX ? 16 : 24; dma16s(A.b1 + 448 * j + cr, (lane < nch ? lane : 0) * 16, lbase + G1_BIAS); }
  issue(0);
#pragma unroll
  for (int i = 1; i < RD; ++i) issue(i);
  wait_vm<4 * (RD - 1)>();
  bar();

  f32x4 acc[4][NF];
  {
    LAS char* bp = lds + G1_BIAS + 8 * g;
#pragma unroll
    for (int f = 0; f < NF; ++f) {
      f32x4 b4 = {0.f, 0.f, 0.f, 0.f};
      if (f >= (ISX ? 4 : 2)) {
        const int col = f < 4 ? colT + 16 * (f - 2) : f < 6 ? colU + 16 * (f - 4) : co;
        const u32x2 bb = *(LAS u32x2*)(bp + (col - cr) * 2);
        b4 = (f32x4){bflo(bb[0]), bfhi(bb[0]), bflo(bb[1]), bfhi(bb[1])};
      }
#pragma unroll
      for (int m = 0; m < 4; ++m) acc[m][f] = b4;
    }
  }
  const int lb = G1_B + PSTR * (q + 4 * (g & 1)) + 512 * (g >> 1) + 8 * p;
  LAS char* pA = lds + wm * 4096 + lane * 16;
  LAS char* pS = lds + lb + 64 * GRP;
  LAS char* pT = lds + lb + (ISX ? 128 + 64 * GRP : RBASE + (colT - cr) * 2);
  LAS char* pU = lds + lb + RBASE + (colU - cr) * 2;
  LAS char* pO = lds + lb + RBASE + (co - cr) * 2;
  auto rdB = [&](int so, int f) -> bf16x8 {
    LAS char* pp = (f < 2 ? pS : f < 4 ? pT : f < 6 ? pU : pO) + so + (f < 6 ? (f & 1) * 32 : 0);
    return cat(trread(pp), trread(pp + 8 * PSTR));
  };
  bf16x8 a0[4], b0[NF], a1[4], b1[NF];
#pragma unroll
  for (int f = 0; f < NF; ++f) b0[f] = rdB(0, f);
#pragma unroll
  for (int m = 0; m < 4; ++m) a0[m] = *(LAS bf16x8*)(pA + m * 1024);
  wait_lgkm0();
  wait_vm<4 * (RD - 2)>();
  bar();
  int sa = 0, sb = 0;
  auto step = [&](bf16x8 (&a)[4], bf16x8 (&b)[NF], bf16x8 (&an)[4], bf16x8 (&bn)[NF], auto tag) {
    constexpr int T = decltype(tag)::value;
    constexpr bool ISSUE = T >= RD;
    const int sfree = GRP == 0 ? sb : sa;
    sa = sa + 1 == RA ? 0 : sa + 1; sb = sb + 1 == RB ? 0 : sb + 1;
    const int soa = sa * 16384, so = sb * BSL;
    auto mm = [&](int i) {
      const int f = i >> 2, m = i & 3;
      if (ISX && (f == 2 || f == 3)) acc[m][f] = MFMA16(a[m], b[f], acc[m][f]);
      else acc[m][f] = MFMA16(b[f], a[m], acc[m][f]);
    };
    s16x4 lo[NF];
    auto rd = [&](int i) {
      if (i < 4) an[i] = *(LAS bf16x8*)(pA + soa + i * 1024);
      else {
        const int f = (i - 4) >> 1;
        LAS char* pp = (f < 2 ? pS : f < 4 ? pT : f < 6 ? pU : pO) + so + (f < 6 ? (f & 1) * 32 : 0);
        if (((i - 4) & 1) == 0) lo[f] = trread(pp); else bn[f] = cat(lo[f], trread(pp + 8 * PSTR));
      }
    };
    if (GRP == 1) {
      if (ISSUE) issue(sfree);
      if (T > 0) {
#pragma unroll
        for (int i = 0; i < 4 + 2 * NF; ++i) rd(i);
      }
      SB();
#pragma unroll
      for (int i = 0; i < 4 * NF; ++i) mm(i);
      SB();
    } else {
#pragma unroll
      for (int i = 0; i < 4 * NF; ++i) mm(i);
      SB();
      if (T > 0) {
#pragma unroll
        for (int i = 0; i < 4 + 2 * NF; ++i) rd(i);
      }
      if (ISSUE) issue(sfree);
    }
    if (T > 0) {
      wait_lgkm0();
      if (T > 1) wait_vm<4 * ((T < RD ? T : RD) - 2)>();
      bar();
    }
  };
#pragma unroll 1
  for (int s = 0; s < 24; s += 2) { step(a0, b0, a1, b1, IC<8>{}); step(a1, b1, a0, b0, IC<8>{}); }
  step(a0, b0, a1, b1, IC<7>{}); step(a1, b1, a0, b0, IC<6>{}); step(a0, b0, a1, b1, IC<5>{}); step(a1, b1, a0, b0, IC<4>{});
  step(a0, b0, a1, b1, IC<3>{}); step(a1, b1, a0, b0, IC<2>{}); step(a0, b0, a1, b1, IC<1>{}); step(a1, b1, a0, b0, IC<0>{});

  const int mf0 = rb * 16 + wm * 4;
  {
    auto rs = __builtin_amdgcn_make_buffer_rsrc((void*)A.af, 0, NKB * 128 * 1024, 0x00020000);
    const int o0 = (mf0 * 64 + lane) * 16;
    if (!ISX) {
      const int kb = (448 * j + colT) >> 5;
#pragma unroll
      for (int m = 0; m < 4; ++m) __builtin_amdgcn_raw_buffer_store_b128(gelu8(acc[m][2], acc[m][3]), rs, o0 + kb * 131072 + m * 1024, 0, ST_AUX);
    }
    {
      const int kb = (448 * j + colU) >> 5;
#pragma unroll
      for (int m = 0; m < 4; ++m) __builtin_amdgcn_raw_buffer_store_b128(gelu8(acc[m][4], acc[m][5]), rs, o0 + kb * 131072 + m * 1024, 0, ST_AUX);
    }
    if (GRP == 0) {
      const int kb = (448 * j + co) >> 5, n = (co >> 4) & 1;
#pragma unroll
      for (int m = 0; m < 4; m += 2) {
        const u32x4 gv = gelu8(acc[m][NF - 1], acc[m + 1][NF - 1]);
        __builtin_amdgcn_raw_buffer_store_b64((u32x2){gv[0], gv[1]}, rs, o0 + kb * 131072 + n * 8 + m * 1024, 0, ST_AUX);
        __builtin_amdgcn_raw_buffer_store_b64((u32x2){gv[2], gv[3]}, rs, o0 + kb * 131072 + n * 8 + (m + 1) * 1024, 0, ST_AUX);
      }
    }
  }
  const int head = 2 * hp + GRP;
  if (!ISX) {
    auto rs = __builtin_amdgcn_make_buffer_rsrc((void*)A.phiq, 0, 4 << 20, 0x00020000);
#pragma unroll
    for (int m = 0; m < 4; ++m)
      __builtin_amdgcn_raw_buffer_store_b128(__builtin_bit_cast(u32x4, pack8(acc[m][0], acc[m][1])), rs, lane * 16 + ((mf0 + m) * 32 + head) * 1024, 0, ST_AUX);
    const int yw = ((rb * 16 + hp) * 8 + GRP * 4 + wm) * 72;
#pragma unroll
    for (int t = 0; t < 2; ++t) {
      const int ln = yw + t * 36 + (lane < 36 ? lane : 0);
      touch(ln < 57344 ? (const char*)A.w2 + (size_t)ln * 128 : (const char*)A.wo + (size_t)(ln - 57344) * 128, lbase + G1_DUMP);
    }
  } else {
    bf16x8 wp[2];
    {
      LAS char* pw = lds + G1_WPHI + GRP * 2048 + (4 * g + q) * 64 + 8 * p;
      wp[0] = cat(trread(pw), trread(pw + 1024));
      wp[1] = cat(trread(pw + 32), trread(pw + 1024 + 32));
    }
    const f32x4 z4 = {0.f, 0.f, 0.f, 0.f};
    f32x4 p0[4], p1[4];
#pragma unroll
    for (int m = 0; m < 4; ++m) {
      const bf16x8 qb = pack8(acc[m][0], acc[m][1]);
      p0[m] = MFMA16(wp[0], qb, z4); p1[m] = MFMA16(wp[1], qb, z4);
    }
    constexpr int TS = 144;
    LAS char* tb = lds + (GRP * 4 + wm) * 12288;
    {
      LAS char* wrp = tb + (lane & 15) * TS + 8 * g;
      bf16x8 fe[4], fn[4];
      dual_softmax4(p0, p1, fe, fn);
#pragma unroll
      for (int m = 0; m < 4; ++m) {
        const u32x4 pe = __builtin_bit_cast(u32x4, fe[m]), pn = __builtin_bit_cast(u32x4, fn[m]);
        *(LAS u32x2*)(wrp + m * 16 * TS) = (u32x2){pe[0], pe[1]};
        *(LAS u32x2*)(wrp + m * 16 * TS + 32) = (u32x2){pe[2], pe[3]};
        *(LAS u32x2*)(wrp + m * 16 * TS + 64) = (u32x2){pn[0], pn[1]};
        *(LAS u32x2*)(wrp + m * 16 * TS + 96) = (u32x2){pn[2], pn[3]};
      }
    }
    wait_lgkm0();
    f32x4 kv[4][3];
    const bf16x8 ones = {0x3F80, 0x3F80, 0x3F80, 0x3F80, 0x3F80, 0x3F80, 0x3F80, 0x3F80};
    LAS char* rdp = tb + (4 * g + q) * TS + 8 * p;
    bf16x8 pa[2][4];
#pragma unroll
    for (int u = 0; u < 2; ++u)
#pragma unroll
      for (int fq = 0; fq < 4; ++fq) pa[u][fq] = cat(trread(rdp + (32 * u) * TS + 32 * fq), trread(rdp + (32 * u + 16) * TS + 32 * fq));
#pragma unroll
    for (int u = 0; u < 2; ++u) {
      const bf16x8 v0 = pack8(acc[2 * u][2], acc[2 * u + 1][2]), v1 = pack8(acc[2 * u][3], acc[2 * u + 1][3]);
#pragma unroll
      for (int fq = 0; fq < 4; ++fq) {
        kv[fq][0] = MFMA16(pa[u][fq], v0, u == 0 ? z4 : kv[fq][0]);
        kv[fq][1] = MFMA16(pa[u][fq], v1, u == 0 ? z4 : kv[fq][1]);
        kv[fq][2] = MFMA16(pa[u][fq], ones, u == 0 ? z4 : kv[fq][2]);
      }
    }
    wait_lgkm0();
    LAS f32x4* rbuf = (LAS f32x4*)tb + lane;
#pragma unroll
    for (int fq = 0; fq < 4; ++fq)
#pragma unroll
      for (int dc = 0; dc < 3; ++dc) rbuf[(fq * 3 + dc) * 64] = kv[fq][dc];
    wait_lgkm0();
    bar();
    LAS f32x4* sbuf = (LAS f32x4*)(lds + GRP * 4 * 12288) + lane;
    auto rs = __builtin_amdgcn_make_buffer_rsrc((void*)A.kvp, 0, 3 << 20, 0x00020000);
#pragma unroll
    for (int t = 0; t < 3; ++t) {
      const int qd = 3 * wm + t;
      const f32x4 sv = (sbuf[qd * 64] + sbuf[768 + qd * 64]) + (sbuf[1536 + qd * 64] + sbuf[2304 + qd * 64]);
      __builtin_amdgcn_raw_buffer_store_b128(__builtin_bit_cast(u32x4, sv), rs, lane * 16 + ((rb * 32 + head) * 12 + qd) * 1024, 0, ST_AUX);
    }
  }
}

__global__ __launch_bounds__(512, 2) void k1_gemm1(G1Args A) {
  extern __shared__ __attribute__((aligned(16))) char smem[];
  LAS char* lds = (LAS char*)smem;
  const int wid = __builtin_amdgcn_readfirstlane(threadIdx.x >> 6), lane = threadIdx.x & 63;
  const int bid = blockIdx.x, xcd = bid & 7, idx = bid >> 3;
  const int rb = (xcd & 1) * 4 + (idx & 3), ct = (xcd >> 1) * 8 + (idx >> 2);
  const int j = ct >> 2, pos4 = ct & 3, wm = wid & 3;
  const bool isx = (pos4 == 0) || (pos4 == 3);
  if (isx) { if (wid < 4) g1_body<true, 0>(A, lds, rb, j, pos4, wm, lane); else g1_body<true, 1>(A, lds, rb, j, pos4, wm, lane); }
  else { if (wid < 4) g1_body<false, 0>(A, lds, rb, j, pos4, wm, lane); else g1_body<false, 1>(A, lds, rb, j, pos4, wm, lane); }
}

struct G2Args {
  u16* af; const u16* w2; const u16* wo; const u16* phiq; const float* kvp; float* part;
  const u16* x; const u16* b2; u16* out; unsigned* flags; const u16* wphi;
};

DI unsigned peek_flag(unsigned* flag) { return __hip_atomic_load((GAS unsigned*)flag, RLX_AGENT); }
DI void await_flag(unsigned seen, unsigned* flag, unsigned want, unsigned* tmo) {
  unsigned spins = 0;
  while ((unsigned)__builtin_amdgcn_readfirstlane(seen) < want) {
    __builtin_amdgcn_s_sleep(1);
    if (++spins > (1u << 18)) { __hip_atomic_store((GAS unsigned*)tmo, 1u, RLX_AGENT); break; }
    seen = peek_flag(flag);
  }
  __builtin_amdgcn_fence(__ATOMIC_ACQUIRE, "workgroup");
}

template <bool FIN, int GRP>
DI void g2_body(const G2Args& A, LAS char* lds, int rb, int ct, int w4, int lane) {
  constexpr int NS = FIN ? N2 : N1;
  constexpr int KB0 = FIN ? 2 * N1 : 0;
  const int g = lane >> 4, q = (lane & 15) >> 2, p = lane & 3;
  const int wm = w4 & 1, wn = w4 >> 1;
  const int tile = rb * 8 + ct, wv = GRP * 4 + w4;
  const int col0 = ct * 128 + wn * 64 + 4 * g;
  u32x2 bb[4];
  if (FIN && GRP == 0) {
#pragma unroll
    for (int n = 0; n < 4; ++n) bb[n] = *(const u32x2*)(A.b2 + col0 + n * 16);
  }
  if (!FIN) {
    const int head = tile & 31, rq = tile >> 5;
    LAS char* kvl = lds + RING;
    u32x4 qf[4];
    {
      const u32x4* q4 = (const u32x4*)A.phiq + ((size_t)(rq * 32 + wv * 4) * 32 + head) * 64 + lane;
#pragma unroll
      for (int m = 0; m < 4; ++m) qf[m] = q4[m * 2048];
    }
    u16 wraw[2][8];
    {
      const u16* wsrc = A.wphi + head * 1024 + 4 * g * 32 + (lane & 15);
#pragma unroll
      for (int fc = 0; fc < 2; ++fc)
#pragma unroll
        for (int e = 0; e < 8; ++e) wraw[fc][e] = wsrc[((e & 3) + 16 * (e >> 2)) * 32 + 16 * fc];
    }
    f32x4 kp[8][2];
    const int kh = wv < 4 ? wv >> 1 : (lane >> 2) & 1, dc = wv < 4 ? wv & 1 : 2, sl = wv < 4 ? lane : (lane & 3) * 16;
    const bool kact = wv < 4 || (wv == 4 && lane < 8);
    if (kact) {
      const f32x4* src = (const f32x4*)A.kvp + ((size_t)head * 12 + (2 * kh) * 3 + dc) * 64 + sl;
#pragma unroll
      for (int r8 = 0; r8 < 8; ++r8) { kp[r8][0] = src[(size_t)r8 * 32 * 768]; kp[r8][1] = src[(size_t)r8 * 32 * 768 + 192]; }
    }
    bar();
    const f32x4 z4 = {0.f, 0.f, 0.f, 0.f};
    bf16x8 pqe[4], pqn[4];
    {
      bf16x8 wp[2];
#pragma unroll
      for (int fc = 0; fc < 2; ++fc)
#pragma unroll
        for (int e = 0; e < 8; ++e) wp[fc][e] = (short)wraw[fc][e];
      f32x4 p0[4], p1[4];
#pragma unroll
      for (int m = 0; m < 4; ++m) {
        const bf16x8 qb = __builtin_bit_cast(bf16x8, qf[m]);
        p0[m] = MFMA16(wp[0], qb, z4); p1[m] = MFMA16(wp[1], qb, z4);
      }
      dual_softmax4(p0, p1, pqe, pqn);
    }
    if (kact) {
      f32x4 s0 = kp[0][0], s1 = kp[0][1];
#pragma unroll
      for (int r8 = 1; r8 < 8; ++r8) { s0 += kp[r8][0]; s1 += kp[r8][1]; }
      *(LAS bf16x8*)(kvl + (kh * 3 + dc) * 1024 + (wv < 4 ? lane : lane & 3) * 16) = pack8(s0, s1);
    }
    wait_lgkm0();
    bar();
    bf16x8 ka[2][3];
#pragma unroll
    for (int k2 = 0; k2 < 2; ++k2)
#pragma unroll
      for (int d2 = 0; d2 < 3; ++d2) ka[k2][d2] = *(LAS bf16x8*)(kvl + (k2 * 3 + d2) * 1024 + (d2 < 2 ? lane : g) * 16);
    auto rsrc = __builtin_amdgcn_make_buffer_rsrc((void*)(A.af + (size_t)(112 + head) * 128 * 512), 0, 128 * 1024, 0x00020000);
#pragma unroll
    for (int m = 0; m < 4; ++m) {
      const bf16x8 b0 = pqe[m], b1 = pqn[m];
      f32x4 n0 = MFMA16(ka[0][0], b0, z4); n0 = MFMA16(ka[1][0], b1, n0);
      f32x4 n1 = MFMA16(ka[0][1], b0, z4); n1 = MFMA16(ka[1][1], b1, n1);
      f32x4 dn = MFMA16(ka[0][2], b0, z4); dn = MFMA16(ka[1][2], b1, dn);
      const float rd = __builtin_amdgcn_rcpf(rbf(rbf(dn[0]) + 1e-6f));
      const f32x4 o0 = n0 * rd, o1 = n1 * rd;
      const int mf = rq * 32 + wv * 4 + m;
      __builtin_amdgcn_raw_buffer_store_b128(__builtin_bit_cast(u32x4, pack8(o0, o1)), rsrc, (mf * 64 + lane) * 16, 0, 16);
    }
  }
  bar();

  f32x4 acc[4][4];
#pragma unroll
  for (int m = 0; m < 4; ++m)
#pragma unroll
    for (int n = 0; n < 4; ++n) {
      if (FIN && GRP == 0) acc[m][n] = (f32x4){bflo(bb[n][0]), bfhi(bb[n][0]), bflo(bb[n][1]), bfhi(bb[n][1])};
      else acc[m][n] = (f32x4){0.f, 0.f, 0.f, 0.f};
    }
  LAS char* pA = lds + GRP * 8192 + wm * 4096 + lane * 16;
  LAS char* pB = lds + BOFF + GRP * 8 * PSTR + PSTR * (4 * (g & 1) + q) + 256 * (g >> 1) + 8 * p + 128 * wn;
  constexpr int SO_FIRST = (112 - KB0) / 2;
  bf16x8 a0[4], b0[4], a1[4], b1[4];
#pragma unroll
  for (int n = 0; n < 4; ++n) b0[n] = cat(trread(pB + n * 32), trread(pB + n * 32 + 512));
#pragma unroll
  for (int m = 0; m < 4; ++m) a0[m] = *(LAS bf16x8*)(pA + m * 1024);
  wait_lgkm0();
  bar();

  auto step = [&](int st, bf16x8 (&a)[4], bf16x8 (&b)[4], bf16x8 (&an)[4], bf16x8 (&bn)[4], auto tag) {
    constexpr int T = decltype(tag)::value;
    const int so = ((st + 1) & 3) * SLOT;
    s16x4 lo[4];
    auto rd = [&](int i) {
      if (i < 4) an[i] = *(LAS bf16x8*)(pA + so + i * 1024);
      else {
        const int n = (i - 4) >> 1;
        LAS char* pp = pB + so + n * 32;
        if (((i - 4) & 1) == 0) lo[n] = trread(pp); else bn[n] = cat(lo[n], trread(pp + 512));
      }
    };
    if (GRP == 1 && T > 0) {
#pragma unroll
      for (int i = 0; i < 12; ++i) rd(i);
    }
    SB();
#pragma unroll
    for (int i = 0; i < 16; ++i) acc[i & 3][i >> 2] = MFMA16(b[i >> 2], a[i & 3], acc[i & 3][i >> 2]);
    SB();
    if (GRP == 0 && T > 0) {
#pragma unroll
      for (int i = 0; i < 12; ++i) rd(i);
    }
    if (T > 0) {
      wait_lgkm0();
      bar();
    }
  };
  unsigned* const ocnt = A.flags + (FLAG_OCNT + (rb >> 2)) * FLAG_STRIDE;
  unsigned* const pflag = A.flags + (FLAG_PART + tile) * FLAG_STRIDE;
  unsigned* const tmo = A.flags + FLAG_TMO * FLAG_STRIDE;
  unsigned seen = 0;
#pragma unroll 1
  for (int st = 0; st < NS - 4; st += 2) {
    if (wv == 0) {
      if (!FIN && st == 2 && lane == 0)
        __hip_atomic_fetch_add((GAS unsigned*)(A.flags + (FLAG_OCNT + (tile >> 5)) * FLAG_STRIDE), 1u, RLX_AGENT);
      if (FIN) {
        constexpr int S0 = (SO_FIRST - 8) & ~1;
        if (st == S0) seen = peek_flag(ocnt);
        if (st == S0 + 2) await_flag(seen, ocnt, 32u, tmo);
      }
    }
    step(st, a0, b0, a1, b1, IC<4>{});
    if (!FIN && st == 0) wait_vm<0>();
    step(st + 1, a1, b1, a0, b0, IC<4>{});
  }
  if (FIN && wv == 0) seen = peek_flag(pflag);
  step(NS - 4, a0, b0, a1, b1, IC<3>{}); step(NS - 3, a1, b1, a0, b0, IC<2>{});
  if (FIN && wv == 0) await_flag(seen, pflag, 1u, tmo);
  step(NS - 2, a0, b0, a1, b1, IC<1>{});
  f32x4 pv[2][4];
  if (FIN) {
    auto rsrc = __builtin_amdgcn_make_buffer_rsrc((void*)(A.part + (size_t)tile * 16384), 0, 65536, 0x00020000);
#pragma unroll
    for (int mm = 0; mm < 2; ++mm)
#pragma unroll
      for (int n = 0; n < 4; ++n)
        pv[mm][n] = __builtin_bit_cast(f32x4, __builtin_amdgcn_raw_buffer_load_b128(rsrc, ((wv * 8 + mm * 4 + n) * 64 + lane) * 16, 0, 16));
    SB();
  }
  step(NS - 1, a1, b1, a0, b0, IC<0>{});

  {
    LAS f32x4* wb = (LAS f32x4*)(lds + CBASE + wv * 8192) + lane;
#pragma unroll
    for (int mm = 0; mm < 2; ++mm)
#pragma unroll
      for (int n = 0; n < 4; ++n) wb[(mm * 4 + n) * 64] = acc[GRP == 0 ? 2 + mm : mm][n];
  }
  wait_lgkm0();
  bar();
  f32x4 r[2][4];
  {
    LAS f32x4* rbuf = (LAS f32x4*)(lds + CBASE + ((1 - GRP) * 4 + w4) * 8192) + lane;
#pragma unroll
    for (int mm = 0; mm < 2; ++mm)
#pragma unroll
      for (int n = 0; n < 4; ++n) r[mm][n] = acc[GRP == 0 ? mm : 2 + mm][n] + rbuf[(mm * 4 + n) * 64];
  }
  if (!FIN) {
    auto rsrc = __builtin_amdgcn_make_buffer_rsrc((void*)(A.part + (size_t)tile * 16384), 0, 65536, 0x00020000);
#pragma unroll
    for (int mm = 0; mm < 2; ++mm)
#pragma unroll
      for (int n = 0; n < 4; ++n)
        __builtin_amdgcn_raw_buffer_store_b128(__builtin_bit_cast(u32x4, r[mm][n]), rsrc, ((wv * 8 + mm * 4 + n) * 64 + lane) * 16, 0, 16);
    wait_vm<0>();
    bar();
    if (wv == 0 && lane == 0) __hip_atomic_store((GAS unsigned*)(A.flags + (FLAG_PART + tile) * FLAG_STRIDE), 1u, RLX_AGENT);
  } else {
    int lz = lane; asm volatile("" : "+v"(lz));
    const int rl = wm * 64 + GRP * 32 + (lz & 15), cl = wn * 64 + 4 * (lz >> 4);
    LAS char* xl = lds + XSLOT * SLOT + (rl >> 2) * XPSTR + (rl & 3) * 256 + cl * 2;
    u16* op = A.out + (size_t)(rb * 128 + rl) * D_ + ct * 128 + cl;
#pragma unroll
    for (int mm = 0; mm < 2; ++mm)
#pragma unroll
      for (int n = 0; n < 4; ++n) {
        const u32x2 xr = *(LAS u32x2*)(xl + mm * 4 * XPSTR + n * 32);
        const f32x4 xv = {bflo(xr[0]), bfhi(xr[0]), bflo(xr[1]), bfhi(xr[1])};
        const f32x4 o = (r[mm][n] + pv[mm][n]) + xv;
        u32x2 ov = {pk2(o[0], o[1]), pk2(o[2], o[3])};
        *(u32x2*)(op + (size_t)mm * 16 * D_ + n * 16) = ov;
      }
  }
}

template <bool FIN>
DI void g2_loader(const G2Args& A, LAS char* lds, int rb, int ct, int w4, int lane) {
  constexpr int NS = FIN ? N2 : N1;
  constexpr int KB0 = FIN ? 2 * N1 : 0;
  const int kbs = w4 >> 1, u0 = 4 * (w4 & 1);
  const unsigned lbase = (unsigned)(size_t)lds;
  const char* asrc = (const char*)A.af + ((size_t)kbs * 128 + rb * 8 + u0) * 1024;
  const unsigned aoff = lane * 16;
  const unsigned boff = (unsigned)(((kbs * 32 + u0 + 8 * (lane >> 4)) * D_ + ct * 128 + (lane & 15) * 8) * 2);
  const unsigned adst = lbase + kbs * 8192 + u0 * 1024, bdst = lbase + BOFF + kbs * 8 * PSTR + u0 * PSTR;
  auto issue = [&](int st) {
    const int kb = KB0 + 2 * st, slot = (st & 3) * SLOT;
    const char* wb = kb < 112 ? (const char*)A.w2 + (size_t)kb * 32 * D_ * 2 : (const char*)A.wo + (size_t)(kb - 112) * 32 * D_ * 2;
    if (FIN && kb >= 112) dma16s_x4k_sc1(asrc + (size_t)kb * 128 * 1024, aoff, adst + slot);
    else dma16s_x4k(asrc + (size_t)kb * 128 * 1024, aoff, adst + slot);
#pragma unroll
    for (int jj = 0; jj < 4; ++jj) dma16s(wb + jj * (D_ * 2), boff, bdst + slot + jj * PSTR);
  };
  if (!FIN) bar();
  issue(0); issue(1); issue(2); issue(3);
  if (!FIN) bar();
  wait_vm<24>();
  bar();
  wait_vm<16>();
  bar();
#pragma unroll 1
  for (int st = 0; st < NS - 4; ++st) { issue(st + 4); wait_vm<16>(); bar(); }
  if (FIN) {
    const char* xs = (const char*)A.x + ((size_t)(rb * 128 + w4 * 32) * D_ + ct * 128) * 2;
    const unsigned xo = (unsigned)((lane >> 4) * (D_ * 2) + (lane & 15) * 16);
#pragma unroll
    for (int jj = 0; jj < 8; ++jj) dma16s(xs + jj * 4 * (D_ * 2), xo, lbase + XSLOT * SLOT + (w4 * 8 + jj) * XPSTR);
    wait_vm<16>(); bar();
    wait_vm<8>(); bar();
    wait_vm<0>(); bar();
  } else {
    wait_vm<8>(); bar();
    wait_vm<0>(); bar();
  }
}

__global__ __launch_bounds__(768) void k2_gemm2(G2Args A) {
  extern __shared__ __attribute__((aligned(16))) char smem[];
  LAS char* lds = (LAS char*)smem;
  const int wid = __builtin_amdgcn_readfirstlane(threadIdx.x >> 6), lane = threadIdx.x & 63;
  const int bid = blockIdx.x, xcd = bid & 7, idx = bid >> 3;
  const int rb = (xcd & 3) * 4 + (idx & 3), ct = (xcd >> 2) * 4 + ((idx >> 2) & 3), fin = idx >> 4;
  const int w4 = wid & 3;
  if (wid >= 8) { if (fin) g2_loader<true>(A, lds, rb, ct, w4, lane); else g2_loader<false>(A, lds, rb, ct, w4, lane); return; }
  if (fin) { if (wid < 4) g2_body<true, 0>(A, lds, rb, ct, w4, lane); else g2_body<true, 1>(A, lds, rb, ct, w4, lane); }
  else { if (wid < 4) g2_body<false, 0>(A, lds, rb, ct, w4, lane); else g2_body<false, 1>(A, lds, rb, ct, w4, lane); }
}

extern "C" void kernel_launch(void* const* d_in, const int* in_sizes, int n_in, void* d_out, int out_size, void* d_ws, size_t ws_size,
                              hipStream_t stream) {
  const u16* x = (const u16*)d_in[0];
  const u16* wqkv = (const u16*)d_in[1];
  const u16* wphi = (const u16*)d_in[2];
  const u16* wo = (const u16*)d_in[3];
  const u16* w1 = (const u16*)d_in[5];
  const u16* b1 = (const u16*)d_in[6];
  const u16* w2 = (const u16*)d_in[7];
  const u16* b2 = (const u16*)d_in[8];
  char* ws = (char*)d_ws;
  unsigned* flags = (unsigned*)(ws + WS_FLAGS);
  u16* xnf = (u16*)(ws + WS_XNF);
  u16* af = (u16*)(ws + WS_AF);
  u16* phiq = (u16*)(ws + WS_PHIQ);
  float* kvp = (float*)(ws + WS_KVP);
  float* part = (float*)(ws + WS_PART);
  (void)hipFuncSetAttribute((const void*)k1_gemm1, hipFuncAttributeMaxDynamicSharedMemorySize, G1_LDS);
  (void)hipFuncSetAttribute((const void*)k2_gemm2, hipFuncAttributeMaxDynamicSharedMemorySize, G2_LDS);
  k0_ln<<<256, 512, 0, stream>>>(x, xnf, flags);
  G1Args a1 = {xnf, wqkv, w1, b1, wphi, af, phiq, kvp, w2, wo};
  k1_gemm1<<<256, 512, G1_LDS, stream>>>(a1);
  G2Args a2 = {af, w2, wo, phiq, kvp, part, x, b2, (u16*)d_out, flags, wphi};
  k2_gemm2<<<256, 768, G2_LDS, stream>>>(a2);
}
```

```cpp
#include <hip/hip_runtime.h>
#include <hip/hip_bf16.h>
#include <stdint.h>

typedef unsigned short u16;
typedef short bf16x8 __attribute__((ext_vector_type(8)));
typedef short s16x4 __attribute__((ext_vector_type(4)));
typedef float f32x4 __attribute__((ext_vector_type(4)));
typedef float f32x2 __attribute__((ext_vector_type(2)));
typedef unsigned u32x4 __attribute__((ext_vector_type(4)));
typedef unsigned u32x2 __attribute__((ext_vector_type(2)));
typedef __bf16 bf16v2 __attribute__((ext_vector_type(2)));
#define LAS __attribute__((address_space(3)))
#define GAS __attribute__((address_space(1)))
#define DI __device__ __forceinline__
#define RLX_AGENT __ATOMIC_RELAXED, __HIP_MEMORY_SCOPE_AGENT

constexpr int D_ = 1024, F_ = 3584;
constexpr int SLOT = 33280;
constexpr int BOFF = 16384;
constexpr int PSTR = 1056;
constexpr int RING = 4 * SLOT;
constexpr int RA = 3, RB = 6;
constexpr int BSL = 16 * PSTR;
constexpr int G1_B = RA * 16384;
constexpr int G1_RING = G1_B + RB * BSL;
constexpr int G1_WPHI = G1_RING;
constexpr int G1_BIAS = G1_RING + 4096;
constexpr int G1_DUMP = G1_RING + 5120;
constexpr int G1_LDS = G1_RING + 5120 + 256;
constexpr int G2_LDS = RING + 6144 + 256;
constexpr int NKB = 144;
constexpr int N1 = 30;
constexpr int N2 = 72 - N1;
constexpr int XSLOT = (N2 - 4) & 3;
constexpr int XPSTR = 1040;
constexpr int CBASE = XSLOT >= 2 ? 0 : 2 * SLOT;
static_assert(32 * XPSTR <= SLOT && N1 % 2 == 0, "");

constexpr size_t WS_FLAGS = 0;
constexpr size_t WS_XNF = 1u << 20;
constexpr size_t WS_AF = WS_XNF + (4u << 20);
constexpr size_t WS_PHIQ = WS_AF + (size_t)NKB * 128 * 1024;
constexpr size_t WS_KVP = WS_PHIQ + (8u << 20);
constexpr size_t WS_PART = WS_KVP + (3u << 20);
constexpr int FLAG_STRIDE = 64;
constexpr int FLAG_OCNT = 0;
constexpr int FLAG_PART = 8;
constexpr int FLAG_TMO = 8 + 128;
constexpr int FLAG_WORDS = (8 + 128 + 1) * FLAG_STRIDE;

template <int N> struct IC { static constexpr int value = N; };
constexpr int ST_AUX = 16;
#define MFMA16(a, b, c) __builtin_amdgcn_mfma_f32_16x16x32_bf16((a), (b), (c), 0, 0, 0)
template <int N> DI void wait_vm() { asm volatile("s_waitcnt vmcnt(%0)" ::"i"(N) : "memory"); }
DI void wait_lgkm0() { asm volatile("" ::: "memory"); __builtin_amdgcn_s_waitcnt(0xC07F); asm volatile("" ::: "memory"); }
DI void bar() { __builtin_amdgcn_sched_barrier(0); __builtin_amdgcn_s_barrier(); __builtin_amdgcn_sched_barrier(0); }
DI void dma16(const void* g, unsigned l) {
  asm volatile("s_mov_b32 m0, %1\n\ts_nop 0\n\tglobal_load_lds_dwordx4 %0, off" ::"v"(g), "s"(l) : "memory");
}
DI void dma16s(const void* sb, unsigned voff, unsigned l) {
  asm volatile("s_mov_b32 m0, %2\n\ts_nop 0\n\tglobal_load_lds_dwordx4 %0, %1" ::"v"(voff), "s"(sb), "s"(l) : "memory");
}
DI void dma16s_x4k(const void* sb, unsigned voff, unsigned l) {
  asm volatile("s_mov_b32 m0, %2\n\ts_nop 0\n\tglobal_load_lds_dwordx4 %0, %1\n\tglobal_load_lds_dwordx4 %0, %1 offset:1024\n\t"
               "global_load_lds_dwordx4 %0, %1 offset:2048\n\tglobal_load_lds_dwordx4 %0, %1 offset:3072" ::"v"(voff), "s"(sb), "s"(l) : "memory");
}
DI void dma16s_x4k_sc1(const void* sb, unsigned voff, unsigned l) {
  asm volatile("s_mov_b32 m0, %2\n\ts_nop 0\n\tglobal_load_lds_dwordx4 %0, %1 sc1\n\tglobal_load_lds_dwordx4 %0, %1 offset:1024 sc1\n\t"
               "global_load_lds_dwordx4 %0, %1 offset:2048 sc1\n\tglobal_load_lds_dwordx4 %0, %1 offset:3072 sc1" ::"v"(voff), "s"(sb), "s"(l) : "memory");
}
DI void touch(const void* g, unsigned l) { asm volatile("s_mov_b32 m0, %1\n\ts_nop 0\n\tglobal_load_lds_dword %0, off" ::"v"(g), "s"(l) : "memory"); }
DI s16x4 trread(LAS char* p) { return __builtin_amdgcn_ds_read_tr16_b64_v4i16((LAS s16x4*)p); }
DI bf16x8 cat(s16x4 lo, s16x4 hi) { return __builtin_shufflevector(lo, hi, 0, 1, 2, 3, 4, 5, 6, 7); }
DI unsigned pk2(float lo, float hi) { f32x2 v = {lo, hi}; return __builtin_bit_cast(unsigned, __builtin_convertvector(v, bf16v2)); }
DI bf16x8 pack8(f32x4 a, f32x4 b) { u32x4 r = {pk2(a[0], a[1]), pk2(a[2], a[3]), pk2(b[0], b[1]), pk2(b[2], b[3])}; return __builtin_bit_cast(bf16x8, r); }
DI float bflo(unsigned u) { return __uint_as_float(u << 16); }
DI float bfhi(unsigned u) { return __uint_as_float(u & 0xffff0000u); }
DI float rbf(float x) { return bflo(pk2(x, x)); }
template <int CTRL> DI float dpp(float x) { return __builtin_bit_cast(float, __builtin_amdgcn_mov_dpp(__builtin_bit_cast(int, x), CTRL, 0xf, 0xf, true)); }
constexpr int ROR8 = 0x128;
DI float xg_sum(float x) {
  auto s = __builtin_amdgcn_permlane16_swap(__float_as_uint(x), __float_as_uint(x), false, false);
  x = __uint_as_float(s[0]) + __uint_as_float(s[1]);
  auto t = __builtin_amdgcn_permlane32_swap(__float_as_uint(x), __float_as_uint(x), false, false);
  return __uint_as_float(t[0]) + __uint_as_float(t[1]);
}
constexpr float LOG2E = 1.4426950408889634f;
#define SB() __builtin_amdgcn_sched_barrier(0)
DI u32x4 gelu8(f32x4 a, f32x4 b) {
  const float c1 = -2.0f * 0.7978845608028654f * LOG2E, c3 = c1 * 0.044715f;
  f32x4 ta = a * a, tb = b * b;
  ta = ta * c3 + c1; tb = tb * c3 + c1;
  ta = ta * a; tb = tb * b;
  SB();
#pragma unroll
  for (int r = 0; r < 4; ++r) { ta[r] = __builtin_amdgcn_exp2f(ta[r]); tb[r] = __builtin_amdgcn_exp2f(tb[r]); }
  SB();
  ta = ta + 1.0f; tb = tb + 1.0f;
  SB();
#pragma unroll
  for (int r = 0; r < 4; ++r) { ta[r] = __builtin_amdgcn_rcpf(ta[r]); tb[r] = __builtin_amdgcn_rcpf(tb[r]); }
  SB();
  return __builtin_bit_cast(u32x4, pack8(a * ta, b * tb));
}
template <int OP> DI float rop(float a, float b) { return OP == 0 ? fmaxf(a, b) : OP == 1 ? fminf(a, b) : a + b; }
template <int OP> DI void xg_red4(float (&v)[4]) {
  auto s0 = __builtin_amdgcn_permlane16_swap(__float_as_uint(v[0]), __float_as_uint(v[1]), false, false);
  auto s1 = __builtin_amdgcn_permlane16_swap(__float_as_uint(v[2]), __float_as_uint(v[3]), false, false);
  const float x = rop<OP>(__uint_as_float(s0[0]), __uint_as_float(s0[1])), y = rop<OP>(__uint_as_float(s1[0]), __uint_as_float(s1[1]));
  auto t = __builtin_amdgcn_permlane32_swap(__float_as_uint(x), __float_as_uint(y), false, false);
  const unsigned e = __float_as_uint(rop<OP>(__uint_as_float(t[0]), __uint_as_float(t[1])));
  auto u = __builtin_amdgcn_permlane32_swap(e, e, false, false);
  auto b0 = __builtin_amdgcn_permlane16_swap(u[0], u[0], false, false);
  auto b1 = __builtin_amdgcn_permlane16_swap(u[1], u[1], false, false);
  v[0] = __uint_as_float(b0[0]); v[1] = __uint_as_float(b0[1]); v[2] = __uint_as_float(b1[0]); v[3] = __uint_as_float(b1[1]);
}
DI void dual_softmax4(f32x4 (&p0)[4], f32x4 (&p1)[4], bf16x8 (&pe)[4], bf16x8 (&pn)[4]) {
  float mx[4], mn[4], sp[4], sn[4];
#pragma unroll
  for (int m = 0; m < 4; ++m) {
    const u32x4 pr = __builtin_bit_cast(u32x4, pack8(p0[m], p1[m]));
    p0[m] = (f32x4){bflo(pr[0]), bfhi(pr[0]), bflo(pr[1]), bfhi(pr[1])} * LOG2E;
    p1[m] = (f32x4){bflo(pr[2]), bfhi(pr[2]), bflo(pr[3]), bfhi(pr[3])} * LOG2E;
    mx[m] = fmaxf(fmaxf(fmaxf(p0[m][0], p0[m][1]), fmaxf(p0[m][2], p0[m][3])), fmaxf(fmaxf(p1[m][0], p1[m][1]), fmaxf(p1[m][2], p1[m][3])));
    mn[m] = fminf(fminf(fminf(p0[m][0], p0[m][1]), fminf(p0[m][2], p0[m][3])), fminf(fminf(p1[m][0], p1[m][1]), fminf(p1[m][2], p1[m][3])));
  }
  xg_red4<0>(mx); xg_red4<1>(mn);
  f32x4 n0[4], n1[4];
#pragma unroll
  for (int m = 0; m < 4; ++m) {
    n0[m] = mn[m] - p0[m]; n1[m] = mn[m] - p1[m]; p0[m] = p0[m] - mx[m]; p1[m] = p1[m] - mx[m];
#pragma unroll
    for (int r = 0; r < 4; ++r) {
      p0[m][r] = __builtin_amdgcn_exp2f(p0[m][r]); p1[m][r] = __builtin_amdgcn_exp2f(p1[m][r]);
      n0[m][r] = __builtin_amdgcn_exp2f(n0[m][r]); n1[m][r] = __builtin_amdgcn_exp2f(n1[m][r]);
    }
    const f32x4 se = p0[m] + p1[m], sq = n0[m] + n1[m];
    sp[m] = (se[0] + se[1]) + (se[2] + se[3]); sn[m] = (sq[0] + sq[1]) + (sq[2] + sq[3]);
  }
  xg_red4<2>(sp); xg_red4<2>(sn);
#pragma unroll
  for (int m = 0; m < 4; ++m) {
    const float rp = __builtin_amdgcn_rcpf(sp[m]), rn = __builtin_amdgcn_rcpf(sn[m]);
    pe[m] = pack8(p0[m] * rp, p1[m] * rp); pn[m] = pack8(n0[m] * rn, n1[m] * rn);
  }
}

__global__ __launch_bounds__(512) void k0_ln(const u16* __restrict__ x, u16* __restrict__ xnf, unsigned* __restrict__ flags) {
  __shared__ float red[2][8][8];
  const int tid = threadIdx.x, w = tid >> 6, l = tid & 63, i = l & 7, c = l >> 3;
  const int row = blockIdx.x * 8 + i;
  const u32x4* src = (const u32x4*)(x + (size_t)row * D_) + 16 * w + c;
  const u32x4 v0 = src[0], v1 = src[8];
  const float x0 = bflo(x[(size_t)row * D_]);
  if (blockIdx.x == 0) { for (int j = tid; j < FLAG_WORDS; j += 512) flags[j] = 0u; }
  float f[16];
#pragma unroll
  for (int j = 0; j < 4; ++j) { f[2 * j] = bflo(v0[j]); f[2 * j + 1] = bfhi(v0[j]); f[8 + 2 * j] = bflo(v1[j]); f[9 + 2 * j] = bfhi(v1[j]); }
  float s = 0.f, q = 0.f;
#pragma unroll
  for (int j = 0; j < 16; ++j) { const float d = f[j] - x0; s += d; q = fmaf(d, d, q); }
  s += dpp<ROR8>(s); q += dpp<ROR8>(q); s = xg_sum(s); q = xg_sum(q);
  if (l < 8) { red[0][w][i] = s; red[1][w][i] = q; }
  __syncthreads();
  float md = 0.f, sq = 0.f;
#pragma unroll
  for (int j = 0; j < 8; ++j) { md += red[0][j][i]; sq += red[1][j][i]; }
  md *= (1.0f / D_);
  const float mean = x0 + md, var = fmaxf(sq * (1.0f / D_) - md * md, 0.f);
  const float mb = rbf(mean);
  const float sd = rbf(sqrtf(rbf(rbf(var) + 1e-5f)));
  const float rs = 1.0f / sd;
  u32x4 o0, o1;
#pragma unroll
  for (int j = 0; j < 4; ++j) {
    o0[j] = pk2(rbf(f[2 * j] - mb) * rs, rbf(f[2 * j + 1] - mb) * rs);
    o1[j] = pk2(rbf(f[8 + 2 * j] - mb) * rs, rbf(f[9 + 2 * j] - mb) * rs);
  }
  const int mfrag = row >> 4, ri = row & 15;
  const int ch0 = 16 * w + c, ch1 = ch0 + 8;
  auto rsx = __builtin_amdgcn_make_buffer_rsrc((void*)xnf, 0, 4 << 20, 0x00020000);
  __builtin_amdgcn_raw_buffer_store_b128(o0, rsx, (((ch0 >> 2) * 128 + mfrag) * 64 + (ch0 & 3) * 16 + ri) * 16, 0, ST_AUX);
  __builtin_amdgcn_raw_buffer_store_b128(o1, rsx, (((ch1 >> 2) * 128 + mfrag) * 64 + (ch1 & 3) * 16 + ri) * 16, 0, ST_AUX);
}

struct G1Args {
  const u16* xnf; const u16* wqkv; const u16* w1; const u16* b1; const u16* wphi;
  u16* af; u16* phiq; float* kvp; const u16* w2; const u16* wo;
};

template <bool ISX, int GRP>
DI void g1_body(const G1Args& A, LAS char* lds, int rb, int j, int pos4, int wm, int lane) {
  constexpr int NF = GRP == 0 ? 7 : 6;
  const int g = lane >> 4, q = (lane & 15) >> 2, p = lane & 3;
  const int hp = 2 * j + (pos4 >> 1);
  const int cp = pos4 == 0 ? 0 : pos4 == 3 ? 384 : pos4 == 1 ? 96 : 224;
  const int co = pos4 == 0 ? 64 : pos4 == 3 ? 368 : pos4 == 1 ? 80 : 352;
  const int cr = pos4 == 0 ? 0 : pos4 == 3 ? 320 : pos4 == 1 ? 64 : 192;
  constexpr int RBASE = ISX ? 256 : 128;
  const int colT = cp + 32 * (2 * GRP), colU = cp + 32 * (ISX ? GRP : 2 * GRP + 1);

  const unsigned lbase = (unsigned)(size_t)lds;
  const char* dsrc = nullptr; long dstep = 0; int dj = 0;
  const char* xsrc = (const char*)A.xnf + (size_t)(rb * 16 + 4 * wm) * 1024;
  const unsigned xoff = lane * 16;
  if (GRP == 0) {
    const int u0 = 8 * (wm & 1) + 4 * (wm >> 1), c = lane & 31, line = c >> 3, ch = c & 7, row = u0 + 16 * (lane >> 5);
    const bool fromq = ISX ? (line < 2) : (line < 1);
    const int ld = fromq ? 3 * D_ : F_;
    const u16* base = fromq ? (A.wqkv + (ISX ? (1 + line) * D_ : 0) + 64 * hp) : (A.w1 + 448 * j + cr + 64 * (line - (ISX ? 2 : 1)));
    dsrc = (const char*)(base + (size_t)row * ld + ch * 8); dstep = 64L * ld; dj = 2 * ld;
  }
  const unsigned ddst = lbase + (GRP == 0 ? G1_B + 4 * wm * PSTR : 4 * wm * 1024);
  constexpr int RD = GRP == 0 ? RB : RA;
  auto issue = [&](int slot) {
    if (GRP == 0) {
#pragma unroll
      for (int jj = 0; jj < 4; ++jj) dma16(dsrc + jj * dj, ddst + slot * BSL + jj * PSTR);
      dsrc += dstep;
    } else {
      dma16s_x4k(xsrc, xoff, ddst + slot * 16384);
      xsrc += 128 * 1024;
    }
  };
  if (GRP == 0) dma16s(A.wphi + (size_t)hp * 2048 + wm * 512, xoff, lbase + G1_WPHI + wm * 1024);
  else if (wm == 0) { const int nch = ISX ? 16 : 24; dma16s(A.b1 + 448 * j + cr, (lane < nch ? lane : 0) * 16, lbase + G1_BIAS); }
  issue(0);
#pragma unroll
  for (int i = 1; i < RD; ++i) issue(i);
  wait_vm<4 * (RD - 1)>();
  bar();

  f32x4 acc[4][NF];
  {
    LAS char* bp = lds + G1_BIAS + 8 * g;
#pragma unroll
    for (int f = 0; f < NF; ++f) {
      f32x4 b4 = {0.f, 0.f, 0.f, 0.f};
      if (f >= (ISX ? 4 : 2)) {
        const int col = f < 4 ? colT + 16 * (f - 2) : f < 6 ? colU + 16 * (f - 4) : co;
        const u32x2 bb = *(LAS u32x2*)(bp + (col - cr) * 2);
        b4 = (f32x4){bflo(bb[0]), bfhi(bb[0]), bflo(bb[1]), bfhi(bb[1])};
      }
#pragma unroll
      for (int m = 0; m < 4; ++m) acc[m][f] = b4;
    }
  }
  const int lb = G1_B + PSTR * (q + 4 * (g & 1)) + 512 * (g >> 1) + 8 * p;
  LAS char* pA = lds + wm * 4096 + lane * 16;
  LAS char* pS = lds + lb + 64 * GRP;
  LAS char* pT = lds + lb + (ISX ? 128 + 64 * GRP : RBASE + (colT - cr) * 2);
  LAS char* pU = lds + lb + RBASE + (colU - cr) * 2;
  LAS char* pO = lds + lb + RBASE + (co - cr) * 2;
  auto rdB = [&](int so, int f) -> bf16x8 {
    LAS char* pp = (f < 2 ? pS : f < 4 ? pT : f < 6 ? pU : pO) + so + (f < 6 ? (f & 1) * 32 : 0);
    return cat(trread(pp), trread(pp + 8 * PSTR));
  };
  bf16x8 a0[4], b0[NF], a1[4], b1[NF];
#pragma unroll
  for (int f = 0; f < NF; ++f) b0[f] = rdB(0, f);
#pragma unroll
  for (int m = 0; m < 4; ++m) a0[m] = *(LAS bf16x8*)(pA + m * 1024);
  wait_lgkm0();
  wait_vm<4 * (RD - 2)>();
  bar();
  int sa = 0, sb = 0;
  auto step = [&](bf16x8 (&a)[4], bf16x8 (&b)[NF], bf16x8 (&an)[4], bf16x8 (&bn)[NF], auto tag) {
    constexpr int T = decltype(tag)::value;
    constexpr bool ISSUE = T >= RD;
    const int sfree = GRP == 0 ? sb : sa;
    sa = sa + 1 == RA ? 0 : sa + 1; sb = sb + 1 == RB ? 0 : sb + 1;
    const int soa = sa * 16384, so = sb * BSL;
    auto mm = [&](int i) {
      const int f = i >> 2, m = i & 3;
      if (ISX && (f == 2 || f == 3)) acc[m][f] = MFMA16(a[m], b[f], acc[m][f]);
      else acc[m][f] = MFMA16(b[f], a[m], acc[m][f]);
    };
    s16x4 lo[NF];
    auto rd = [&](int i) {
      if (i < 4) an[i] = *(LAS bf16x8*)(pA + soa + i * 1024);
      else {
        const int f = (i - 4) >> 1;
        LAS char* pp = (f < 2 ? pS : f < 4 ? pT : f < 6 ? pU : pO) + so + (f < 6 ? (f & 1) * 32 : 0);
        if (((i - 4) & 1) == 0) lo[f] = trread(pp); else bn[f] = cat(lo[f], trread(pp + 8 * PSTR));
      }
    };
    if (GRP == 1) {
      if (ISSUE) issue(sfree);
      if (T > 0) {
#pragma unroll
        for (int i = 0; i < 4 + 2 * NF; ++i) rd(i);
      }
      SB();
#pragma unroll
      for (int i = 0; i < 4 * NF; ++i) mm(i);
      SB();
    } else {
#pragma unroll
      for (int i = 0; i < 4 * NF; ++i) mm(i);
      SB();
      if (T > 0) {
#pragma unroll
        for (int i = 0; i < 4 + 2 * NF; ++i) rd(i);
      }
      if (ISSUE) issue(sfree);
    }
    if (T > 0) {
      wait_lgkm0();
      if (T > 1) wait_vm<4 * ((T < RD ? T : RD) - 2)>();
      bar();
    }
  };
#pragma unroll 1
  for (int s = 0; s < 24; s += 2) { step(a0, b0, a1, b1, IC<8>{}); step(a1, b1, a0, b0, IC<8>{}); }
  step(a0, b0, a1, b1, IC<7>{}); step(a1, b1, a0, b0, IC<6>{}); step(a0, b0, a1, b1, IC<5>{}); step(a1, b1, a0, b0, IC<4>{});
  step(a0, b0, a1, b1, IC<3>{}); step(a1, b1, a0, b0, IC<2>{}); step(a0, b0, a1, b1, IC<1>{}); step(a1, b1, a0, b0, IC<0>{});

  const int mf0 = rb * 16 + wm * 4;
  {
    auto rs = __builtin_amdgcn_make_buffer_rsrc((void*)A.af, 0, NKB * 128 * 1024, 0x00020000);
    const int o0 = (mf0 * 64 + lane) * 16;
    if (!ISX) {
      const int kb = (448 * j + colT) >> 5;
#pragma unroll
      for (int m = 0; m < 4; ++m) __builtin_amdgcn_raw_buffer_store_b128(gelu8(acc[m][2], acc[m][3]), rs, o0 + kb * 131072 + m * 1024, 0, ST_AUX);
    }
    {
      const int kb = (448 * j + colU) >> 5;
#pragma unroll
      for (int m = 0; m < 4; ++m) __builtin_amdgcn_raw_buffer_store_b128(gelu8(acc[m][4], acc[m][5]), rs, o0 + kb * 131072 + m * 1024, 0, ST_AUX);
    }
    if (GRP == 0) {
      const int kb = (448 * j + co) >> 5, n = (co >> 4) & 1;
#pragma unroll
      for (int m = 0; m < 4; m += 2) {
        const u32x4 gv = gelu8(acc[m][NF - 1], acc[m + 1][NF - 1]);
        __builtin_amdgcn_raw_buffer_store_b64((u32x2){gv[0], gv[1]}, rs, o0 + kb * 131072 + n * 8 + m * 1024, 0, ST_AUX);
        __builtin_amdgcn_raw_buffer_store_b64((u32x2){gv[2], gv[3]}, rs, o0 + kb * 131072 + n * 8 + (m + 1) * 1024, 0, ST_AUX);
      }
    }
  }
  const int head = 2 * hp + GRP;
  if (!ISX) {
    auto rs = __builtin_amdgcn_make_buffer_rsrc((void*)A.phiq, 0, 4 << 20, 0x00020000);
#pragma unroll
    for (int m = 0; m < 4; ++m)
      __builtin_amdgcn_raw_buffer_store_b128(__builtin_bit_cast(u32x4, pack8(acc[m][0], acc[m][1])), rs, lane * 16 + ((mf0 + m) * 32 + head) * 1024, 0, ST_AUX);
    const int yw = ((rb * 16 + hp) * 8 + GRP * 4 + wm) * 72;
#pragma unroll
    for (int t = 0; t < 2; ++t) {
      const int ln = yw + t * 36 + (lane < 36 ? lane : 0);
      touch(ln < 57344 ? (const char*)A.w2 + (size_t)ln * 128 : (const char*)A.wo + (size_t)(ln - 57344) * 128, lbase + G1_DUMP);
    }
  } else {
    bf16x8 wp[2];
    {
      LAS char* pw = lds + G1_WPHI + GRP * 2048 + (4 * g + q) * 64 + 8 * p;
      wp[0] = cat(trread(pw), trread(pw + 1024));
      wp[1] = cat(trread(pw + 32), trread(pw + 1024 + 32));
    }
    const f32x4 z4 = {0.f, 0.f, 0.f, 0.f};
    f32x4 p0[4], p1[4];
#pragma unroll
    for (int m = 0; m < 4; ++m) {
      const bf16x8 qb = pack8(acc[m][0], acc[m][1]);
      p0[m] = MFMA16(wp[0], qb, z4); p1[m] = MFMA16(wp[1], qb, z4);
    }
    constexpr int TS = 144;
    LAS char* tb = lds + (GRP * 4 + wm) * 12288;
    {
      LAS char* wrp = tb + (lane & 15) * TS + 8 * g;
      bf16x8 fe[4], fn[4];
      dual_softmax4(p0, p1, fe, fn);
#pragma unroll
      for (int m = 0; m < 4; ++m) {
        const u32x4 pe = __builtin_bit_cast(u32x4, fe[m]), pn = __builtin_bit_cast(u32x4, fn[m]);
        *(LAS u32x2*)(wrp + m * 16 * TS) = (u32x2){pe[0], pe[1]};
        *(LAS u32x2*)(wrp + m * 16 * TS + 32) = (u32x2){pe[2], pe[3]};
        *(LAS u32x2*)(wrp + m * 16 * TS + 64) = (u32x2){pn[0], pn[1]};
        *(LAS u32x2*)(wrp + m * 16 * TS + 96) = (u32x2){pn[2], pn[3]};
      }
    }
    wait_lgkm0();
    f32x4 kv[4][3];
    const bf16x8 ones = {0x3F80, 0x3F80, 0x3F80, 0x3F80, 0x3F80, 0x3F80, 0x3F80, 0x3F80};
    LAS char* rdp = tb + (4 * g + q) * TS + 8 * p;
    bf16x8 pa[2][4];
#pragma unroll
    for (int u = 0; u < 2; ++u)
#pragma unroll
      for (int fq = 0; fq < 4; ++fq) pa[u][fq] = cat(trread(rdp + (32 * u) * TS + 32 * fq), trread(rdp + (32 * u + 16) * TS + 32 * fq));
#pragma unroll
    for (int u = 0; u < 2; ++u) {
      const bf16x8 v0 = pack8(acc[2 * u][2], acc[2 * u + 1][2]), v1 = pack8(acc[2 * u][3], acc[2 * u + 1][3]);
#pragma unroll
      for (int fq = 0; fq < 4; ++fq) {
        kv[fq][0] = MFMA16(pa[u][fq], v0, u == 0 ? z4 : kv[fq][0]);
        kv[fq][1] = MFMA16(pa[u][fq], v1, u == 0 ? z4 : kv[fq][1]);
        kv[fq][2] = MFMA16(pa[u][fq], ones, u == 0 ? z4 : kv[fq][2]);
      }
    }
    wait_lgkm0();
    LAS f32x4* rbuf = (LAS f32x4*)tb + lane;
#pragma unroll
    for (int fq = 0; fq < 4; ++fq)
#pragma unroll
      for (int dc = 0; dc < 3; ++dc) rbuf[(fq * 3 + dc) * 64] = kv[fq][dc];
    wait_lgkm0();
    bar();
    LAS f32x4* sbuf = (LAS f32x4*)(lds + GRP * 4 * 12288) + lane;
    auto rs = __builtin_amdgcn_make_buffer_rsrc((void*)A.kvp, 0, 3 << 20, 0x00020000);
#pragma unroll
    for (int t = 0; t < 3; ++t) {
      const int qd = 3 * wm + t;
      const f32x4 sv = (sbuf[qd * 64] + sbuf[768 + qd * 64]) + (sbuf[1536 + qd * 64] + sbuf[2304 + qd * 64]);
      __builtin_amdgcn_raw_buffer_store_b128(__builtin_bit_cast(u32x4, sv), rs, lane * 16 + ((rb * 32 + head) * 12 + qd) * 1024, 0, ST_AUX);
    }
  }
}

__global__ __launch_bounds__(512, 2) void k1_gemm1(G1Args A) {
  extern __shared__ __attribute__((aligned(16))) char smem[];
  LAS char* lds = (LAS char*)smem;
  const int wid = __builtin_amdgcn_readfirstlane(threadIdx.x >> 6), lane = threadIdx.x & 63;
  const int bid = blockIdx.x, xcd = bid & 7, idx = bid >> 3;
  const int rb = (xcd & 1) * 4 + (idx & 3), ct = (xcd >> 1) * 8 + (idx >> 2);
  const int j = ct >> 2, pos4 = ct & 3, wm = wid & 3;
  const bool isx = (pos4 == 0) || (pos4 == 3);
  if (isx) { if (wid < 4) g1_body<true, 0>(A, lds, rb, j, pos4, wm, lane); else g1_body<true, 1>(A, lds, rb, j, pos4, wm, lane); }
  else { if (wid < 4) g1_body<false, 0>(A, lds, rb, j, pos4, wm, lane); else g1_body<false, 1>(A, lds, rb, j, pos4, wm, lane); }
}

struct G2Args {
  u16* af; const u16* w2; const u16* wo; const u16* phiq; const float* kvp; float* part;
  const u16* x; const u16* b2; u16* out; unsigned* flags; const u16* wphi;
};

DI unsigned peek_flag(unsigned* flag) { return __hip_atomic_load((GAS unsigned*)flag, RLX_AGENT); }
DI void await_flag(unsigned seen, unsigned* flag, unsigned want, unsigned* tmo) {
  unsigned spins = 0;
  while ((unsigned)__builtin_amdgcn_readfirstlane(seen) < want) {
    __builtin_amdgcn_s_sleep(1);
    if (++spins > (1u << 18)) { __hip_atomic_store((GAS unsigned*)tmo, 1u, RLX_AGENT); break; }
    seen = peek_flag(flag);
  }
  __builtin_amdgcn_fence(__ATOMIC_ACQUIRE, "workgroup");
}

template <bool FIN, int GRP>
DI void g2_body(const G2Args& A, LAS char* lds, int rb, int ct, int w4, int lane) {
  constexpr int NS = FIN ? N2 : N1;
  constexpr int KB0 = FIN ? 2 * N1 : 0;
  const int g = lane >> 4, q = (lane & 15) >> 2, p = lane & 3;
  const int wm = w4 & 1, wn = w4 >> 1;
  const int tile = rb * 8 + ct, wv = GRP * 4 + w4;
  const int col0 = ct * 128 + wn * 64 + 4 * g;
  u32x2 bb[4];
  if (FIN && GRP == 0) {
#pragma unroll
    for (int n = 0; n < 4; ++n) bb[n] = *(const u32x2*)(A.b2 + col0 + n * 16);
  }
  if (!FIN) {
    const int head = tile & 31, rq = tile >> 5;
    LAS char* kvl = lds + RING;
    u32x4 qf[4];
    {
      const u32x4* q4 = (const u32x4*)A.phiq + ((size_t)(rq * 32 + wv * 4) * 32 + head) * 64 + lane;
#pragma unroll
      for (int m = 0; m < 4; ++m) qf[m] = q4[m * 2048];
    }
    u16 wraw[2][8];
    {
      const u16* wsrc = A.wphi + head * 1024 + 4 * g * 32 + (lane & 15);
#pragma unroll
      for (int fc = 0; fc < 2; ++fc)
#pragma unroll
        for (int e = 0; e < 8; ++e) wraw[fc][e] = wsrc[((e & 3) + 16 * (e >> 2)) * 32 + 16 * fc];
    }
    f32x4 kp[8][2];
    const int kh = wv < 4 ? wv >> 1 : (lane >> 2) & 1, dc = wv < 4 ? wv & 1 : 2, sl = wv < 4 ? lane : (lane & 3) * 16;
    const bool kact = wv < 4 || (wv == 4 && lane < 8);
    if (kact) {
      const f32x4* src = (const f32x4*)A.kvp + ((size_t)head * 12 + (2 * kh) * 3 + dc) * 64 + sl;
#pragma unroll
      for (int r8 = 0; r8 < 8; ++r8) { kp[r8][0] = src[(size_t)r8 * 32 * 768]; kp[r8][1] = src[(size_t)r8 * 32 * 768 + 192]; }
    }
    bar();
    const f32x4 z4 = {0.f, 0.f, 0.f, 0.f};
    bf16x8 pqe[4], pqn[4];
    {
      bf16x8 wp[2];
#pragma unroll
      for (int fc = 0; fc < 2; ++fc)
#pragma unroll
        for (int e = 0; e < 8; ++e) wp[fc][e] = (short)wraw[fc][e];
      f32x4 p0[4], p1[4];
#pragma unroll
      for (int m = 0; m < 4; ++m) {
        const bf16x8 qb = __builtin_bit_cast(bf16x8, qf[m]);
        p0[m] = MFMA16(wp[0], qb, z4); p1[m] = MFMA16(wp[1], qb, z4);
      }
      dual_softmax4(p0, p1, pqe, pqn);
    }
    if (kact) {
      f32x4 s0 = kp[0][0], s1 = kp[0][1];
#pragma unroll
      for (int r8 = 1; r8 < 8; ++r8) { s0 += kp[r8][0]; s1 += kp[r8][1]; }
      *(LAS bf16x8*)(kvl + (kh * 3 + dc) * 1024 + (wv < 4 ? lane : lane & 3) * 16) = pack8(s0, s1);
    }
    wait_lgkm0();
    bar();
    bf16x8 ka[2][3];
#pragma unroll
    for (int k2 = 0; k2 < 2; ++k2)
#pragma unroll
      for (int d2 = 0; d2 < 3; ++d2) ka[k2][d2] = *(LAS bf16x8*)(kvl + (k2 * 3 + d2) * 1024 + (d2 < 2 ? lane : g) * 16);
    auto rsrc = __builtin_amdgcn_make_buffer_rsrc((void*)(A.af + (size_t)(112 + head) * 128 * 512), 0, 128 * 1024, 0x00020000);
#pragma unroll
    for (int m = 0; m < 4; ++m) {
      const bf16x8 b0 = pqe[m], b1 = pqn[m];
      f32x4 n0 = MFMA16(ka[0][0], b0, z4); n0 = MFMA16(ka[1][0], b1, n0);
      f32x4 n1 = MFMA16(ka[0][1], b0, z4); n1 = MFMA16(ka[1][1], b1, n1);
      f32x4 dn = MFMA16(ka[0][2], b0, z4); dn = MFMA16(ka[1][2], b1, dn);
      const float rd = __builtin_amdgcn_rcpf(rbf(rbf(dn[0]) + 1e-6f));
      const f32x4 o0 = n0 * rd, o1 = n1 * rd;
      const int mf = rq * 32 + wv * 4 + m;
      __builtin_amdgcn_raw_buffer_store_b128(__builtin_bit_cast(u32x4, pack8(o0, o1)), rsrc, (mf * 64 + lane) * 16, 0, 16);
    }
  }
  bar();

  f32x4 acc[4][4];
#pragma unroll
  for (int m = 0; m < 4; ++m)
#pragma unroll
    for (int n = 0; n < 4; ++n) {
      if (FIN && GRP == 0) acc[m][n] = (f32x4){bflo(bb[n][0]), bfhi(bb[n][0]), bflo(bb[n][1]), bfhi(bb[n][1])};
      else acc[m][n] = (f32x4){0.f, 0.f, 0.f, 0.f};
    }
  LAS char* pA = lds + GRP * 8192 + wm * 4096 + lane * 16;
  LAS char* pB = lds + BOFF + GRP * 8 * PSTR + PSTR * (4 * (g & 1) + q) + 256 * (g >> 1) + 8 * p + 128 * wn;
  constexpr int SO_FIRST = (112 - KB0) / 2;
  bf16x8 a0[4], b0[4], a1[4], b1[4];
#pragma unroll
  for (int n = 0; n < 4; ++n) b0[n] = cat(trread(pB + n * 32), trread(pB + n * 32 + 512));
#pragma unroll
  for (int m = 0; m < 4; ++m) a0[m] = *(LAS bf16x8*)(pA + m * 1024);
  wait_lgkm0();
  bar();

  auto step = [&](int st, bf16x8 (&a)[4], bf16x8 (&b)[4], bf16x8 (&an)[4], bf16x8 (&bn)[4], auto tag) {
    constexpr int T = decltype(tag)::value;
    const int so = ((st + 1) & 3) * SLOT;
    s16x4 lo[4];
    auto rd = [&](int i) {
      if (i < 4) an[i] = *(LAS bf16x8*)(pA + so + i * 1024);
      else {
        const int n = (i - 4) >> 1;
        LAS char* pp = pB + so + n * 32;
        if (((i - 4) & 1) == 0) lo[n] = trread(pp); else bn[n] = cat(lo[n], trread(pp + 512));
      }
    };
    if (GRP == 1 && T > 0) {
#pragma unroll
      for (int i = 0; i < 12; ++i) rd(i);
    }
    SB();
#pragma unroll
    for (int i = 0; i < 16; ++i) acc[i & 3][i >> 2] = MFMA16(b[i >> 2], a[i & 3], acc[i & 3][i >> 2]);
    SB();
    if (GRP == 0 && T > 0) {
#pragma unroll
      for (int i = 0; i < 12; ++i) rd(i);
    }
    if (T > 0) {
      wait_lgkm0();
      bar();
    }
  };
  unsigned* const ocnt = A.flags + (FLAG_OCNT + (rb >> 2)) * FLAG_STRIDE;
  unsigned* const pflag = A.flags + (FLAG_PART + tile) * FLAG_STRIDE;
  unsigned* const tmo = A.flags + FLAG_TMO * FLAG_STRIDE;
  unsigned seen = 0;
#pragma unroll 1
  for (int st = 0; st < NS - 4; st += 2) {
    if (wv == 0) {
      if (!FIN && st == 2 && lane == 0)
        __hip_atomic_fetch_add((GAS unsigned*)(A.flags + (FLAG_OCNT + (tile >> 5)) * FLAG_STRIDE), 1u, RLX_AGENT);
      if (FIN) {
        constexpr int S0 = (SO_FIRST - 8) & ~1;
        if (st == S0) seen = peek_flag(ocnt);
        if (st == S0 + 2) await_flag(seen, ocnt, 32u, tmo);
      }
    }
    step(st, a0, b0, a1, b1, IC<4>{});
    if (!FIN && st == 0) wait_vm<0>();
    step(st + 1, a1, b1, a0, b0, IC<4>{});
  }
  if (FIN && wv == 0) seen = peek_flag(pflag);
  step(NS - 4, a0, b0, a1, b1, IC<3>{}); step(NS - 3, a1, b1, a0, b0, IC<2>{});
  if (FIN && wv == 0) await_flag(seen, pflag, 1u, tmo);
  step(NS - 2, a0, b0, a1, b1, IC<1>{});
  f32x4 pv[2][4];
  if (FIN) {
    auto rsrc = __builtin_amdgcn_make_buffer_rsrc((void*)(A.part + (size_t)tile * 16384), 0, 65536, 0x00020000);
#pragma unroll
    for (int mm = 0; mm < 2; ++mm)
#pragma unroll
      for (int n = 0; n < 4; ++n)
        pv[mm][n] = __builtin_bit_cast(f32x4, __builtin_amdgcn_raw_buffer_load_b128(rsrc, ((wv * 8 + mm * 4 + n) * 64 + lane) * 16, 0, 16));
    SB();
  }
  step(NS - 1, a1, b1, a0, b0, IC<0>{});

  {
    LAS f32x4* wb = (LAS f32x4*)(lds + CBASE + wv * 8192) + lane;
#pragma unroll
    for (int mm = 0; mm < 2; ++mm)
#pragma unroll
      for (int n = 0; n < 4; ++n) wb[(mm * 4 + n) * 64] = acc[GRP == 0 ? 2 + mm : mm][n];
  }
  wait_lgkm0();
  bar();
  f32x4 r[2][4];
  {
    LAS f32x4* rbuf = (LAS f32x4*)(lds + CBASE + ((1 - GRP) * 4 + w4) * 8192) + lane;
#pragma unroll
    for (int mm = 0; mm < 2; ++mm)
#pragma unroll
      for (int n = 0; n < 4; ++n) r[mm][n] = acc[GRP == 0 ? mm : 2 + mm][n] + rbuf[(mm * 4 + n) * 64];
  }
  if (!FIN) {
    auto rsrc = __builtin_amdgcn_make_buffer_rsrc((void*)(A.part + (size_t)tile * 16384), 0, 65536, 0x00020000);
#pragma unroll
    for (int mm = 0; mm < 2; ++mm)
#pragma unroll
      for (int n = 0; n < 4; ++n)
        __builtin_amdgcn_raw_buffer_store_b128(__builtin_bit_cast(u32x4, r[mm][n]), rsrc, ((wv * 8 + mm * 4 + n) * 64 + lane) * 16, 0, 16);
    wait_vm<0>();
    bar();
    if (wv == 0 && lane == 0) __hip_atomic_store((GAS unsigned*)(A.flags + (FLAG_PART + tile) * FLAG_STRIDE), 1u, RLX_AGENT);
  } else {
    int lz = lane; asm volatile("" : "+v"(lz));
    const int rl = wm * 64 + GRP * 32 + (lz & 15), cl = wn * 64 + 4 * (lz >> 4);
    LAS char* xl = lds + XSLOT * SLOT + (rl >> 2) * XPSTR + (rl & 3) * 256 + cl * 2;
    u16* op = A.out + (size_t)(rb * 128 + rl) * D_ + ct * 128 + cl;
#pragma unroll
    for (int mm = 0; mm < 2; ++mm)
#pragma unroll
      for (int n = 0; n < 4; ++n) {
        const u32x2 xr = *(LAS u32x2*)(xl + mm * 4 * XPSTR + n * 32);
        const f32x4 xv = {bflo(xr[0]), bfhi(xr[0]), bflo(xr[1]), bfhi(xr[1])};
        const f32x4 o = (r[mm][n] + pv[mm][n]) + xv;
        u32x2 ov = {pk2(o[0], o[1]), pk2(o[2], o[3])};
        *(u32x2*)(op + (size_t)mm * 16 * D_ + n * 16) = ov;
      }
  }
}

template <bool FIN>
DI void g2_loader(const G2Args& A, LAS char* lds, int rb, int ct, int w4, int lane) {
  constexpr int NS = FIN ? N2 : N1;
  constexpr int KB0 = FIN ? 2 * N1 : 0;
  const int kbs = w4 >> 1, u0 = 4 * (w4 & 1);
  const unsigned lbase = (unsigned)(size_t)lds;
  const char* asrc = (const char*)A.af + ((size_t)kbs * 128 + rb * 8 + u0) * 1024;
  const unsigned aoff = lane * 16;
  const unsigned boff = (unsigned)(((kbs * 32 + u0 + 8 * (lane >> 4)) * D_ + ct * 128 + (lane & 15) * 8) * 2);
  const unsigned adst = lbase + kbs * 8192 + u0 * 1024, bdst = lbase + BOFF + kbs * 8 * PSTR + u0 * PSTR;
  auto issue = [&](int st) {
    const int kb = KB0 + 2 * st, slot = (st & 3) * SLOT;
    const char* wb = kb < 112 ? (const char*)A.w2 + (size_t)kb * 32 * D_ * 2 : (const char*)A.wo + (size_t)(kb - 112) * 32 * D_ * 2;
    if (FIN && kb >= 112) dma16s_x4k_sc1(asrc + (size_t)kb * 128 * 1024, aoff, adst + slot);
    else dma16s_x4k(asrc + (size_t)kb * 128 * 1024, aoff, adst + slot);
#pragma unroll
    for (int jj = 0; jj < 4; ++jj) dma16s(wb + jj * (D_ * 2), boff, bdst + slot + jj * PSTR);
  };
  if (!FIN) bar();
  issue(0); issue(1); issue(2); issue(3);
  if (!FIN) bar();
  wait_vm<24>();
  bar();
  wait_vm<16>();
  bar();
#pragma unroll 1
  for (int st = 0; st < NS - 4; ++st) { issue(st + 4); wait_vm<16>(); bar(); }
  if (FIN) {
    const char* xs = (const char*)A.x + ((size_t)(rb * 128 + w4 * 32) * D_ + ct * 128) * 2;
    const unsigned xo = (unsigned)((lane >> 4) * (D_ * 2) + (lane & 15) * 16);
#pragma unroll
    for (int jj = 0; jj < 8; ++jj) dma16s(xs + jj * 4 * (D_ * 2), xo, lbase + XSLOT * SLOT + (w4 * 8 + jj) * XPSTR);
    wait_vm<16>(); bar();
    wait_vm<8>(); bar();
    wait_vm<0>(); bar();
  } else {
    wait_vm<8>(); bar();
    wait_vm<0>(); bar();
  }
}

__global__ __launch_bounds__(768) void k2_gemm2(G2Args A) {
  extern __shared__ __attribute__((aligned(16))) char smem[];
  LAS char* lds = (LAS char*)smem;
  const int wid = __builtin_amdgcn_readfirstlane(threadIdx.x >> 6), lane = threadIdx.x & 63;
  const int bid = blockIdx.x, xcd = bid & 7, idx = bid >> 3;
  const int rb = (xcd & 3) * 4 + (idx & 3), ct = (xcd >> 2) * 4 + ((idx >> 2) & 3), fin = idx >> 4;
  const int w4 = wid & 3;
  if (wid >= 8) { if (fin) g2_loader<true>(A, lds, rb, ct, w4, lane); else g2_loader<false>(A, lds, rb, ct, w4, lane); return; }
  if (fin) { if (wid < 4) g2_body<true, 0>(A, lds, rb, ct, w4, lane); else g2_body<true, 1>(A, lds, rb, ct, w4, lane); }
  else { if (wid < 4) g2_body<false, 0>(A, lds, rb, ct, w4, lane); else g2_body<false, 1>(A, lds, rb, ct, w4, lane); }
}

extern "C" void kernel_launch(void* const* d_in, const int* in_sizes, int n_in, void* d_out, int out_size, void* d_ws, size_t ws_size,
                              hipStream_t stream) {
  const u16* x = (const u16*)d_in[0];
  const u16* wqkv = (const u16*)d_in[1];
  const u16* wphi = (const u16*)d_in[2];
  const u16* wo = (const u16*)d_in[3];
  const u16* w1 = (const u16*)d_in[5];
  const u16* b1 = (const u16*)d_in[6];
  const u16* w2 = (const u16*)d_in[7];
  const u16* b2 = (const u16*)d_in[8];
  char* ws = (char*)d_ws;
  unsigned* flags = (unsigned*)(ws + WS_FLAGS);
  u16* xnf = (u16*)(ws + WS_XNF);
  u16* af = (u16*)(ws + WS_AF);
  u16* phiq = (u16*)(ws + WS_PHIQ);
  float* kvp = (float*)(ws + WS_KVP);
  float* part = (float*)(ws + WS_PART);
  (void)hipFuncSetAttribute((const void*)k1_gemm1, hipFuncAttributeMaxDynamicSharedMemorySize, G1_LDS);
  (void)hipFuncSetAttribute((const void*)k2_gemm2, hipFuncAttributeMaxDynamicSharedMemorySize, G2_LDS);
  k0_ln<<<256, 512, 0, stream>>>(x, xnf, flags);
  G1Args a1 = {xnf, wqkv, w1, b1, wphi, af, phiq, kvp, w2, wo};
  k1_gemm1<<<256, 512, G1_LDS, stream>>>(a1);
  G2Args a2 = {af, w2, wo, phiq, kvp, part, x, b2, (u16*)d_out, flags, wphi};
  k2_gemm2<<<256, 768, G2_LDS, stream>>>(a2);
}
```
